```python
import torch

def _softmax(x, axis=-1):
    return torch.softmax(x, dim=axis)
def _gelu(x):
    return torch.nn.functional.gelu(x, approximate="tanh")
def _silu(x):
    return torch.nn.functional.silu(x)
def _sigmoid(x):
    return torch.sigmoid(x)
def _layernorm(x, axis=-1, eps=1e-5):
    m = x.mean(dim=axis, keepdim=True)
    v = x.var(dim=axis, keepdim=True, unbiased=False)
    return (x - m) / torch.sqrt(v + eps)
def _scatter_add(tgt, idx, src, axis=0):
    return tgt.clone().index_add_(axis, idx, src)
def _segment_sum(src, seg, n):
    out = torch.zeros((n,) + src.shape[1:], dtype=src.dtype)
    return out.index_add_(0, seg, src)
def _pad(x, pw):
    flat = tuple(v for pair in reversed(pw) for v in pair)
    return torch.nn.functional.pad(x, flat)

kernel_name = 'recurrent_scan__compressed_kv_cla__dense_activation_relu__prompt'

def setup_inputs():

    _w = (lambda a: (lambda t: t.to(torch.bfloat16) if t.is_floating_point() else t)(torch.as_tensor(a)))
    def _A_setup():
        B, H, Sq, Sk, D, M = 1, 32, 2048, 2048, 128, 512
        Dc = 4 * D
        return {
            "x": _w((lambda *s: torch.randn(*s, dtype=torch.bfloat16) * 0.05)(B, Sq, M)),
            "c_kv": _w((lambda *s: torch.randn(*s, dtype=torch.bfloat16) * 0.2)(B, Sk, Dc)),
            "w_q": _w((lambda *s: torch.randn(*s, dtype=torch.bfloat16) * 3.2)(M, H, D) * (M ** -0.5)),
            "w_uk": _w((lambda *s: torch.randn(*s, dtype=torch.bfloat16) * 3.2)(Dc, H, D) * (Dc ** -0.5)),
            "w_uv": _w((lambda *s: torch.randn(*s, dtype=torch.bfloat16) * 0.2)(Dc, H, D) * (Dc ** -0.5)),
            "w_o": _w((lambda *s: torch.randn(*s, dtype=torch.bfloat16) * 0.2)(H, D, M) * ((H * D) ** -0.5)),
        }
    _A_in = _A_setup()

    def _B_setup():
        B, S, D, Dff = 1, 2048, 512, 1376
        return {
            "x": _w((lambda *s: torch.randn(*s, dtype=torch.bfloat16) * 0.05)(B, S, D)),
            "w1": _w((lambda *s: torch.randn(*s, dtype=torch.bfloat16) * 0.1)(D, Dff)),
            "w2": _w((lambda *s: torch.randn(*s, dtype=torch.bfloat16) * 0.1)(Dff, D)),
        }
    _B_in = _B_setup()

    return {
        "A_x": _A_in["x"],
        "A_c_kv": _A_in["c_kv"],
        "A_w_q": _A_in["w_q"],
        "A_w_uk": _A_in["w_uk"],
        "A_w_uv": _A_in["w_uv"],
        "A_w_o": _A_in["w_o"],
        "B_x": _B_in["x"],
        "B_w1": _B_in["w1"],
        "B_w2": _B_in["w2"],
    }

def reference(A_x, A_c_kv, A_w_q, A_w_uk, A_w_uv, A_w_o, B_x, B_w1, B_w2):
    def _A_ref(x, c_kv, w_q, w_uk, w_uv, w_o):
        B, Sq, M = x.shape[0], x.shape[1], x.shape[2]
        Sk = c_kv.shape[1]
        H, D = w_q.shape[1], w_q.shape[2]
        q = torch.einsum("bqm,mhd->bhqd", x, w_q)
        k = torch.einsum("bkc,chd->bhkd", c_kv, w_uk)
        v = torch.einsum("bkc,chd->bhkd", c_kv, w_uv)
        scores = torch.einsum("bhqd,bhkd->bhqk", q, k) * (D ** -0.5)
        off = Sk - Sq
        qi = torch.arange(Sq)[:, None]
        kj = torch.arange(Sk)[None, :]
        cmask = kj <= (qi + off)
        scores = torch.where(cmask[None, None], scores, -1e9)
        p = _softmax(scores, axis=-1)
        ctx = torch.einsum("bhqk,bhkd->bhqd", p, v)
        return torch.einsum("bhqd,hdm->bqm", ctx, w_o)

    def _B_ref(x, w1, w2):
        h = torch.einsum("bsd,df->bsf", x, w1)
        zero = torch.zeros_like(h)
        h = torch.maximum(h, zero)
        return torch.einsum("bsf,fd->bsd", h, w2)

    x = A_x
    xn = _layernorm(x, axis=-1)
    s = _A_ref(xn, A_c_kv, A_w_q, A_w_uk, A_w_uv, A_w_o)
    g = _silu(_B_ref(xn, B_w1, B_w2))
    return x + s * g
```

```cpp
#include <hip/hip_runtime.h>
#include <hip/hip_bf16.h>
#include <stdint.h>
namespace nref {
typedef unsigned short u16;
__device__ __forceinline__ float bf2f(u16 v) { return __uint_as_float(((unsigned)v) << 16); }
__device__ __forceinline__ u16 f2bf(float f) { unsigned u = __float_as_uint(f); u += 0x7FFFu + ((u >> 16) & 1u); return (u16)(u >> 16); }
__device__ __forceinline__ float rbf(float f) { return bf2f(f2bf(f)); }

__global__ void ln_k(const u16* x, u16* xn, int cols) {
  int row = blockIdx.x; const u16* xr = x + (size_t)row * cols;
  float s = 0; for (int c = threadIdx.x; c < cols; c += 64) s += bf2f(xr[c]);
  for (int o = 32; o; o >>= 1) s += __shfl_xor(s, o);
  float mean = s / cols; float v = 0;
  for (int c = threadIdx.x; c < cols; c += 64) { float d = bf2f(xr[c]) - mean; v += d * d; }
  for (int o = 32; o; o >>= 1) v += __shfl_xor(v, o);
  v /= cols;
  float mb = rbf(mean), vb = rbf(v);
  float den = rbf(sqrtf(rbf(vb + 1e-5f)));
  for (int c = threadIdx.x; c < cols; c += 64) { float d = rbf(bf2f(xr[c]) - mb); xn[(size_t)row * cols + c] = f2bf(d / den); }
}
__global__ void gemm_k(const u16* A, const u16* B, u16* C, int M, int N, int K, int act) {
  int n = blockIdx.x * 64 + threadIdx.x; int m = blockIdx.y;
  if (n >= N) return;
  float acc = 0;
  for (int k = 0; k < K; ++k) acc += bf2f(A[(size_t)m * K + k]) * bf2f(B[(size_t)k * N + n]);
  if (act == 1) acc = fmaxf(rbf(acc), 0.f);
  C[(size_t)m * N + n] = f2bf(acc);
}
__global__ void attn_k(const u16* q, const u16* k, const u16* v, u16* ctx, int S, int H, int D) {
  extern __shared__ float sh[];
  __shared__ float red[128];
  int h = blockIdx.x, qi = blockIdx.y; int t = threadIdx.x;
  const float scale = rbf(0.08838834764831845f);
  const u16* qr = q + (size_t)qi * H * D + h * D;
  float mx = -1e30f;
  for (int j = t; j <= qi; j += 128) {
    const u16* kr = k + (size_t)j * H * D + h * D; float a = 0;
    for (int d = 0; d < D; ++d) a += bf2f(qr[d]) * bf2f(kr[d]);
    a = rbf(rbf(a) * scale); sh[j] = a; mx = fmaxf(mx, a);
  }
  red[t] = mx; __syncthreads();
  for (int o = 64; o; o >>= 1) { if (t < o) red[t] = fmaxf(red[t], red[t + o]); __syncthreads(); }
  mx = red[0]; __syncthreads();
  float sm = 0;
  for (int j = t; j <= qi; j += 128) { float e = __expf(sh[j] - mx); sh[j] = e; sm += e; }
  red[t] = sm; __syncthreads();
  for (int o = 64; o; o >>= 1) { if (t < o) red[t] += red[t + o]; __syncthreads(); }
  sm = red[0]; __syncthreads();
  for (int j = t; j <= qi; j += 128) sh[j] = rbf(sh[j] / sm);
  __syncthreads();
  float acc = 0;
  for (int j = 0; j <= qi; ++j) acc += sh[j] * bf2f(v[(size_t)j * H * D + h * D + t]);
  ctx[(size_t)qi * H * D + h * D + t] = f2bf(acc);
}
__global__ void final_k(const u16* x, const u16* s, const u16* y, u16* out, int n) {
  int i = blockIdx.x * 256 + threadIdx.x; if (i >= n) return;
  float yy = bf2f(y[i]); float g = rbf(yy / (1.f + __expf(-yy)));
  float sg = rbf(bf2f(s[i]) * g);
  out[i] = f2bf(bf2f(x[i]) + sg);
}
struct Bufs { u16 *xn, *q, *k, *v, *ctx, *h, *s, *y; };
inline Bufs carve(void* ws) { Bufs b; u16* p = (u16*)ws; b.xn = p; p += 2048 * 512; b.q = p; p += 2048 * 4096; b.k = p; p += 2048 * 4096; b.v = p; p += 2048 * 4096; b.ctx = p; p += 2048 * 4096; b.h = p; p += 2048 * 1376 + 1024; b.s = p; p += 2048 * 512; b.y = p; return b; }
inline void run(const u16* x, const u16* ckv, const u16* wq, const u16* wuk, const u16* wuv, const u16* wo, const u16* w1, const u16* w2, u16* out, void* ws, hipStream_t st) {
  Bufs b = carve(ws);
  ln_k<<<2048, 64, 0, st>>>(x, b.xn, 512);
  gemm_k<<<dim3(64, 2048), 64, 0, st>>>(b.xn, wq, b.q, 2048, 4096, 512, 0);
  gemm_k<<<dim3(64, 2048), 64, 0, st>>>(ckv, wuk, b.k, 2048, 4096, 512, 0);
  gemm_k<<<dim3(64, 2048), 64, 0, st>>>(ckv, wuv, b.v, 2048, 4096, 512, 0);
  attn_k<<<dim3(32, 2048), 128, 2048 * 4, st>>>(b.q, b.k, b.v, b.ctx, 2048, 32, 128);
  gemm_k<<<dim3(8, 2048), 64, 0, st>>>(b.ctx, wo, b.s, 2048, 512, 4096, 0);
  gemm_k<<<dim3(22, 2048), 64, 0, st>>>(b.xn, w1, b.h, 2048, 1376, 512, 1);
  gemm_k<<<dim3(8, 2048), 64, 0, st>>>(b.h, w2, b.y, 2048, 512, 1376, 0);
  final_k<<<2048 * 512 / 256, 256, 0, st>>>(x, b.s, b.y, out, 2048 * 512);
}
}
extern "C" void kernel_launch(void* const* d_in, const int* in_sizes, int n_in, void* d_out, int out_size, void* d_ws, size_t ws_size, hipStream_t stream) {
  using nref::u16;
  nref::run((const u16*)d_in[0], (const u16*)d_in[1], (const u16*)d_in[2], (const u16*)d_in[3], (const u16*)d_in[4], (const u16*)d_in[5], (const u16*)d_in[7], (const u16*)d_in[8], (u16*)d_out, d_ws, stream);
}
```

```cpp
#include <hip/hip_runtime.h>
#include <hip/hip_bf16.h>
#include <stdint.h>

typedef unsigned short u16;
typedef short bf16x8 __attribute__((ext_vector_type(8)));
typedef short s16x4 __attribute__((ext_vector_type(4)));
typedef float f32x16 __attribute__((ext_vector_type(16)));
typedef float f32x4 __attribute__((ext_vector_type(4)));
typedef float f32x2 __attribute__((ext_vector_type(2)));
typedef __bf16 bf16x2_t __attribute__((ext_vector_type(2)));
typedef unsigned u32x4 __attribute__((ext_vector_type(4)));
#define LAS __attribute__((address_space(3)))
typedef LAS const char* lds_cptr;
#define DI __device__ __forceinline__
#define MFMA(a, b, c) __builtin_amdgcn_mfma_f32_32x32x16_bf16((a), (b), (c), 0, 0, 0)
#define SBAR() __builtin_amdgcn_sched_barrier(0)

constexpr size_t WS_KF = 0;
constexpr size_t WS_VF = 16u << 20;
constexpr size_t WS_CTX = 32u << 20;
constexpr size_t WS_XN = 48u << 20;
constexpr size_t WS_H = 50u << 20;
constexpr size_t WS_PART = 56u << 20;
constexpr size_t WS_FLAG = 80u << 20;

constexpr size_t WS_DBG = 96u << 20;
#define RSTAMP(i) do {} while (0)
#define STAMP(i) do {} while (0)
DI unsigned cvtpk(float lo, float hi) { f32x2 v = {lo, hi}; bf16x2_t b = __builtin_convertvector(v, bf16x2_t); return __builtin_bit_cast(unsigned, b); }
DI float bf2f(unsigned v16) { return __uint_as_float(v16 << 16); }
DI float rbf(float f) { return bf2f(cvtpk(f, f) & 0xffffu); }
DI u32x4 pack8(const f32x16& x, int s) {
  u32x4 p = {cvtpk(x[8 * s], x[8 * s + 1]), cvtpk(x[8 * s + 2], x[8 * s + 3]), cvtpk(x[8 * s + 4], x[8 * s + 5]), cvtpk(x[8 * s + 6], x[8 * s + 7])};
  return p;
}
DI __amdgpu_buffer_rsrc_t mk_srd(const void* p, unsigned bytes) { return __builtin_amdgcn_make_buffer_rsrc((void*)p, (short)0, (int)bytes, 0x00020000); }
DI void dma16(__amdgpu_buffer_rsrc_t srd, unsigned voff, unsigned soff, unsigned lds_dst) {
  unsigned keep;
  asm volatile("s_mov_b32 %0, m0\n\ts_mov_b32 m0, %1\n\ts_nop 0\n\tbuffer_load_dwordx4 %2, %3, %4 offen lds\n\ts_mov_b32 m0, %0"
               : "=&s"(keep) : "s"(lds_dst), "v"(voff), "s"(srd), "s"(soff) : "memory");
}
DI bf16x8 ldsr128(lds_cptr p) { return *(const LAS bf16x8*)p; }
typedef short v4i16_t __attribute__((ext_vector_type(4)));
DI s16x4 ldstr(lds_cptr p) { return __builtin_bit_cast(s16x4, __builtin_amdgcn_ds_read_tr16_b64_v4i16((LAS v4i16_t*)p)); }
DI bf16x8 cat8(s16x4 a, s16x4 b) { return __builtin_shufflevector(a, b, 0, 1, 2, 3, 4, 5, 6, 7); }
#define ILV() do { _Pragma("unroll") for (int i_ = 0; i_ < 8; ++i_) { __builtin_amdgcn_sched_group_barrier(0x008, 1, 0); __builtin_amdgcn_sched_group_barrier(0x100, 1, 0); } } while (0)
#define ILV2() do { _Pragma("unroll") for (int i_ = 0; i_ < 2; ++i_) { __builtin_amdgcn_sched_group_barrier(0x008, 1, 0); __builtin_amdgcn_sched_group_barrier(0x100, 1, 0); } } while (0)
#define ILVF() do { _Pragma("unroll") for (int i_ = 0; i_ < 4; ++i_) { __builtin_amdgcn_sched_group_barrier(0x008, 1, 0); __builtin_amdgcn_sched_group_barrier(0x100, 2, 0); } __builtin_amdgcn_sched_group_barrier(0x008, 4, 0); } while (0)
#define WAIT_BAR0() asm volatile("s_waitcnt vmcnt(0) lgkmcnt(0)\n\ts_barrier" ::: "memory")

template <int N> DI void wait_bar() { asm volatile("s_waitcnt vmcnt(%0) lgkmcnt(0)\n\ts_barrier" :: "i"(N) : "memory"); }

#define MFMA16(a, b, c) __builtin_amdgcn_mfma_f32_16x16x32_bf16((a), (b), (c), 0, 0, 0)
#define SGB(mask, n) __builtin_amdgcn_sched_group_barrier(mask, n, 0)
template <bool KT>
DI void k0_body(char* lds, __amdgpu_buffer_rsrc_t sC, __amdgpu_buffer_rsrc_t sUK, __amdgpu_buffer_rsrc_t sUV, unsigned vC, unsigned vW, unsigned sCbase, unsigned sWbase,
                int h, int mt, int wid, int lane, char* outbase, unsigned long long* dbg) {
  const int c = lane & 15, g = lane >> 4, km = wid & 1, nq = wid >> 1;
  const unsigned lds0 = (unsigned)(uintptr_t)lds;
  auto piece = [&](int s, int m) {
    const unsigned b = lds0 + (unsigned)(s & 3) * 32768u;
    if (m < 2) dma16(sC, vC, sCbase + (unsigned)(m * 16 * 1024 + s * 64), b + (unsigned)((wid * 2 + m) * 1024));
    else       dma16((m & 1) ? sUV : sUK, vW, sWbase + (unsigned)((s * 32 * 4096) * 2), b + 16384u + (unsigned)((wid * 2 + (m & 1)) * 1024));
  };
  const int cb = (128 * km + c) * 64 + ((g ^ (3 * ((c >> 3) & 1))) * 16);
  int wb[2];
#pragma unroll
  for (int par = 0; par < 2; ++par) wb[par] = 16384 + g * 4096 + nq * 512 + ((lane >> 2) & 3) * 64 + ((par ^ (g & 1)) * 32) + (lane & 3) * 8;
  f32x4 acc[8][4];
#pragma unroll
  for (int i = 0; i < 8; ++i)
#pragma unroll
    for (int j = 0; j < 4; ++j) acc[i][j] = f32x4{};
  bf16x8 cf[2][4], wf[2][4];
  auto rdW = [&](int s, int buf) {
    lds_cptr base = (lds_cptr)lds + (s & 3) * 32768;
#pragma unroll
    for (int j = 0; j < 4; ++j) wf[buf][j] = cat8(ldstr(base + wb[j & 1] + (j >> 1) * 256), ldstr(base + wb[j & 1] + 2048 + (j >> 1) * 256));
  };
  auto rdC = [&](int s, int half, int buf) {
    lds_cptr base = (lds_cptr)lds + (s & 3) * 32768;
#pragma unroll
    for (int i = 0; i < 4; ++i) cf[buf][i] = ldsr128(base + cb + (half * 4 + i) * 1024);
  };
  auto mm1 = [&](int wbuf, int cbuf, int half, int i) {
#pragma unroll
    for (int j = 0; j < 4; ++j) acc[half * 4 + i][j] = KT ? MFMA16(wf[wbuf][j], cf[cbuf][i], acc[half * 4 + i][j]) : MFMA16(cf[cbuf][i], wf[wbuf][j], acc[half * 4 + i][j]);
  };
    wait_bar<12>();
    rdW(0, 0); rdC(0, 0, 0);
#pragma unroll
  for (int s = 0; s < 16; ++s) {
    const int wbuf = s & 1;
    rdC(s, 1, 1);
    mm1(wbuf, 0, 0, 0); mm1(wbuf, 0, 0, 1); mm1(wbuf, 0, 0, 2); mm1(wbuf, 0, 0, 3);
    SGB(0x008, 2); SGB(0x100, 1); SGB(0x008, 2); SGB(0x100, 1); SGB(0x008, 2); SGB(0x100, 1); SGB(0x008, 2); SGB(0x100, 1); SGB(0x008, 8); SBAR();
    if (s < 15) {
      if (s <= 12) wait_bar<8>(); else if (s == 13) wait_bar<4>(); else wait_bar<0>();
      SBAR();
      rdW(s + 1, wbuf ^ 1); rdC(s + 1, 0, 0);
      if (s + 4 < 16) {
        mm1(wbuf, 1, 1, 0); SGB(0x008, 1); SGB(0x100, 3); SGB(0x008, 1); SGB(0x100, 3); SGB(0x008, 1); SGB(0x100, 3); SGB(0x008, 1); SGB(0x100, 3); SBAR(); piece(s + 4, 0); SBAR();
        mm1(wbuf, 1, 1, 1); SBAR(); piece(s + 4, 1); SBAR();
        mm1(wbuf, 1, 1, 2); SBAR(); piece(s + 4, 2); SBAR();
        mm1(wbuf, 1, 1, 3); SBAR(); piece(s + 4, 3); SBAR();
      } else { mm1(wbuf, 1, 1, 0); SGB(0x008, 1); SGB(0x100, 3); SGB(0x008, 1); SGB(0x100, 3); SGB(0x008, 1); SGB(0x100, 3); SGB(0x008, 1); SGB(0x100, 3); SBAR(); mm1(wbuf, 1, 1, 1); mm1(wbuf, 1, 1, 2); mm1(wbuf, 1, 1, 3); SBAR(); }
    } else { mm1(wbuf, 1, 1, 0); mm1(wbuf, 1, 1, 1); mm1(wbuf, 1, 1, 2); mm1(wbuf, 1, 1, 3); }
  }
  if (KT) {
#pragma unroll
    for (int i = 0; i < 8; ++i) {
      const int T = mt * 4 + 2 * km + (i >> 2), kb = (i >> 1) & 1;
#pragma unroll
      for (int dp = 0; dp < 2; ++dp) {
        const int ks = 2 * (2 * nq + dp) + (g >> 1);
        const int frag = ((h * 32 + T) * 2 + kb) * 8 + ks;
        u32x4 w = {cvtpk(acc[i][2 * dp][0], acc[i][2 * dp][1]), cvtpk(acc[i][2 * dp][2], acc[i][2 * dp][3]), cvtpk(acc[i][2 * dp + 1][0], acc[i][2 * dp + 1][1]), cvtpk(acc[i][2 * dp + 1][2], acc[i][2 * dp + 1][3])};
        *(u32x4*)(outbase + (size_t)frag * 1024 + ((g & 1) * 32 + (i & 1) * 16 + c) * 16) = w;
      }
    }
  } else {
#pragma unroll
    for (int ip = 0; ip < 4; ++ip) {
      const int T = mt * 4 + 2 * km + (ip >> 1);
#pragma unroll
      for (int j = 0; j < 4; ++j) {
        unsigned a0 = cvtpk(acc[2 * ip][j][0], acc[2 * ip][j][1]), a1 = cvtpk(acc[2 * ip][j][2], acc[2 * ip][j][3]);
        unsigned b0 = cvtpk(acc[2 * ip + 1][j][0], acc[2 * ip + 1][j][1]), b1 = cvtpk(acc[2 * ip + 1][j][2], acc[2 * ip + 1][j][3]);
        auto r0 = __builtin_amdgcn_permlane32_swap(a0, b0, false, false);
        auto r1 = __builtin_amdgcn_permlane32_swap(a1, b1, false, false);
        u32x4 w = {r0[0], r1[0], r0[1], r1[1]};
        const int ks = 2 * (ip & 1) + (g >> 1), db = 2 * (nq - 2) + (j >> 1);
        const int frag = ((h * 32 + T) * 4 + db) * 4 + ks;
        *(u32x4*)(outbase + (size_t)frag * 1024 + ((g & 1) * 32 + (j & 1) * 16 + c) * 16) = w;
      }
    }
  }
  }

__global__ __launch_bounds__(512, 2) void k0_kvproj(const u16* __restrict__ x, const u16* __restrict__ ckv, const u16* __restrict__ wuk, const u16* __restrict__ wuv, char* __restrict__ ws) {
  extern __shared__ __attribute__((aligned(16))) char lds[];
  const int tid = threadIdx.x, lane = tid & 63, wid = __builtin_amdgcn_readfirstlane(tid >> 6);
  const int b = blockIdx.x, xcd = b & 7, jj = b >> 3, h = 4 * xcd + (jj >> 3), mt = jj & 7;
  const __amdgpu_buffer_rsrc_t sC = mk_srd(ckv, 2048u * 512u * 2u), sUK = mk_srd(wuk, 512u * 4096u * 2u), sUV = mk_srd(wuv, 512u * 4096u * 2u);
  unsigned long long* dbg = (unsigned long long*)(ws + WS_DBG);
    if (b < 64 && tid == 0) ((unsigned*)(ws + WS_FLAG))[b * 64] = 0u;
  const int R = b * 8 + wid;
  u32x4 xr;
  { const u16* xp = x + (size_t)R * 512 + lane * 8; asm volatile("global_load_dwordx4 %0, %1, off" : "=v"(xr) : "v"(xp) : "memory"); }
  const unsigned vC = (unsigned)((lane >> 2) * 1024 + (((lane & 3) ^ (3 * ((lane >> 5) & 1))) * 16));
  const unsigned vW = (unsigned)((((lane >> 2) & 3) * 4096 + (lane >> 4) * 32 + ((lane & 3) ^ (2 * ((wid >> 1) & 1))) * 8) * 2);
  const unsigned sCbase = (unsigned)((mt * 256 + wid * 32) * 1024);
  const unsigned sWbase = (unsigned)((wid * 4 * 4096 + h * 128) * 2);
  {
    const unsigned lds0 = (unsigned)(uintptr_t)lds;
#pragma unroll
    for (int s = 0; s < 4; ++s) {
      const unsigned bb = lds0 + (unsigned)s * 32768u;
#pragma unroll
      for (int m = 0; m < 2; ++m) dma16(sC, vC, sCbase + (unsigned)(m * 16 * 1024 + s * 64), bb + (unsigned)((wid * 2 + m) * 1024));
#pragma unroll
      for (int m = 0; m < 2; ++m) dma16(m ? sUV : sUK, vW, sWbase + (unsigned)((s * 32 * 4096) * 2), bb + 16384u + (unsigned)((wid * 2 + m) * 1024));
    }
  }
  {
    asm volatile("s_waitcnt vmcnt(16)" : "+v"(xr) :: "memory");
    float v[8];
#pragma unroll
    for (int e = 0; e < 4; ++e) { v[2 * e] = __uint_as_float(xr[e] << 16); v[2 * e + 1] = __uint_as_float(xr[e] & 0xffff0000u); }
    float s = 0;
#pragma unroll
    for (int e = 0; e < 8; ++e) s += v[e];
#pragma unroll
    for (int o = 32; o; o >>= 1) s += __shfl_xor(s, o);
    const float mean = s * (1.f / 512.f);
    float q = 0;
#pragma unroll
    for (int e = 0; e < 8; ++e) { const float d = v[e] - mean; q += d * d; }
#pragma unroll
    for (int o = 32; o; o >>= 1) q += __shfl_xor(q, o);
    const float mb = rbf(mean), vb = rbf(q * (1.f / 512.f));
    const float den = rbf(__builtin_sqrtf(rbf(vb + 1e-5f)));
    float o8[8];
#pragma unroll
    for (int e = 0; e < 8; ++e) o8[e] = rbf(v[e] - mb) / den;
    u32x4 w = {cvtpk(o8[0], o8[1]), cvtpk(o8[2], o8[3]), cvtpk(o8[4], o8[5]), cvtpk(o8[6], o8[7])};
    *(u32x4*)(ws + WS_XN + ((size_t)((R >> 4) * 16 + (lane >> 2)) * 64 + ((lane & 3) * 16 + (R & 15))) * 16) = w;
  }
  if (wid < 4) k0_body<true>(lds, sC, sUK, sUV, vC, vW, sCbase, sWbase, h, mt, wid, lane, ws + WS_KF, dbg);
  else         k0_body<false>(lds, sC, sUK, sUV, vC, vW, sCbase, sWbase, h, mt, wid, lane, ws + WS_VF, dbg);
}

constexpr int L_V = 0, L_QA = 65536, L_QB = 98304, L_KS2 = 98304, L_KS0 = 131072;
constexpr int PB_STAGE = 28672;
constexpr float QSCALE = 0.08838834764831845f * 1.4426950408889634f;
constexpr float THR = 8.0f;
#define PIN(x) asm volatile("" : "+v"(x))
DI float max3f(float a, float b, float c) { float r; asm("v_max3_f32 %0, %1, %2, %3" : "=v"(r) : "v"(a), "v"(b), "v"(c)); return r; }
DI float max2f(float a, float b) { float r; asm("v_max_f32_e32 %0, %1, %2" : "=v"(r) : "v"(a), "v"(b)); return r; }
DI float fadd_s(float a, float b) { float r; asm("v_add_f32_e32 %0, %1, %2" : "=v"(r) : "v"(a), "v"(b)); return r; }
DI float swap32_max(float m) { auto rr = __builtin_amdgcn_permlane32_swap(__float_as_uint(m), __float_as_uint(m), false, false); return fmaxf(__uint_as_float(rr[0]), __uint_as_float(rr[1])); }
DI float swap32_add(float m) { auto rr = __builtin_amdgcn_permlane32_swap(__float_as_uint(m), __float_as_uint(m), false, false); return __uint_as_float(rr[0]) + __uint_as_float(rr[1]); }

template <bool FFN>
DI void phaseB(unsigned long long* dbg, char* lds, char* ws, const u16* wq, const u16* w1, __amdgpu_buffer_rsrc_t sK, __amdgpu_buffer_rsrc_t sV, int h, int bi, int wid, int lane) {
  const int c = lane & 15, g = lane >> 4, rgp = wid >> 1, ch = wid & 1;
  const unsigned lds0 = (unsigned)(uintptr_t)lds;
  const __amdgpu_buffer_rsrc_t sX = mk_srd(ws + WS_XN, 2048u * 512u * 2u), sQ = mk_srd(wq, 512u * 4096u * 2u), s1 = mk_srd(w1, 512u * 1376u * 2u);
  const int xrb = ((wid < 4) ? (15 - bi) * 8 : bi * 8 - 8) + 2 * wid;
  const unsigned vL = (unsigned)lane * 16u;
  const unsigned sXbase = (unsigned)xrb * 16384u;
  const int c4 = (lane & 3) ^ (2 * ((wid >> 1) & 1));
  const unsigned vQ = (unsigned)((((lane >> 2) & 3) * 4096 + (lane >> 4) * 32 + c4 * 8) * 2);
  const unsigned v1 = (unsigned)((((lane >> 2) & 3) * 1376 + ((lane >> 4) & 1) * 32 + c4 * 8) * 2);
  const unsigned sQbase = (unsigned)((wid * 4 * 4096 + h * 128) * 2), s1base = (unsigned)((wid * 4 * 1376 + h * 64) * 2);
  constexpr int NP = FFN ? 4 : 3;
  auto piece = [&](int s, int m) {
    const unsigned b = lds0 + (unsigned)((s & 3) * PB_STAGE);
    if (m < 2) dma16(sX, vL, sXbase + (unsigned)(m * 16384 + s * 1024), b + (unsigned)((wid * 2 + m) * 1024));
    else if (m == 2) dma16(sQ, vQ, sQbase + (unsigned)(s * 32 * 4096 * 2), b + 16384u + (unsigned)(wid * 1536));
    else if (lane < 32) dma16(s1, v1, s1base + (unsigned)(s * 32 * 1376 * 2), b + 16384u + 1024u + (unsigned)(wid * 1536));
  };
  auto kvpiece = [&](__amdgpu_buffer_rsrc_t sr, int tile, int ldsoff, int m) {
    dma16(sr, vL, (unsigned)(h * 524288 + tile * 16384 + (wid * 2 + m) * 1024), lds0 + (unsigned)(ldsoff + (wid * 2 + m) * 1024));
  };
  const int v0slot = L_V + ((0 - (2 * bi + 2)) & 3) * 16384;
  constexpr int NJ = FFN ? 6 : 4;
  const int xb = rgp * 4096 + lane * 16;
  int wb[2];
#pragma unroll
  for (int par = 0; par < 2; ++par) wb[par] = 16384 + g * 3072 + ((lane >> 2) & 3) * 64 + ((par ^ (g & 1)) * 32) + (lane & 3) * 8;
  f32x4 acc[4][NJ];
#pragma unroll
  for (int i = 0; i < 4; ++i)
#pragma unroll
    for (int j = 0; j < NJ; ++j) acc[i][j] = f32x4{};
  bf16x8 xf[2][2], wf[2][NJ];
  auto rdW = [&](int s, int buf) {
    lds_cptr base = (lds_cptr)lds + (s & 3) * PB_STAGE;
#pragma unroll
    for (int j = 0; j < NJ; ++j) { const int u = (j < 4) ? (2 * ch + (j >> 1)) : (4 + ch);
      wf[buf][j] = cat8(ldstr(base + wb[j & 1] + u * 256), ldstr(base + wb[j & 1] + 1536 + u * 256)); }
  };
  auto rdX = [&](int s, int half, int buf) {
    lds_cptr base = (lds_cptr)lds + (s & 3) * PB_STAGE;
#pragma unroll
    for (int i = 0; i < 2; ++i) xf[buf][i] = ldsr128(base + xb + (half * 2 + i) * 1024);
  };
  auto mm1 = [&](int wbuf, int xbuf, int half, int i) {
#pragma unroll
    for (int j = 0; j < NJ; ++j) acc[half * 2 + i][j] = MFMA16(wf[wbuf][j], xf[xbuf][i], acc[half * 2 + i][j]);
  };
#pragma unroll
  for (int s = 0; s < 4; ++s)
#pragma unroll
    for (int m = 0; m < NP; ++m) piece(s, m);
  STAMP(1); wait_bar<3 * NP>(); STAMP(2); RSTAMP(62);
  rdW(0, 0); rdX(0, 0, 0);
#pragma unroll
  for (int s = 0; s < 16; ++s) {
    const int wbuf = s & 1;
    rdX(s, 1, 1);
    mm1(wbuf, 0, 0, 0); mm1(wbuf, 0, 0, 1);
    SGB(0x008, 1); SGB(0x100, 1); SGB(0x008, 1); SGB(0x100, 1); SGB(0x008, 2 * NJ - 2); SBAR();
    if (s < 15) {
      if (s <= 12) wait_bar<2 * NP>(); else if (s == 13) wait_bar<NP + 4>(); else wait_bar<6>();
      SBAR();
      rdW(s + 1, wbuf ^ 1); rdX(s + 1, 0, 0);
      mm1(wbuf, 1, 1, 0);
#pragma unroll
      for (int q = 0; q < NJ; ++q) { SGB(0x008, 1); SGB(0x100, 3); }
      SBAR();
      if (s + 4 < 16) { piece(s + 4, 0); piece(s + 4, 1); SBAR(); }
      else if (s == 12) { kvpiece(sK, 0, L_KS0, 0); kvpiece(sK, 0, L_KS0, 1); SBAR(); }
      else if (s == 13) { kvpiece(sV, 0, v0slot, 0); SBAR(); }
      mm1(wbuf, 1, 1, 1); SBAR();
      if (s + 4 < 16) { piece(s + 4, 2); if (FFN) piece(s + 4, 3); SBAR(); }
      else if (s == 12) { kvpiece(sK, 1, L_KS0 + 16384, 0); kvpiece(sK, 1, L_KS0 + 16384, 1); SBAR(); }
      else if (s == 13) { kvpiece(sV, 0, v0slot, 1); SBAR(); }
    } else {
      asm volatile("s_waitcnt lgkmcnt(0)\n\ts_barrier" ::: "memory");
      SBAR();
      mm1(wbuf, 1, 1, 0); mm1(wbuf, 1, 1, 1);
    }
  }
  STAMP(3);
  char* qdst = lds + ((rgp < 2) ? L_QA : L_QB);
#pragma unroll
  for (int ii = 0; ii < 4; ++ii) {
    const int rb32 = 2 * (rgp & 1) + (ii >> 1), r = 16 * (ii & 1) + c;
#pragma unroll
    for (int dpp = 0; dpp < 2; ++dpp) {
      const int ks = 2 * (2 * ch + dpp) + (g >> 1);
      const f32x4 a = acc[ii][2 * dpp] * QSCALE, b = acc[ii][2 * dpp + 1] * QSCALE;
      u32x4 w = {cvtpk(a[0], a[1]), cvtpk(a[2], a[3]), cvtpk(b[0], b[1]), cvtpk(b[2], b[3])};
      *(u32x4*)(qdst + ((rb32 * 8 + ks) * 64 + (g & 1) * 32 + r) * 16) = w;
    }
    if (FFN) {
      const int rb16 = ((rgp < 2) ? (15 - bi) * 8 : bi * 8) + 4 * (rgp & 1) + ii;
      const bool valid = (h * 64 + ch * 32) < 1376;
      f32x4 a = acc[ii][NJ - 2], b = acc[ii][NJ - 1];
#pragma unroll
      for (int e = 0; e < 4; ++e) { a[e] = valid ? fmaxf(a[e], 0.f) : 0.f; b[e] = valid ? fmaxf(b[e], 0.f) : 0.f; }
      u32x4 w = {cvtpk(a[0], a[1]), cvtpk(a[2], a[3]), cvtpk(b[0], b[1]), cvtpk(b[2], b[3])};
      *(u32x4*)(ws + WS_H + ((size_t)(rb16 * 44 + 2 * h + ch) * 64 + lane) * 16) = w;
    }
  }
}

DI void store_ctx(char* ws, const float (&y)[16], int rowblk16, int h, int db, int r, int hi) {
#pragma unroll
  for (int sx = 0; sx < 2; ++sx) {
    u32x4 w = {cvtpk(y[8 * sx], y[8 * sx + 1]), cvtpk(y[8 * sx + 2], y[8 * sx + 3]), cvtpk(y[8 * sx + 4], y[8 * sx + 5]), cvtpk(y[8 * sx + 6], y[8 * sx + 7])};
    *(u32x4*)(ws + WS_CTX + ((size_t)((rowblk16 + (r >> 4)) * 128 + h * 4 + db) * 64 + (2 * sx + hi) * 16 + (r & 15)) * 16) = w;
  }
}

template <int OFF> DI void dma2(__amdgpu_buffer_rsrc_t srd, unsigned voff, unsigned soff, unsigned lds_dst) {
  asm volatile("s_mov_b32 m0, %0\n\ts_nop 0\n\tbuffer_load_dwordx4 %1, %2, %3 offen offset:%4 lds\n\tbuffer_load_dwordx4 %1, %2, %3 offen offset:%5 lds"
               :: "s"(lds_dst), "v"(voff), "s"(srd), "s"(soff), "i"(OFF), "i"(OFF + 1024) : "memory");
}
DI unsigned rdl(unsigned v, int l) { return (unsigned)__builtin_amdgcn_readlane((int)v, l); }

DI void phaseC(unsigned long long* dbg, char* lds, char* ws, __amdgpu_buffer_rsrc_t sK, int h, int bi, int wid, int lane) {
  const int rg = wid & 3, grp = wid >> 2, r = lane & 31, hi = lane >> 5;
  const unsigned lds0 = (unsigned)(uintptr_t)lds;
  const int P1 = 2 * bi + 2;
  const unsigned vL = (unsigned)lane * 16u;
  unsigned tKs, tVs, tC;
  {
    const unsigned hbase = (unsigned)(h * 524288), OOBS = 0x7f000000u;
    auto tix = [&](int g) { return g < P1 ? g : P1 + 2 * (g - P1) + grp; };
    auto kslot = [&](int g) { return ((grp && g >= P1) ? L_KS2 : L_KS0) + (g & 1) * 16384; };
    auto vslot = [&](int g) { return g < P1 ? L_V + ((g - P1) & 3) * 16384 : ((grp && g == 16) ? L_KS2 + 16384 : L_V + (2 * grp + (g & 1)) * 16384); };
    const int k = lane, gk = k + 1;
    const bool ksh = gk < P1, kok = gk <= 16;
    const unsigned ko = ksh ? wid * 2048 : rg * 4096;
    tKs = kok ? (hbase + (unsigned)(tix(gk) * 16384) + ko) | (unsigned)(kslot(gk) >> 14) : (OOBS | 14u);
    const bool vsh = k < P1;
    const int gv = (vsh || !grp) ? k : k - 1;
    const bool vok = vsh || gv >= P1;
    const unsigned vo = vsh ? wid * 2048 : rg * 4096;
    tVs = vok ? ((16u << 20) + hbase + (unsigned)(tix(gv) * 16384) + vo) | (unsigned)(vslot(gv) >> 14) : (OOBS | 14u);
    const int g = lane - 1;
    const bool first = (g < 0) || (grp && g + 1 == P1);
    const bool mask = (g + 1 == 16) || (grp && (g + 1 == P1 - 2 || g + 1 == P1 - 1));
    const unsigned qsrc = (g < 0 && grp) ? (unsigned)L_QB : (unsigned)L_QA;
    tC = (unsigned)(kslot(g + 1) >> 14) | ((unsigned)((g < 0 ? L_KS0 : vslot(g)) >> 14) << 4) | ((unsigned)(kslot(g + 2) >> 14) << 8) | ((qsrc >> 14) << 12) |
         (first ? 1u << 16 : 0u) | (mask ? 1u << 17 : 0u) | ((kok && !ksh) ? 1u << 18 : 0u) | ((vok && !vsh) ? 1u << 19 : 0u) |
         ((unsigned)((((grp && g + 1 < P1) ? bi : 15 - bi) * 4 + rg - tix(g + 1) * 2) & 255) << 24);
  }
  const float rl = (float)(r - 4 * hi) + 0.5f;
  bf16x8 qf[8];
  f32x16 o[4], S1, T0, T1, negm;
  u32x4 pk0 = {}, pk1 = {}, pk2, pk3;
  float m = 0.f, l = 0.f, lB = 0.f, ma, mb;
  bf16x8 k0, k1, k2, k3, v0, v1, v2, v3;
#pragma unroll
  for (int d = 0; d < 4; ++d) o[d] = f32x16{};
  S1 = f32x16{};
#define KF(i) ldsr128(kp + (i) * 1024)
#define VF(db, ks) ldsr128(vp + ((db) * 4 + (ks)) * 1024)
#define EX(v) __builtin_amdgcn_exp2f(v)
#define BFR(x) __builtin_bit_cast(bf16x8, x)
#define ADD2(a0, a1) asm("v_add_f32_e32 %0, %0, %2\n\tv_add_f32_e32 %1, %1, %3" : "+v"(sa), "+v"(sb) : "v"(a0), "v"(a1))
#define MX2(X, i) asm("v_max3_f32 %0, %0, %2, %3\n\tv_max3_f32 %1, %1, %4, %5" : "+v"(ma), "+v"(mb) : "v"(X[i]), "v"(X[i + 1]), "v"(X[i + 2]), "v"(X[i + 3]))
  unsigned ksrc = 0x7f000000u, kdst = lds0 + 0x38000u, vsrc = 0x7f000000u, vdst = lds0 + 0x38000u, dfl = 0;
#define HOOK0() do { if (dfl & 1) dma2<0>(sK, vL, ksrc, kdst); SBAR(); } while (0)
#define HOOK1() do { if (dfl & 2) dma2<2048>(sK, vL, ksrc, kdst); SBAR(); } while (0)
#define HOOK2() do { if (dfl & 1) dma2<0>(sK, vL, vsrc, vdst); SBAR(); } while (0)
#define HOOK3() do { if (dfl & 4) dma2<2048>(sK, vL, vsrc, vdst); SBAR(); } while (0)
#define PLAN(k) do { const unsigned a_ = rdl(tKs, k), b_ = rdl(tVs, k); dfl = 1u | ((rdl(tC, k) >> 17) & 6u); ksrc = a_ & ~15u; vsrc = b_ & ~15u; \
    kdst = lds0 + (a_ & 15u) * 16384u + ((dfl & 2u) ? rg * 4096u : wid * 2048u); vdst = lds0 + (b_ & 15u) * 16384u + ((dfl & 4u) ? rg * 4096u : wid * 2048u); } while (0)
  STAMP(4); RSTAMP(63);
#pragma unroll 1
  for (int g = -1; g < 16; ++g) {
    const unsigned tc = rdl(tC, g + 1);
    const lds_cptr kp = (lds_cptr)lds + (lane * 16 + (tc & 15u) * 16384u);
    const lds_cptr vp = (lds_cptr)lds + (lane * 16 + ((tc >> 4) & 15u) * 16384u);
    const bool first_next = (tc >> 16) & 1u;
    dfl = 0;
    if (grp == 0 || g <= 0) {
      if (g < 0) asm volatile("s_waitcnt vmcnt(4) lgkmcnt(0)\n\ts_barrier" ::: "memory");
      else { asm volatile("s_waitcnt vmcnt(0) lgkmcnt(0)\n\ts_barrier" ::: "memory"); PLAN(g + 1); }
      STAMP(6 + g);
      k0 = KF(0); k1 = KF(1); k2 = KF(2); k3 = KF(3);
    }
    if (first_next) {
      const lds_cptr qp = (lds_cptr)lds + (lane * 16 + rg * 8192 + ((tc >> 12) & 15u) * 16384u);
#pragma unroll
      for (int ks = 0; ks < 8; ++ks) qf[ks] = ldsr128(qp + ks * 1024);
      negm = f32x16{};
    }
    PIN(negm);
    SBAR();
    {
      float sa = 0.f, sb = 0.f;
      bf16x8 k4, k5;
      T0 = MFMA(k0, qf[0], negm); k4 = KF(4); k5 = KF(5); ADD2(S1[0], S1[1]); pk2[0] = cvtpk(S1[0], S1[1]); PIN(pk2); SBAR();
      T0 = MFMA(k1, qf[1], T0); ADD2(S1[2], S1[3]); pk2[1] = cvtpk(S1[2], S1[3]); PIN(pk2); SBAR();
      T0 = MFMA(k2, qf[2], T0); k0 = KF(6); k1 = KF(7); ADD2(S1[4], S1[5]); pk2[2] = cvtpk(S1[4], S1[5]); PIN(pk2); SBAR();
      T0 = MFMA(k3, qf[3], T0); ADD2(S1[6], S1[7]); pk2[3] = cvtpk(S1[6], S1[7]); PIN(pk2); SBAR();
      HOOK0();
      T0 = MFMA(k4, qf[4], T0); k2 = KF(8); k3 = KF(9); ADD2(S1[8], S1[9]); pk3[0] = cvtpk(S1[8], S1[9]); PIN(pk3); SBAR();
      T0 = MFMA(k5, qf[5], T0); ADD2(S1[10], S1[11]); pk3[1] = cvtpk(S1[10], S1[11]); PIN(pk3); SBAR();
      T0 = MFMA(k0, qf[6], T0); k4 = KF(10); k5 = KF(11); ADD2(S1[12], S1[13]); pk3[2] = cvtpk(S1[12], S1[13]); PIN(pk3); SBAR();
      T0 = MFMA(k1, qf[7], T0); ADD2(S1[14], S1[15]); pk3[3] = cvtpk(S1[14], S1[15]); PIN(pk3); SBAR();
      HOOK1();
      l += sa + sb; PIN(pk2); PIN(pk3);
      T1 = MFMA(k2, qf[0], negm); k0 = KF(12); k1 = KF(13); SBAR();
      T1 = MFMA(k3, qf[1], T1); SBAR();
      T1 = MFMA(k4, qf[2], T1); k2 = KF(14); k3 = KF(15); SBAR();
      T1 = MFMA(k5, qf[3], T1); SBAR();
      HOOK2();
      ma = T0[0]; mb = T0[1];
      T1 = MFMA(k0, qf[4], T1); v0 = VF(0, 0); v1 = VF(1, 0); MX2(T0, 2); SBAR();
      T1 = MFMA(k1, qf[5], T1); MX2(T0, 6); SBAR();
      T1 = MFMA(k2, qf[6], T1); v2 = VF(2, 0); v3 = VF(3, 0); MX2(T0, 10); SBAR();
      T1 = MFMA(k3, qf[7], T1); ma = max3f(ma, T0[14], T0[15]); SBAR();
      HOOK3();
    }
    if (grp == 1 && g >= 0) {
      asm volatile("s_waitcnt vmcnt(0) lgkmcnt(0)\n\ts_barrier" ::: "memory");
      v0 = VF(0, 0); v1 = VF(1, 0); v2 = VF(2, 0); v3 = VF(3, 0);
      if (g < 15) PLAN(g + 2); else dfl = 0;
      STAMP(6 + g);
    } else dfl = 0;
    {
      const lds_cptr kp = (lds_cptr)lds + (lane * 16 + ((tc >> 8) & 15u) * 16384u);
      bf16x8 v4, v5;
      o[0] = MFMA(v0, BFR(pk0), o[0]); v4 = VF(0, 1); v5 = VF(1, 1); SBAR();
      o[1] = MFMA(v1, BFR(pk0), o[1]); SBAR();
      o[2] = MFMA(v2, BFR(pk0), o[2]); v0 = VF(2, 1); v1 = VF(3, 1); MX2(T1, 0); MX2(T1, 4); SBAR();
      o[3] = MFMA(v3, BFR(pk0), o[3]); MX2(T1, 8); MX2(T1, 12); SBAR();
      ma = max2f(ma, mb);
      bool resc = false; float fsc = 1.f;
      if (__builtin_expect(((tc >> 16) & 3u) != 0u || __any(ma > THR), 0)) {
        if ((tc >> 17) & 1u) {
          const float lfB = ((float)(((int)tc >> 24) * 32 + 64) + rl) * 1.0e30f;
#pragma unroll
          for (int i = 0; i < 16; ++i) { const int kk = (i & 3) + 8 * (i >> 2); float t0_, t1_;
            asm("v_add_f32_e32 %2, %5, %4\n\tv_add_f32_e32 %3, %6, %4\n\tv_min_f32_e32 %0, %0, %2\n\tv_min_f32_e32 %1, %1, %3"
                : "+v"(T0[i]), "+v"(T1[i]), "=&v"(t0_), "=&v"(t1_) : "v"(lfB), "i"(__builtin_bit_cast(int, -(float)(kk + 64) * 1.0e30f)), "i"(__builtin_bit_cast(int, -(float)(kk + 96) * 1.0e30f))); }
        }
        ma = T0[0]; mb = T0[1];
        MX2(T0, 2); MX2(T0, 6); MX2(T0, 10); ma = max3f(ma, T0[14], T0[15]); MX2(T1, 0); MX2(T1, 4); MX2(T1, 8); MX2(T1, 12);
        const float a = swap32_max(max2f(ma, mb));
        float dl;
        if (first_next) { dl = fmaxf(a, -1.0e29f); lB = l; l = 0.f; m = dl; }
        else { dl = fmaxf(a, 0.f); m += dl; fsc = EX(-dl); l *= fsc; resc = true; }
#pragma unroll
        for (int i = 0; i < 16; ++i) { T0[i] -= dl; T1[i] -= dl; negm[i] = -m; }
      }
      SBAR();
      float sa = 0.f, sb = 0.f;
#define EX4(X, i) do { X[i] = EX(X[i]); X[i + 1] = EX(X[i + 1]); X[i + 2] = EX(X[i + 2]); X[i + 3] = EX(X[i + 3]); PIN(X); } while (0)
#define EX2(X, i) do { X[i] = EX(X[i]); X[i + 1] = EX(X[i + 1]); PIN(X); } while (0)
      HOOK0();
      o[0] = MFMA(v4, BFR(pk1), o[0]); v2 = VF(0, 2); v3 = VF(1, 2); EX4(T0, 0); SBAR();
      o[1] = MFMA(v5, BFR(pk1), o[1]); EX4(T0, 4); SBAR();
      o[2] = MFMA(v0, BFR(pk1), o[2]); v4 = VF(2, 2); v5 = VF(3, 2); EX4(T0, 8); SBAR();
      o[3] = MFMA(v1, BFR(pk1), o[3]); EX4(T0, 12); SBAR();
      HOOK1();
      o[0] = MFMA(v2, BFR(pk2), o[0]); v0 = VF(0, 3); v1 = VF(1, 3); EX2(T1, 0); ADD2(T0[0], T0[1]); pk0[0] = cvtpk(T0[0], T0[1]); PIN(pk0); SBAR();
      o[1] = MFMA(v3, BFR(pk2), o[1]); EX2(T1, 2); ADD2(T0[2], T0[3]); pk0[1] = cvtpk(T0[2], T0[3]); PIN(pk0); SBAR();
      o[2] = MFMA(v4, BFR(pk2), o[2]); v2 = VF(2, 3); v3 = VF(3, 3); EX2(T1, 4); ADD2(T0[4], T0[5]); pk0[2] = cvtpk(T0[4], T0[5]); PIN(pk0); SBAR();
      o[3] = MFMA(v5, BFR(pk2), o[3]); EX2(T1, 6); ADD2(T0[6], T0[7]); pk0[3] = cvtpk(T0[6], T0[7]); PIN(pk0); SBAR();
      HOOK2();
      o[0] = MFMA(v0, BFR(pk3), o[0]); k0 = KF(0); k1 = KF(1); EX2(T1, 8); ADD2(T0[8], T0[9]); pk1[0] = cvtpk(T0[8], T0[9]); PIN(pk1); SBAR();
      o[1] = MFMA(v1, BFR(pk3), o[1]); EX2(T1, 10); ADD2(T0[10], T0[11]); pk1[1] = cvtpk(T0[10], T0[11]); PIN(pk1); SBAR();
      o[2] = MFMA(v2, BFR(pk3), o[2]); k2 = KF(2); k3 = KF(3); EX2(T1, 12); ADD2(T0[12], T0[13]); pk1[2] = cvtpk(T0[12], T0[13]); PIN(pk1); SBAR();
      o[3] = MFMA(v3, BFR(pk3), o[3]); EX2(T1, 14); ADD2(T0[14], T0[15]); pk1[3] = cvtpk(T0[14], T0[15]); PIN(pk1); SBAR();
      HOOK3();
      l += sa + sb;
      S1 = T1;
      if (__builtin_expect(resc, 0)) {
#pragma unroll
        for (int d = 0; d < 4; ++d)
#pragma unroll
          for (int i = 0; i < 16; ++i) o[d][i] *= fsc;
      }
      if (first_next && g >= 0) {
        const float inv = 1.0f / swap32_add(lB);
#pragma unroll
        for (int d = 0; d < 4; ++d) {
          float y[16];
#pragma unroll
          for (int i = 0; i < 16; ++i) y[i] = o[d][i] * inv;
          store_ctx(ws, y, bi * 8 + 2 * rg, h, d, r, hi);
          o[d] = f32x16{};
        }
      }
      if (grp == 1 && g == 14) {
        const unsigned src = (16u << 20) + (unsigned)(h * 524288 + (P1 + 2 * (16 - P1) + 1) * 16384 + rg * 4096), dst = lds0 + (unsigned)(L_KS2 + 16384 + rg * 4096);
        dma2<0>(sK, vL, src, dst); dma2<2048>(sK, vL, src, dst);
      }
    }
  }
  if (grp == 0) asm volatile("s_waitcnt vmcnt(0) lgkmcnt(0)\n\ts_barrier" ::: "memory");
  STAMP(30);
  {
    const lds_cptr vp = (lds_cptr)lds + (lane * 16 + (grp ? L_KS2 + 16384 : L_V));
#pragma unroll
    for (int i = 0; i < 16; ++i) l += S1[i];
    pk2 = pack8(S1, 0); pk3 = pack8(S1, 1);
#pragma unroll
    for (int d = 0; d < 4; ++d) { o[d] = MFMA(VF(d, 0), BFR(pk0), o[d]); o[d] = MFMA(VF(d, 1), BFR(pk1), o[d]); o[d] = MFMA(VF(d, 2), BFR(pk2), o[d]); o[d] = MFMA(VF(d, 3), BFR(pk3), o[d]); }
  }
  {
    l = swap32_add(l);
    char* mybuf = lds + grp * 32768 + rg * 8192;
    const char* pbuf = lds + (grp ^ 1) * 32768 + rg * 8192;
    float* ml = (float*)(lds + L_QA);
    if (grp) { f32x16 t0 = o[0], t1 = o[1]; o[0] = o[2]; o[1] = o[3]; o[2] = t0; o[3] = t1; }
    asm volatile("s_waitcnt lgkmcnt(0)\n\ts_barrier" ::: "memory");
#pragma unroll
    for (int t = 0; t < 2; ++t)
#pragma unroll
      for (int q = 0; q < 4; ++q) {
        f32x4 v = {o[2 + t][4 * q], o[2 + t][4 * q + 1], o[2 + t][4 * q + 2], o[2 + t][4 * q + 3]};
        *(f32x4*)(mybuf + ((t * 4 + q) * 64 + lane) * 16) = v;
      }
    if (hi == 0) { ml[wid * 64 + r] = m; ml[wid * 64 + 32 + r] = l; }
    asm volatile("s_waitcnt lgkmcnt(0)\n\ts_barrier" ::: "memory");
    const int pw = wid ^ 4;
    const float m2 = ml[pw * 64 + r], l2 = ml[pw * 64 + 32 + r];
    const float mm = fmaxf(m, m2);
    const float f1 = EX(m - mm), f2 = EX(m2 - mm);
    const float inv = 1.0f / (l * f1 + l2 * f2);
    const float g1 = f1 * inv, g2 = f2 * inv;
#pragma unroll
    for (int t = 0; t < 2; ++t) {
      float y[16];
#pragma unroll
      for (int q = 0; q < 4; ++q) {
        const f32x4 v = *(const f32x4*)(pbuf + ((t * 4 + q) * 64 + lane) * 16);
#pragma unroll
        for (int e = 0; e < 4; ++e) y[4 * q + e] = o[t][4 * q + e] * g1 + v[e] * g2;
      }
      store_ctx(ws, y, (15 - bi) * 8 + 2 * rg, h, 2 * grp + t, r, hi);
    }
  }
#undef KF
#undef VF
}

__global__ __launch_bounds__(512, 2) void k1_attn(const u16* __restrict__ wq, const u16* __restrict__ w1, char* __restrict__ ws) {
  extern __shared__ __attribute__((aligned(16))) char lds[];
  const int tid = threadIdx.x, lane = tid & 63, wid = __builtin_amdgcn_readfirstlane(tid >> 6);
  const int b = blockIdx.x, xcd = b & 7, jj = b >> 3, h = 4 * xcd + (jj >> 3), bi = jj & 7;
  const __amdgpu_buffer_rsrc_t sK = mk_srd(ws + WS_KF, 32u << 20), sV = mk_srd(ws + WS_VF, 16u << 20);
  unsigned long long* dbg = (unsigned long long*)(ws + WS_DBG);
  STAMP(0); RSTAMP(60);
  if (h < 22) phaseB<true>(dbg, lds, ws, wq, w1, sK, sV, h, bi, wid, lane);
  else        phaseB<false>(dbg, lds, ws, wq, w1, sK, sV, h, bi, wid, lane);
  phaseC(dbg, lds, ws, sK, h, bi, wid, lane);
  STAMP(32); RSTAMP(61);
}

typedef unsigned u32x2 __attribute__((ext_vector_type(2)));
__global__ __launch_bounds__(512, 2) void k2_out(const u16* __restrict__ x, const u16* __restrict__ wo, const u16* __restrict__ w2, u16* __restrict__ out, char* __restrict__ ws) {
  extern __shared__ __attribute__((aligned(16))) char lds[];
  const int tid = threadIdx.x, lane = tid & 63, wid = __builtin_amdgcn_readfirstlane(tid >> 6);
  const int b = blockIdx.x, xcd = b & 7, jj = b >> 3, role = xcd >> 1, mt = (xcd & 1) * 8 + (jj >> 2), nt = jj & 3;
  const int c = lane & 15, g = lane >> 4, kgrp = wid >> 2, wr = (wid >> 1) & 1, wc = wid & 1;
  const unsigned lds0 = (unsigned)(uintptr_t)lds;
  const bool ish = role == 3;
  const int s0 = ish ? 0 : role * 21, NS = (role >= 2) ? 22 : 21;
  const __amdgpu_buffer_rsrc_t sA = ish ? mk_srd(ws + WS_H, 128u * 44u * 1024u) : mk_srd(ws + WS_CTX, 16u << 20);
  const __amdgpu_buffer_rsrc_t sW = ish ? mk_srd(w2, 1376u * 512u * 2u) : mk_srd(wo, 4096u * 512u * 2u);
  const unsigned vL = (unsigned)lane * 16u;
  const unsigned aRow = (unsigned)((mt * 8 + wid) * (ish ? 44 : 128)) * 1024u;
  unsigned vW[2];
#pragma unroll
  for (int m = 0; m < 2; ++m) vW[m] = (unsigned)(((m * 4 + ((lane >> 2) & 3)) * 512 + (lane >> 4) * 32 + ((lane & 3) ^ (2 * m)) * 8) * 2);
  const unsigned wBase = (unsigned)((wid * 8 * 512 + nt * 128) * 2);
  auto stage = [&](int s) {
    const bool ok = s < NS;
    const unsigned dst = ok ? lds0 + (unsigned)((s & 3) * 32768) : lds0 + 0x38000u;
    const unsigned sa = ok ? aRow + (unsigned)(s0 + s) * 2048u : 0x7f000000u;
    const unsigned sw = ok ? wBase + (unsigned)(s0 + s) * 65536u : 0x7f000000u;
    dma2<0>(sA, vL, sa, dst + (unsigned)(wid * 2048));
    dma16(sW, vW[0], sw, dst + 16384u + (unsigned)(wid * 2048));
    dma16(sW, vW[1], sw, dst + 16384u + (unsigned)(wid * 2048 + 1024));
  };
  const int ab = (wr * 4) * 2048 + kgrp * 1024 + lane * 16;
  int wb[2][2];
  {
    const int kq0 = ish ? g : 4 * (g >> 1) + (g & 1), kst = ish ? 4 : 2;
#pragma unroll
    for (int par = 0; par < 2; ++par)
#pragma unroll
      for (int t = 0; t < 2; ++t) { const int kq = kgrp * 8 + kq0 + t * kst;
        wb[par][t] = 16384 + kq * 1024 + wc * 512 + ((lane >> 2) & 3) * 64 + ((par ^ (kq & 1)) * 32) + (lane & 3) * 8; }
  }
  f32x4 acc[4][4];
#pragma unroll
  for (int i = 0; i < 4; ++i)
#pragma unroll
    for (int j = 0; j < 4; ++j) acc[i][j] = f32x4{};
  stage(0); stage(1); stage(2);
#pragma unroll 1
  for (int s = 0; s < NS; ++s) {
    asm volatile("s_waitcnt vmcnt(8) lgkmcnt(0)\n\ts_barrier" ::: "memory");
    lds_cptr base = (lds_cptr)lds + (s & 3) * 32768;
    bf16x8 af[4], wf[4];
#pragma unroll
    for (int j = 0; j < 4; ++j) wf[j] = cat8(ldstr(base + wb[j & 1][0] + (j >> 1) * 256), ldstr(base + wb[j & 1][1] + (j >> 1) * 256));
#pragma unroll
    for (int i = 0; i < 4; ++i) af[i] = ldsr128(base + ab + i * 2048);
    SBAR();
    stage(s + 3);
    SBAR();
#pragma unroll
    for (int i = 0; i < 4; ++i)
#pragma unroll
      for (int j = 0; j < 4; ++j) acc[i][j] = MFMA16(wf[j], af[i], acc[i][j]);
  }
  asm volatile("s_waitcnt vmcnt(0) lgkmcnt(0)\n\ts_barrier" ::: "memory");
  {
    char* mybuf = lds + wid * 8192;
    const char* pbuf = lds + (wid ^ 4) * 8192;
    if (kgrp) {
#pragma unroll
      for (int j = 0; j < 4; ++j) { f32x4 t0 = acc[0][j], t1 = acc[1][j]; acc[0][j] = acc[2][j]; acc[1][j] = acc[3][j]; acc[2][j] = t0; acc[3][j] = t1; }
    }
#pragma unroll
    for (int i = 0; i < 2; ++i)
#pragma unroll
      for (int j = 0; j < 4; ++j) *(f32x4*)(mybuf + ((i * 4 + j) * 64 + lane) * 16) = acc[2 + i][j];
    asm volatile("s_waitcnt lgkmcnt(0)\n\ts_barrier" ::: "memory");
#pragma unroll
    for (int i = 0; i < 2; ++i)
#pragma unroll
      for (int j = 0; j < 4; ++j) acc[i][j] += *(const f32x4*)(pbuf + ((i * 4 + j) * 64 + lane) * 16);
  }
  const int qd = 2 * wr + kgrp, tile = mt * 4 + nt;
  const __amdgpu_buffer_rsrc_t sP = mk_srd(ws + WS_PART, 16u << 20);
  unsigned* cnt = (unsigned*)(ws + WS_FLAG) + tile * 64;
  if (qd != role) {
    const unsigned off = (unsigned)((((tile * 4 + qd) * 4 + role) * 2 + wc) * 8192) + vL;
#pragma unroll
    for (int i = 0; i < 2; ++i)
#pragma unroll
      for (int j = 0; j < 4; ++j) __builtin_amdgcn_raw_buffer_store_b128(__builtin_bit_cast(u32x4, acc[i][j]), sP, off + (unsigned)((i * 4 + j) * 1024), 0, 16);
  }
  asm volatile("s_waitcnt vmcnt(0)\n\ts_barrier" ::: "memory");
  if (tid == 0) __hip_atomic_fetch_add(cnt, 1u, __ATOMIC_RELAXED, __HIP_MEMORY_SCOPE_AGENT);
  if (qd == role) {
    u32x2 xv[2][4];
#pragma unroll
    for (int i = 0; i < 2; ++i)
#pragma unroll
      for (int j = 0; j < 4; ++j) xv[i][j] = *(const u32x2*)(x + (size_t)(mt * 128 + qd * 32 + i * 16 + c) * 512 + nt * 128 + wc * 64 + j * 16 + 4 * g);
    for (int spin = 0; spin < (1 << 22); ++spin) {
      const unsigned v = __hip_atomic_load(cnt, __ATOMIC_RELAXED, __HIP_MEMORY_SCOPE_AGENT);
      if (__builtin_amdgcn_readfirstlane(v) >= 4u) break;
      __builtin_amdgcn_s_sleep(1);
    }
    __builtin_amdgcn_fence(__ATOMIC_ACQUIRE, "wavefront");
    f32x4 sa[2][4], ya[2][4];
#pragma unroll
    for (int i = 0; i < 2; ++i)
#pragma unroll
      for (int j = 0; j < 4; ++j) { sa[i][j] = ish ? f32x4{} : acc[i][j]; ya[i][j] = acc[i][j]; }
#pragma unroll
    for (int rr = 1; rr < 4; ++rr) {
      const int src = (role + rr) & 3;
      const unsigned off = (unsigned)((((tile * 4 + role) * 4 + src) * 2 + wc) * 8192) + vL;
      f32x4 p[2][4];
#pragma unroll
      for (int i = 0; i < 2; ++i)
#pragma unroll
        for (int j = 0; j < 4; ++j) p[i][j] = __builtin_bit_cast(f32x4, __builtin_amdgcn_raw_buffer_load_b128(sP, off + (unsigned)((i * 4 + j) * 1024), 0, 16));
#pragma unroll
      for (int i = 0; i < 2; ++i)
#pragma unroll
        for (int j = 0; j < 4; ++j) { if (src == 3) ya[i][j] = p[i][j]; else sa[i][j] += p[i][j]; }
    }
#pragma unroll
    for (int i = 0; i < 2; ++i)
#pragma unroll
      for (int j = 0; j < 4; ++j) {
        float o4[4];
#pragma unroll
        for (int e = 0; e < 4; ++e) {
          const float y = rbf(ya[i][j][e]);
          const float gt = rbf(y / (1.f + __expf(-y)));
          const float sg = rbf(rbf(sa[i][j][e]) * gt);
          const float xe = (e & 1) ? __uint_as_float(xv[i][j][e >> 1] & 0xffff0000u) : __uint_as_float(xv[i][j][e >> 1] << 16);
          o4[e] = xe + sg;
        }
        u32x2 w = {cvtpk(o4[0], o4[1]), cvtpk(o4[2], o4[3])};
        *(u32x2*)(out + (size_t)(mt * 128 + qd * 32 + i * 16 + c) * 512 + nt * 128 + wc * 64 + j * 16 + 4 * g) = w;
      }
  }
}

extern "C" void kernel_launch(void* const* d_in, const int* in_sizes, int n_in, void* d_out, int out_size, void* d_ws, size_t ws_size, hipStream_t stream) {
  const u16* x = (const u16*)d_in[0]; const u16* ckv = (const u16*)d_in[1];
  const u16* wuk = (const u16*)d_in[3]; const u16* wuv = (const u16*)d_in[4];
  (void)hipFuncSetAttribute((const void*)k0_kvproj, hipFuncAttributeMaxDynamicSharedMemorySize, 131072);
  k0_kvproj<<<256, 512, 131072, stream>>>(x, ckv, wuk, wuv, (char*)d_ws);
  (void)hipFuncSetAttribute((const void*)k1_attn, hipFuncAttributeMaxDynamicSharedMemorySize, 163840);
  k1_attn<<<256, 512, 163840, stream>>>((const u16*)d_in[2], (const u16*)d_in[7], (char*)d_ws);
  (void)hipFuncSetAttribute((const void*)k2_out, hipFuncAttributeMaxDynamicSharedMemorySize, 131072);
  k2_out<<<256, 512, 131072, stream>>>(x, (const u16*)d_in[5], (const u16*)d_in[8], (u16*)d_out, (char*)d_ws);
}
```

```cpp
#include <hip/hip_runtime.h>
#include <hip/hip_bf16.h>
#include <stdint.h>

typedef unsigned short u16;
typedef short bf16x8 __attribute__((ext_vector_type(8)));
typedef short s16x4 __attribute__((ext_vector_type(4)));
typedef float f32x16 __attribute__((ext_vector_type(16)));
typedef float f32x4 __attribute__((ext_vector_type(4)));
typedef float f32x2 __attribute__((ext_vector_type(2)));
typedef __bf16 bf16x2_t __attribute__((ext_vector_type(2)));
typedef unsigned u32x4 __attribute__((ext_vector_type(4)));
#define LAS __attribute__((address_space(3)))
typedef LAS const char* lds_cptr;
#define DI __device__ __forceinline__
#define MFMA(a, b, c) __builtin_amdgcn_mfma_f32_32x32x16_bf16((a), (b), (c), 0, 0, 0)
#define SBAR() __builtin_amdgcn_sched_barrier(0)

constexpr size_t WS_KF = 0;
constexpr size_t WS_VF = 16u << 20;
constexpr size_t WS_CTX = 32u << 20;
constexpr size_t WS_XN = 48u << 20;
constexpr size_t WS_H = 50u << 20;
constexpr size_t WS_PART = 56u << 20;
constexpr size_t WS_FLAG = 80u << 20;

constexpr size_t WS_DBG = 96u << 20;
#define RSTAMP(i) do {} while (0)
#define STAMP(i) do {} while (0)
DI unsigned cvtpk(float lo, float hi) { f32x2 v = {lo, hi}; bf16x2_t b = __builtin_convertvector(v, bf16x2_t); return __builtin_bit_cast(unsigned, b); }
DI float bf2f(unsigned v16) { return __uint_as_float(v16 << 16); }
DI float rbf(float f) { return bf2f(cvtpk(f, f) & 0xffffu); }
DI u32x4 pack8(const f32x16& x, int s) {
  u32x4 p = {cvtpk(x[8 * s], x[8 * s + 1]), cvtpk(x[8 * s + 2], x[8 * s + 3]), cvtpk(x[8 * s + 4], x[8 * s + 5]), cvtpk(x[8 * s + 6], x[8 * s + 7])};
  return p;
}
DI __amdgpu_buffer_rsrc_t mk_srd(const void* p, unsigned bytes) { return __builtin_amdgcn_make_buffer_rsrc((void*)p, (short)0, (int)bytes, 0x00020000); }
DI void dma16(__amdgpu_buffer_rsrc_t srd, unsigned voff, unsigned soff, unsigned lds_dst) {
  unsigned keep;
  asm volatile("s_mov_b32 %0, m0\n\ts_mov_b32 m0, %1\n\ts_nop 0\n\tbuffer_load_dwordx4 %2, %3, %4 offen lds\n\ts_mov_b32 m0, %0"
               : "=&s"(keep) : "s"(lds_dst), "v"(voff), "s"(srd), "s"(soff) : "memory");
}
DI bf16x8 ldsr128(lds_cptr p) { return *(const LAS bf16x8*)p; }
typedef short v4i16_t __attribute__((ext_vector_type(4)));
DI s16x4 ldstr(lds_cptr p) { return __builtin_bit_cast(s16x4, __builtin_amdgcn_ds_read_tr16_b64_v4i16((LAS v4i16_t*)p)); }
DI bf16x8 cat8(s16x4 a, s16x4 b) { return __builtin_shufflevector(a, b, 0, 1, 2, 3, 4, 5, 6, 7); }
#define ILV() do { _Pragma("unroll") for (int i_ = 0; i_ < 8; ++i_) { __builtin_amdgcn_sched_group_barrier(0x008, 1, 0); __builtin_amdgcn_sched_group_barrier(0x100, 1, 0); } } while (0)
#define ILV2() do { _Pragma("unroll") for (int i_ = 0; i_ < 2; ++i_) { __builtin_amdgcn_sched_group_barrier(0x008, 1, 0); __builtin_amdgcn_sched_group_barrier(0x100, 1, 0); } } while (0)
#define ILVF() do { _Pragma("unroll") for (int i_ = 0; i_ < 4; ++i_) { __builtin_amdgcn_sched_group_barrier(0x008, 1, 0); __builtin_amdgcn_sched_group_barrier(0x100, 2, 0); } __builtin_amdgcn_sched_group_barrier(0x008, 4, 0); } while (0)
#define WAIT_BAR0() asm volatile("s_waitcnt vmcnt(0) lgkmcnt(0)\n\ts_barrier" ::: "memory")

template <int N> DI void wait_bar() { asm volatile("s_waitcnt vmcnt(%0) lgkmcnt(0)\n\ts_barrier" :: "i"(N) : "memory"); }
#define MFMA16(a, b, c) __builtin_amdgcn_mfma_f32_16x16x32_bf16((a), (b), (c), 0, 0, 0)
#define SGB(mask, n) __builtin_amdgcn_sched_group_barrier(mask, n, 0)
constexpr unsigned OOBS = 0x7f000000u, DROP = 0x38000u;
constexpr int F_X = 0, F_KV = 16, F_C = 48, F_P = 64, F_WORDS = 128 * 64;
DI int lane_id() { int l; asm volatile("v_mbcnt_lo_u32_b32 %0, -1, 0\n\tv_mbcnt_hi_u32_b32 %0, -1, %0" : "=v"(l)); return l; }
DI unsigned* flagp(char* ws, int i) { return (unsigned*)(ws + WS_FLAG) + i * 64; }
DI void st16_wt(__amdgpu_buffer_rsrc_t srd, unsigned off, u32x4 w) { __builtin_amdgcn_raw_buffer_store_b128(w, srd, off, 0, 16); }
DI void signal(unsigned* p) { __hip_atomic_fetch_add(p, 1u, __ATOMIC_RELAXED, __HIP_MEMORY_SCOPE_AGENT); }
DI void spin_ge(unsigned* p, unsigned want) {
  for (int spin = 0; spin < (1 << 22); ++spin) {
    const unsigned v = __hip_atomic_load(p, __ATOMIC_RELAXED, __HIP_MEMORY_SCOPE_AGENT);
    if (__builtin_amdgcn_readfirstlane(v) >= want) break;
    __builtin_amdgcn_s_sleep(1);
  }
}
constexpr int L_V = 0, L_QA = 65536, L_QB = 98304, L_KS2 = 98304, L_KS0 = 131072;
constexpr float QSCALE = 0.08838834764831845f * 1.4426950408889634f;
constexpr float THR = 8.0f;
#define PIN(x) asm volatile("" : "+v"(x))
DI float max3f(float a, float b, float c) { float r; asm("v_max3_f32 %0, %1, %2, %3" : "=v"(r) : "v"(a), "v"(b), "v"(c)); return r; }
DI float max2f(float a, float b) { float r; asm("v_max_f32_e32 %0, %1, %2" : "=v"(r) : "v"(a), "v"(b)); return r; }
DI float fadd_s(float a, float b) { float r; asm("v_add_f32_e32 %0, %1, %2" : "=v"(r) : "v"(a), "v"(b)); return r; }
DI float swap32_max(float m) { auto rr = __builtin_amdgcn_permlane32_swap(__float_as_uint(m), __float_as_uint(m), false, false); return fmaxf(__uint_as_float(rr[0]), __uint_as_float(rr[1])); }
DI float swap32_add(float m) { auto rr = __builtin_amdgcn_permlane32_swap(__float_as_uint(m), __float_as_uint(m), false, false); return __uint_as_float(rr[0]) + __uint_as_float(rr[1]); }

constexpr int PB_STAGE = 32768;
DI constexpr int pb_slot(int s) { return (s & 3) * PB_STAGE; }
constexpr int PA_BASE = 32768;
struct PBsrc { __amdgpu_buffer_rsrc_t sX, sQ, s1; unsigned vL, vQ, v1, sXbase, sQbase, s1base; };
DI void pb_lanes(PBsrc& p, int wid, int lane) {
  const int c4 = (lane & 3) ^ (2 * ((wid >> 1) & 1));
  p.vL = (unsigned)lane * 16u;
  p.vQ = (unsigned)((((lane >> 2) & 3) * 4096 + (lane >> 4) * 32 + c4 * 8) * 2);
  p.v1 = lane < 32 ? (unsigned)((((lane >> 2) & 3) * 1376 + (lane >> 4) * 32 + c4 * 8) * 2) : OOBS;
}
DI void pb_piece(const PBsrc& p, unsigned lds0, int wid, int s, int m) {
  const unsigned b = lds0 + (unsigned)pb_slot(s);
  if (m < 2) dma16(p.sX, p.vL, p.sXbase + (unsigned)(m * 16384 + s * 1024), b + (unsigned)((wid * 2 + m) * 1024));
  else if (m == 2) dma16(p.sQ, p.vQ, p.sQbase + (unsigned)(s * 32 * 4096 * 2), b + 16384u + (unsigned)(wid * 2048));
  else dma16(p.s1, p.v1, p.s1base + (unsigned)(s * 32 * 1376 * 2), b + 16384u + 1024u + (unsigned)(wid * 2048));
}
DI void dma16_nz(int wid, __amdgpu_buffer_rsrc_t srd, unsigned voff, unsigned soff, unsigned lds_dst) {
  asm volatile("s_cmp_eq_u32 %4, 0\n\ts_cbranch_scc1 1f\n\ts_mov_b32 m0, %0\n\ts_nop 0\n\tbuffer_load_dwordx4 %1, %2, %3 offen lds\n1:"
               :: "s"(lds_dst), "v"(voff), "s"(srd), "s"(soff), "s"(wid) : "memory", "scc");
}
template <int N0, int N> DI void wait_bar2(int wid) {
  asm volatile("s_cmp_eq_u32 %0, 0\n\ts_cbranch_scc1 1f\n\ts_waitcnt vmcnt(%2)\n\ts_branch 2f\n1:\n\ts_waitcnt vmcnt(%1)\n2:\n\ts_waitcnt lgkmcnt(0)\n\ts_barrier" :: "s"(wid), "i"(N0), "i"(N) : "memory", "scc");
}
template <bool DRAIN> DI void signal_w0(int wid, unsigned* p) {
  unsigned z = 0u, one = 1u;
  asm volatile("s_cmp_eq_u32 %0, 0\n\ts_cbranch_scc0 1f\n\t.if %4\n\ts_waitcnt vmcnt(0)\n\t.endif\n\ts_mov_b64 exec, 1\n\tglobal_atomic_add %2, %3, %1\n\ts_mov_b64 exec, -1\n1:"
               :: "s"(wid), "s"(p), "v"(z), "v"(one), "i"(DRAIN ? 1 : 0) : "memory", "scc");
}
template <bool INV> DI void check_w0(int wid, unsigned pv, const unsigned* p, unsigned want) {
  unsigned sv, cnt, z = 0u;
  asm volatile("s_cmp_eq_u32 %3, 0\n\ts_cbranch_scc0 9f\n\tv_readfirstlane_b32 %0, %2\n\ts_mov_b32 %1, 0x400000\n"
               "1:\n\ts_cmp_ge_u32 %0, %5\n\ts_cbranch_scc1 2f\n\ts_sub_u32 %1, %1, 1\n\ts_cmp_eq_u32 %1, 0\n\ts_cbranch_scc1 2f\n\ts_sleep 1\n\t"
               "global_load_dword %2, %6, %4 sc1\n\ts_waitcnt vmcnt(0)\n\tv_readfirstlane_b32 %0, %2\n\ts_branch 1b\n"
               "2:\n\t.if %7\n\tbuffer_inv sc1\n\t.endif\n9:"
               : "=&s"(sv), "=&s"(cnt), "+v"(pv) : "s"(wid), "s"(p), "s"(want), "v"(z), "i"(INV ? 1 : 0) : "memory", "scc");
}
DI unsigned poll_w0(int wid, const unsigned* p) { unsigned v = 0u, z = 0u;
  asm volatile("s_cmp_eq_u32 %1, 0\n\ts_cbranch_scc0 1f\n\tglobal_load_dword %0, %3, %2 sc1\n1:" : "+v"(v) : "s"(wid), "s"(p), "v"(z) : "memory", "scc"); return v; }
DI unsigned poll_issue(const unsigned* p, unsigned voff) { unsigned v; asm volatile("global_load_dword %0, %1, %2 sc1" : "=v"(v) : "v"(voff), "s"(p) : "memory"); return v; }

template <bool KT>
DI void phaseA(char* lds, char* ws, __amdgpu_buffer_rsrc_t sC, __amdgpu_buffer_rsrc_t sUK, __amdgpu_buffer_rsrc_t sUV, unsigned sCbase, unsigned sWbase,
               PBsrc pb, int h, int mt, int wid, int lane, u32x4 (&kv)[16]) {
  const int c = lane & 15, g = lane >> 4, km = wid & 1, nq = wid >> 1;
  const unsigned vC = (unsigned)((lane >> 2) * 1024 + (((lane & 3) ^ (3 * ((lane >> 5) & 1))) * 16));
  const unsigned vW = (unsigned)((((lane >> 2) & 3) * 4096 + (lane >> 4) * 32 + ((lane & 3) ^ (2 * ((wid >> 1) & 1))) * 8) * 2);
  const unsigned lds0 = (unsigned)(uintptr_t)lds;
  auto piece = [&](int s, int m) {
    if (s < 16) {
      const unsigned b = lds0 + (unsigned)(PA_BASE + (s & 3) * 32768);
      if (m < 2) dma16(sC, vC, sCbase + (unsigned)(m * 16 * 1024 + s * 64), b + (unsigned)((wid * 2 + m) * 1024));
      else       dma16((m & 1) ? sUV : sUK, vW, sWbase + (unsigned)((s * 32 * 4096) * 2), b + 16384u + (unsigned)((wid * 2 + (m & 1)) * 1024));
    } else pb_piece(pb, lds0, wid, s - 16, m);
  };
  const int cb = (128 * km + c) * 64 + ((g ^ (3 * ((c >> 3) & 1))) * 16);
  int wb[2];
#pragma unroll
  for (int par = 0; par < 2; ++par) wb[par] = 16384 + g * 4096 + nq * 512 + ((lane >> 2) & 3) * 64 + ((par ^ (g & 1)) * 32) + (lane & 3) * 8;
  f32x4 acc[8][4];
#pragma unroll
  for (int i = 0; i < 8; ++i)
#pragma unroll
    for (int j = 0; j < 4; ++j) acc[i][j] = f32x4{};
  bf16x8 cf[2][4], wf[2][4];
  auto rdW = [&](int s, int buf) {
    lds_cptr base = (lds_cptr)lds + (PA_BASE + (s & 3) * 32768);
#pragma unroll
    for (int j = 0; j < 4; ++j) wf[buf][j] = cat8(ldstr(base + wb[j & 1] + (j >> 1) * 256), ldstr(base + wb[j & 1] + 2048 + (j >> 1) * 256));
  };
  auto rdC = [&](int s, int half, int buf) {
    lds_cptr base = (lds_cptr)lds + (PA_BASE + (s & 3) * 32768);
#pragma unroll
    for (int i = 0; i < 4; ++i) cf[buf][i] = ldsr128(base + cb + (half * 4 + i) * 1024);
  };
  auto mm1 = [&](int wbuf, int cbuf, int half, int i) {
#pragma unroll
    for (int j = 0; j < 4; ++j) acc[half * 4 + i][j] = KT ? MFMA16(wf[wbuf][j], cf[cbuf][i], acc[half * 4 + i][j]) : MFMA16(cf[cbuf][i], wf[wbuf][j], acc[half * 4 + i][j]);
  };
  auto pin4 = [&](int half) {
#pragma unroll
    for (int i = 0; i < 4; ++i) asm volatile("" : "+v"(acc[half * 4 + i][0]), "+v"(acc[half * 4 + i][1]), "+v"(acc[half * 4 + i][2]), "+v"(acc[half * 4 + i][3]));
  };
  unsigned pv = 0, pv2 = 0;
  const int bi = mt;
  wait_bar<12>();
  rdW(0, 0); rdC(0, 0, 0);
#pragma unroll
  for (int s = 0; s < 16; ++s) {
    const int wbuf = s & 1;
    rdC(s, 1, 1);
    mm1(wbuf, 0, 0, 0); mm1(wbuf, 0, 0, 1); mm1(wbuf, 0, 0, 2); mm1(wbuf, 0, 0, 3);
    SGB(0x008, 2); SGB(0x100, 1); SGB(0x008, 2); SGB(0x100, 1); SGB(0x008, 2); SGB(0x100, 1); SGB(0x008, 2); SGB(0x100, 1); SGB(0x008, 8); SBAR(); pin4(0);
    if (KT && s == 10) { check_w0<false>(wid, pv, flagp(ws, F_X + bi), 16u); check_w0<true>(wid, pv2, flagp(ws, F_X + 15 - bi), 16u); }
    if (s < 15) {
      wait_bar<8>();
      SBAR();
      if (s == 12) pb_lanes(pb, wid, lane_id());
      if (KT && s == 4) signal_w0<false>(wid, flagp(ws, F_X + (blockIdx.x >> 4)));
      if (KT && s == 6) { pv = poll_w0(wid, flagp(ws, F_X + bi)); pv2 = poll_w0(wid, flagp(ws, F_X + 15 - bi)); }
      rdW(s + 1, wbuf ^ 1); rdC(s + 1, 0, 0);
      mm1(wbuf, 1, 1, 0); SGB(0x008, 1); SGB(0x100, 3); SGB(0x008, 1); SGB(0x100, 3); SGB(0x008, 1); SGB(0x100, 3); SGB(0x008, 1); SGB(0x100, 3); SBAR(); piece(s + 4, 0); SBAR();
      mm1(wbuf, 1, 1, 1); SBAR(); piece(s + 4, 1); SBAR();
      mm1(wbuf, 1, 1, 2); SBAR(); piece(s + 4, 2); SBAR();
      mm1(wbuf, 1, 1, 3); SBAR(); pin4(1); piece(s + 4, 3); SBAR();
    } else {
      mm1(wbuf, 1, 1, 0); SBAR(); piece(19, 0); SBAR(); mm1(wbuf, 1, 1, 1); SBAR(); piece(19, 1); SBAR(); mm1(wbuf, 1, 1, 2); SBAR(); piece(19, 2); SBAR(); mm1(wbuf, 1, 1, 3); SBAR(); piece(19, 3); SBAR();
    }
  }
  if (KT) {
#pragma unroll
    for (int i = 0; i < 8; ++i)
#pragma unroll
      for (int dp = 0; dp < 2; ++dp)
        kv[i * 2 + dp] = (u32x4){cvtpk(acc[i][2 * dp][0], acc[i][2 * dp][1]), cvtpk(acc[i][2 * dp][2], acc[i][2 * dp][3]), cvtpk(acc[i][2 * dp + 1][0], acc[i][2 * dp + 1][1]), cvtpk(acc[i][2 * dp + 1][2], acc[i][2 * dp + 1][3])};
  } else {
#pragma unroll
    for (int ip = 0; ip < 4; ++ip)
#pragma unroll
      for (int j = 0; j < 4; ++j) {
        unsigned a0 = cvtpk(acc[2 * ip][j][0], acc[2 * ip][j][1]), a1 = cvtpk(acc[2 * ip][j][2], acc[2 * ip][j][3]);
        unsigned b0 = cvtpk(acc[2 * ip + 1][j][0], acc[2 * ip + 1][j][1]), b1 = cvtpk(acc[2 * ip + 1][j][2], acc[2 * ip + 1][j][3]);
        auto r0 = __builtin_amdgcn_permlane32_swap(a0, b0, false, false);
        auto r1 = __builtin_amdgcn_permlane32_swap(a1, b1, false, false);
        kv[ip * 4 + j] = (u32x4){r0[0], r1[0], r0[1], r1[1]};
      }
  }
}

template <bool FFN>
DI void phaseB(char* lds, char* ws, PBsrc pb, __amdgpu_buffer_rsrc_t sK, int h, int bi, int wid, int lane, const u32x4 (&kv)[16]) {
  pb_lanes(pb, wid, lane);
  const int c = lane & 15, g = lane >> 4, rgp = wid >> 1, ch = wid & 1;
  const unsigned lds0 = (unsigned)(uintptr_t)lds;
  constexpr int NP = 4;
  auto kvpiece = [&](unsigned base, int tile, int ldsoff, int m) {
    dma16(sK, pb.vL, base + (unsigned)(h * 524288 + tile * 16384 + (wid * 2 + m) * 1024), lds0 + (unsigned)(ldsoff + (wid * 2 + m) * 1024));
  };
  const int v0slot = L_V + ((0 - (2 * bi + 2)) & 3) * 16384;
  constexpr int NJ = FFN ? 6 : 4;
  const int xb = rgp * 4096 + lane * 16;
  int wb[2];
#pragma unroll
  for (int par = 0; par < 2; ++par) wb[par] = 16384 + g * 4096 + ((lane >> 2) & 3) * 64 + ((par ^ (g & 1)) * 32) + (lane & 3) * 8;
  f32x4 acc[4][NJ];
#pragma unroll
  for (int i = 0; i < 4; ++i)
#pragma unroll
    for (int j = 0; j < NJ; ++j) acc[i][j] = f32x4{};
  bf16x8 xf[2][2], wf[2][NJ];
  auto rdW = [&](int s, int buf) {
    lds_cptr base = (lds_cptr)lds + pb_slot(s);
#pragma unroll
    for (int j = 0; j < NJ; ++j) { const int u = (j < 4) ? (2 * ch + (j >> 1)) : (4 + ch);
      wf[buf][j] = cat8(ldstr(base + wb[j & 1] + u * 256), ldstr(base + wb[j & 1] + 2048 + u * 256)); }
  };
  auto rdX = [&](int s, int half, int buf) {
    lds_cptr base = (lds_cptr)lds + pb_slot(s);
#pragma unroll
    for (int i = 0; i < 2; ++i) xf[buf][i] = ldsr128(base + xb + (half * 2 + i) * 1024);
  };
  auto mm1 = [&](int wbuf, int xbuf, int half, int i) {
#pragma unroll
    for (int j = 0; j < NJ; ++j) acc[half * 2 + i][j] = MFMA16(wf[wbuf][j], xf[xbuf][i], acc[half * 2 + i][j]);
  };
  auto pin1 = [&](int i) {
#pragma unroll
    for (int j = 0; j < NJ; j += 2) asm volatile("" : "+v"(acc[i][j]), "+v"(acc[i][j + 1]));
  };
  const int km = wid & 1, nq = wid >> 1;
  const bool kt = wid < 4;
  const unsigned kvV = (unsigned)((g >> 1) * 1024 + ((g & 1) * 32 + c) * 16);
  const unsigned kvS = kt ? (unsigned)(((h * 32 + bi * 4 + 2 * km) * 16 + 4 * nq) * 1024) : (16u << 20) + (unsigned)(((h * 32 + bi * 4 + 2 * km) * 16 + 8 * (nq - 2)) * 1024);
  auto kvstore = [&](int n) {
    const int i = n >> 1, dp = n & 1, ip = n >> 2, j = n & 3;
    const unsigned ok = (unsigned)(((i >> 2) * 16 + ((i >> 1) & 1) * 8 + 2 * dp) * 1024 + (i & 1) * 256);
    const unsigned ov = (unsigned)(((ip >> 1) * 16 + (j >> 1) * 4 + 2 * (ip & 1)) * 1024 + (j & 1) * 256);
    __builtin_amdgcn_raw_buffer_store_b128(kv[n], sK, kvV, kvS + (kt ? ok : ov), 16);
  };
  unsigned pv = 0;
  wait_bar<12>();
  rdW(0, 0); rdX(0, 0, 0);
#pragma unroll
  for (int s = 0; s < 16; ++s) {
    const int wbuf = s & 1;
    rdX(s, 1, 1);
    mm1(wbuf, 0, 0, 0); mm1(wbuf, 0, 0, 1);
    SGB(0x008, 1); SGB(0x100, 1); SGB(0x008, 1); SGB(0x100, 1); SGB(0x008, 2 * NJ - 2); SBAR(); pin1(0); pin1(1);
    if (s == 14) check_w0<true>(wid, pv, flagp(ws, F_KV + h), 8u);
    if (s < 15) {
      if (s == 0) wait_bar<8>(); else if (s == 1) wait_bar<10>(); else if (s <= 8) wait_bar<12>(); else if (s == 9) wait_bar<10>(); else if (s <= 12) wait_bar<8>();
      else if (s == 13) wait_bar2<0, 4>(wid); else wait_bar<0>();
      SBAR();
      if (s == 10) signal_w0<false>(wid, flagp(ws, F_KV + h));
      if (s == 12) pv = poll_w0(wid, flagp(ws, F_KV + h));
      rdW(s + 1, wbuf ^ 1); rdX(s + 1, 0, 0);
      mm1(wbuf, 1, 1, 0);
#pragma unroll
      for (int q = 0; q < NJ; ++q) { SGB(0x008, 1); SGB(0x100, 3); }
      SBAR(); pin1(2);
      if (s + 4 < 16) { pb_piece(pb, lds0, wid, s + 4, 0); pb_piece(pb, lds0, wid, s + 4, 1); SBAR(); }
      if (s <= 7) { kvstore(2 * s); SBAR(); }
      mm1(wbuf, 1, 1, 1); SBAR(); pin1(3);
      if (s + 4 < 16) { pb_piece(pb, lds0, wid, s + 4, 2); pb_piece(pb, lds0, wid, s + 4, 3); SBAR(); }
      if (s <= 7) { kvstore(2 * s + 1); SBAR(); }
    } else {
      wait_bar<0>();
      kvpiece(0u, 0, L_KS0, 0); kvpiece(0u, 0, L_KS0, 1); kvpiece(0u, 1, L_KS0 + 16384, 0); kvpiece(0u, 1, L_KS0 + 16384, 1);
      kvpiece(16u << 20, 0, v0slot, 0); kvpiece(16u << 20, 0, v0slot, 1);
      SBAR();
      mm1(wbuf, 1, 1, 0); mm1(wbuf, 1, 1, 1);
    }
  }
  char* qdst = lds + ((rgp < 2) ? L_QA : L_QB);
  const __amdgpu_buffer_rsrc_t sH = mk_srd(ws + WS_H, 128u * 44u * 1024u);
#pragma unroll
  for (int ii = 0; ii < 4; ++ii) {
    const int rb32 = 2 * (rgp & 1) + (ii >> 1), r = 16 * (ii & 1) + c;
#pragma unroll
    for (int dpp = 0; dpp < 2; ++dpp) {
      const int ks = 2 * (2 * ch + dpp) + (g >> 1);
      const f32x4 a = acc[ii][2 * dpp] * QSCALE, b = acc[ii][2 * dpp + 1] * QSCALE;
      u32x4 w = {cvtpk(a[0], a[1]), cvtpk(a[2], a[3]), cvtpk(b[0], b[1]), cvtpk(b[2], b[3])};
      *(u32x4*)(qdst + ((rb32 * 8 + ks) * 64 + (g & 1) * 32 + r) * 16) = w;
    }
    if (FFN) {
      const int rb16 = ((rgp < 2) ? (15 - bi) * 8 : bi * 8) + 4 * (rgp & 1) + ii;
      const bool valid = (h * 64 + ch * 32) < 1376;
      f32x4 a = acc[ii][NJ - 2], b = acc[ii][NJ - 1];
#pragma unroll
      for (int e = 0; e < 4; ++e) { a[e] = valid ? fmaxf(a[e], 0.f) : 0.f; b[e] = valid ? fmaxf(b[e], 0.f) : 0.f; }
      u32x4 w = {cvtpk(a[0], a[1]), cvtpk(a[2], a[3]), cvtpk(b[0], b[1]), cvtpk(b[2], b[3])};
      st16_wt(sH, (unsigned)(((rb16 * 44 + 2 * h + ch) * 64 + lane) * 16), w);
    }
  }
}

DI void store_ctx(__amdgpu_buffer_rsrc_t sCtx, const float (&y)[16], int rowblk16, int h, int db, int r, int hi) {
#pragma unroll
  for (int sx = 0; sx < 2; ++sx) {
    u32x4 w = {cvtpk(y[8 * sx], y[8 * sx + 1]), cvtpk(y[8 * sx + 2], y[8 * sx + 3]), cvtpk(y[8 * sx + 4], y[8 * sx + 5]), cvtpk(y[8 * sx + 6], y[8 * sx + 7])};
    st16_wt(sCtx, (unsigned)((((rowblk16 + (r >> 4)) * 128 + h * 4 + db) * 64 + (2 * sx + hi) * 16 + (r & 15)) * 16), w);
  }
}

template <int OFF> DI void dma2(__amdgpu_buffer_rsrc_t srd, unsigned voff, unsigned soff, unsigned lds_dst) {
  asm volatile("s_mov_b32 m0, %0\n\ts_nop 0\n\tbuffer_load_dwordx4 %1, %2, %3 offen offset:%4 lds\n\tbuffer_load_dwordx4 %1, %2, %3 offen offset:%5 lds"
               :: "s"(lds_dst), "v"(voff), "s"(srd), "s"(soff), "i"(OFF), "i"(OFF + 1024) : "memory");
}
DI unsigned rdl(unsigned v, int l) { return (unsigned)__builtin_amdgcn_readlane((int)v, l); }

DI void phaseC(char* lds, char* ws, __amdgpu_buffer_rsrc_t sK, int h, int bi, int wid, int lane) {
  const __amdgpu_buffer_rsrc_t sCtx = mk_srd(ws + WS_CTX, 16u << 20);
  const int rg = wid & 3, grp = wid >> 2, r = lane & 31, hi = lane >> 5;
  const unsigned lds0 = (unsigned)(uintptr_t)lds;
  const int P1 = 2 * bi + 2;
  const unsigned vL = (unsigned)lane * 16u;
  unsigned tKs, tVs, tC;
  {
    const unsigned hbase = (unsigned)(h * 524288);
    auto tix = [&](int g) { return g < P1 ? g : P1 + 2 * (g - P1) + grp; };
    auto kslot = [&](int g) { return ((grp && g >= P1) ? L_KS2 : L_KS0) + (g & 1) * 16384; };
    auto vslot = [&](int g) { return g < P1 ? L_V + ((g - P1) & 3) * 16384 : ((grp && g == 16) ? L_KS2 + 16384 : L_V + (2 * grp + (g & 1)) * 16384); };
    const int k = lane, gk = k + 1;
    const bool ksh = gk < P1, kok = gk <= 16;
    const unsigned ko = ksh ? wid * 2048 : rg * 4096;
    tKs = kok ? (hbase + (unsigned)(tix(gk) * 16384) + ko) | (unsigned)(kslot(gk) >> 14) : (OOBS | 14u);
    const bool vsh = k < P1;
    const int gv = (vsh || !grp) ? k : k - 1;
    const bool vok = vsh || gv >= P1;
    const unsigned vo = vsh ? wid * 2048 : rg * 4096;
    tVs = vok ? ((16u << 20) + hbase + (unsigned)(tix(gv) * 16384) + vo) | (unsigned)(vslot(gv) >> 14) : (OOBS | 14u);
    const int g = lane - 1;
    const bool first = (g < 0) || (grp && g + 1 == P1);
    const bool mask = (g + 1 == 16) || (grp && (g + 1 == P1 - 2 || g + 1 == P1 - 1));
    const unsigned qsrc = (g < 0 && grp) ? (unsigned)L_QB : (unsigned)L_QA;
    tC = (unsigned)(kslot(g + 1) >> 14) | ((unsigned)((g < 0 ? L_KS0 : vslot(g)) >> 14) << 4) | ((unsigned)(kslot(g + 2) >> 14) << 8) | ((qsrc >> 14) << 12) |
         (first ? 1u << 16 : 0u) | (mask ? 1u << 17 : 0u) | ((kok && !ksh) ? 1u << 18 : 0u) | ((vok && !vsh) ? 1u << 19 : 0u) |
         ((unsigned)((((grp && g + 1 < P1) ? bi : 15 - bi) * 4 + rg - tix(g + 1) * 2) & 255) << 24);
  }
  const float rl = (float)(r - 4 * hi) + 0.5f;
  bf16x8 qf[8];
  f32x16 o[4], S1, T0, T1, negm;
  u32x4 pk0 = {}, pk1 = {}, pk2, pk3;
  float m = 0.f, l = 0.f, lB = 0.f, ma, mb;
  bf16x8 k0, k1, k2, k3, v0, v1, v2, v3;
#pragma unroll
  for (int d = 0; d < 4; ++d) o[d] = f32x16{};
  S1 = f32x16{};
#define KF(i) ldsr128(kp + (i) * 1024)
#define VF(db, ks) ldsr128(((db) < 2 ? vpl : vpl_hi) + (((db) & 1) * 4 + (ks)) * 1024)
#define EX(v) __builtin_amdgcn_exp2f(v)
#define BFR(x) __builtin_bit_cast(bf16x8, x)
#define ADD2(a0, a1) asm("v_add_f32_e32 %0, %0, %2\n\tv_add_f32_e32 %1, %1, %3" : "+v"(sa), "+v"(sb) : "v"(a0), "v"(a1))
#define MX2(X, i) asm("v_max3_f32 %0, %0, %2, %3\n\tv_max3_f32 %1, %1, %4, %5" : "+v"(ma), "+v"(mb) : "v"(X[i]), "v"(X[i + 1]), "v"(X[i + 2]), "v"(X[i + 3]))
  unsigned ksrc = 0x7f000000u, kdst = lds0 + 0x38000u, vsrc = 0x7f000000u, vdst = lds0 + 0x38000u, dfl = 0;
#define HOOK0() do { if (dfl & 1) dma2<0>(sK, vL, ksrc, kdst); SBAR(); } while (0)
#define HOOK1() do { if (dfl & 2) dma2<2048>(sK, vL, ksrc, kdst); SBAR(); } while (0)
#define HOOK2() do { if (dfl & 1) dma2<0>(sK, vL, vsrc, vdst); SBAR(); } while (0)
#define HOOK3() do { if (dfl & 4) dma2<2048>(sK, vL, vsrc, vdst); SBAR(); } while (0)
#define PLAN(k) do { const unsigned a_ = rdl(tKs, k), b_ = rdl(tVs, k); dfl = 1u | ((rdl(tC, k) >> 17) & 6u); ksrc = a_ & ~15u; vsrc = b_ & ~15u; \
    kdst = lds0 + (a_ & 15u) * 16384u + ((dfl & 2u) ? rg * 4096u : wid * 2048u); vdst = lds0 + (b_ & 15u) * 16384u + ((dfl & 4u) ? rg * 4096u : wid * 2048u); } while (0)
  #pragma unroll 1
  for (int g = -1; g < 16; ++g) {
    const unsigned tc = rdl(tC, g + 1);
    const lds_cptr kp = (lds_cptr)lds + (lane * 16 + (tc & 15u) * 16384u);
    const lds_cptr vp = (lds_cptr)lds + (lane * 16 + ((tc >> 4) & 15u) * 16384u);
    const lds_cptr vpl = vp + grp * 8192, vpl_hi = vp + (8192 - grp * 8192);
    const bool first_next = (tc >> 16) & 1u;
    dfl = 0;
    if (grp == 0 || g <= 0) {
      if (g < 0) asm volatile("s_waitcnt vmcnt(4) lgkmcnt(0)\n\ts_barrier" ::: "memory");
      else { asm volatile("s_waitcnt vmcnt(0) lgkmcnt(0)\n\ts_barrier" ::: "memory"); PLAN(g + 1); }
            k0 = KF(0); k1 = KF(1); k2 = KF(2); k3 = KF(3);
    }
    if (first_next) {
      const lds_cptr qp = (lds_cptr)lds + (lane * 16 + rg * 8192 + ((tc >> 12) & 15u) * 16384u);
#pragma unroll
      for (int ks = 0; ks < 8; ++ks) qf[ks] = ldsr128(qp + ks * 1024);
      negm = f32x16{};
    }
    PIN(negm);
    SBAR();
    {
      float sa = 0.f, sb = 0.f;
      bf16x8 k4, k5;
      T0 = MFMA(k0, qf[0], negm); k4 = KF(4); k5 = KF(5); ADD2(S1[0], S1[1]); pk2[0] = cvtpk(S1[0], S1[1]); PIN(pk2); SBAR();
      T0 = MFMA(k1, qf[1], T0); ADD2(S1[2], S1[3]); pk2[1] = cvtpk(S1[2], S1[3]); PIN(pk2); SBAR();
      T0 = MFMA(k2, qf[2], T0); k0 = KF(6); k1 = KF(7); ADD2(S1[4], S1[5]); pk2[2] = cvtpk(S1[4], S1[5]); PIN(pk2); SBAR();
      T0 = MFMA(k3, qf[3], T0); ADD2(S1[6], S1[7]); pk2[3] = cvtpk(S1[6], S1[7]); PIN(pk2); SBAR();
      HOOK0();
      T0 = MFMA(k4, qf[4], T0); k2 = KF(8); k3 = KF(9); ADD2(S1[8], S1[9]); pk3[0] = cvtpk(S1[8], S1[9]); PIN(pk3); SBAR();
      T0 = MFMA(k5, qf[5], T0); ADD2(S1[10], S1[11]); pk3[1] = cvtpk(S1[10], S1[11]); PIN(pk3); SBAR();
      T0 = MFMA(k0, qf[6], T0); k4 = KF(10); k5 = KF(11); ADD2(S1[12], S1[13]); pk3[2] = cvtpk(S1[12], S1[13]); PIN(pk3); SBAR();
      T0 = MFMA(k1, qf[7], T0); ADD2(S1[14], S1[15]); pk3[3] = cvtpk(S1[14], S1[15]); PIN(pk3); SBAR();
      HOOK1();
      l += sa + sb; PIN(pk2); PIN(pk3);
      T1 = MFMA(k2, qf[0], negm); k0 = KF(12); k1 = KF(13); SBAR();
      T1 = MFMA(k3, qf[1], T1); SBAR();
      T1 = MFMA(k4, qf[2], T1); k2 = KF(14); k3 = KF(15); SBAR();
      T1 = MFMA(k5, qf[3], T1); SBAR();
      HOOK2();
      ma = T0[0]; mb = T0[1];
      T1 = MFMA(k0, qf[4], T1); v0 = VF(0, 0); v1 = VF(1, 0); MX2(T0, 2); SBAR();
      T1 = MFMA(k1, qf[5], T1); MX2(T0, 6); SBAR();
      T1 = MFMA(k2, qf[6], T1); v2 = VF(2, 0); v3 = VF(3, 0); MX2(T0, 10); SBAR();
      T1 = MFMA(k3, qf[7], T1); ma = max3f(ma, T0[14], T0[15]); SBAR();
      HOOK3();
    }
    if (grp == 1 && g >= 0) {
      asm volatile("s_waitcnt vmcnt(0) lgkmcnt(0)\n\ts_barrier" ::: "memory");
      v0 = VF(0, 0); v1 = VF(1, 0); v2 = VF(2, 0); v3 = VF(3, 0);
      if (g < 15) PLAN(g + 2); else dfl = 0;
          } else dfl = 0;
    {
      const lds_cptr kp = (lds_cptr)lds + (lane * 16 + ((tc >> 8) & 15u) * 16384u);
      bf16x8 v4, v5;
      o[0] = MFMA(v0, BFR(pk0), o[0]); v4 = VF(0, 1); v5 = VF(1, 1); SBAR();
      o[1] = MFMA(v1, BFR(pk0), o[1]); SBAR();
      o[2] = MFMA(v2, BFR(pk0), o[2]); v0 = VF(2, 1); v1 = VF(3, 1); MX2(T1, 0); MX2(T1, 4); SBAR();
      o[3] = MFMA(v3, BFR(pk0), o[3]); MX2(T1, 8); MX2(T1, 12); SBAR();
      ma = max2f(ma, mb);
      bool resc = false; float fsc = 1.f;
      if (__builtin_expect(((tc >> 16) & 3u) != 0u || __any(ma > THR), 0)) {
        if ((tc >> 17) & 1u) {
          const float lfB = ((float)(((int)tc >> 24) * 32 + 64) + rl) * 1.0e30f;
#pragma unroll
          for (int i = 0; i < 16; ++i) { const int kk = (i & 3) + 8 * (i >> 2); float t0_, t1_;
            asm("v_add_f32_e32 %2, %5, %4\n\tv_add_f32_e32 %3, %6, %4\n\tv_min_f32_e32 %0, %0, %2\n\tv_min_f32_e32 %1, %1, %3"
                : "+v"(T0[i]), "+v"(T1[i]), "=&v"(t0_), "=&v"(t1_) : "v"(lfB), "i"(__builtin_bit_cast(int, -(float)(kk + 64) * 1.0e30f)), "i"(__builtin_bit_cast(int, -(float)(kk + 96) * 1.0e30f))); }
        }
        ma = T0[0]; mb = T0[1];
        MX2(T0, 2); MX2(T0, 6); MX2(T0, 10); ma = max3f(ma, T0[14], T0[15]); MX2(T1, 0); MX2(T1, 4); MX2(T1, 8); MX2(T1, 12);
        const float a = swap32_max(max2f(ma, mb));
        float dl;
        if (first_next) { dl = fmaxf(a, -1.0e29f); lB = l; l = 0.f; m = dl; }
        else { dl = fmaxf(a, 0.f); m += dl; fsc = EX(-dl); l *= fsc; resc = true; }
#pragma unroll
        for (int i = 0; i < 16; ++i) { T0[i] -= dl; T1[i] -= dl; negm[i] = -m; }
      }
      SBAR();
      float sa = 0.f, sb = 0.f;
#define EX4(X, i) do { X[i] = EX(X[i]); X[i + 1] = EX(X[i + 1]); X[i + 2] = EX(X[i + 2]); X[i + 3] = EX(X[i + 3]); PIN(X); } while (0)
#define EX2(X, i) do { X[i] = EX(X[i]); X[i + 1] = EX(X[i + 1]); PIN(X); } while (0)
      HOOK0();
      o[0] = MFMA(v4, BFR(pk1), o[0]); v2 = VF(0, 2); v3 = VF(1, 2); EX4(T0, 0); SBAR();
      o[1] = MFMA(v5, BFR(pk1), o[1]); EX4(T0, 4); SBAR();
      o[2] = MFMA(v0, BFR(pk1), o[2]); v4 = VF(2, 2); v5 = VF(3, 2); EX4(T0, 8); SBAR();
      o[3] = MFMA(v1, BFR(pk1), o[3]); EX4(T0, 12); SBAR();
      HOOK1();
      o[0] = MFMA(v2, BFR(pk2), o[0]); v0 = VF(0, 3); v1 = VF(1, 3); EX2(T1, 0); ADD2(T0[0], T0[1]); pk0[0] = cvtpk(T0[0], T0[1]); PIN(pk0); SBAR();
      o[1] = MFMA(v3, BFR(pk2), o[1]); EX2(T1, 2); ADD2(T0[2], T0[3]); pk0[1] = cvtpk(T0[2], T0[3]); PIN(pk0); SBAR();
      o[2] = MFMA(v4, BFR(pk2), o[2]); v2 = VF(2, 3); v3 = VF(3, 3); EX2(T1, 4); ADD2(T0[4], T0[5]); pk0[2] = cvtpk(T0[4], T0[5]); PIN(pk0); SBAR();
      o[3] = MFMA(v5, BFR(pk2), o[3]); EX2(T1, 6); ADD2(T0[6], T0[7]); pk0[3] = cvtpk(T0[6], T0[7]); PIN(pk0); SBAR();
      HOOK2();
      o[0] = MFMA(v0, BFR(pk3), o[0]); k0 = KF(0); k1 = KF(1); EX2(T1, 8); ADD2(T0[8], T0[9]); pk1[0] = cvtpk(T0[8], T0[9]); PIN(pk1); SBAR();
      o[1] = MFMA(v1, BFR(pk3), o[1]); EX2(T1, 10); ADD2(T0[10], T0[11]); pk1[1] = cvtpk(T0[10], T0[11]); PIN(pk1); SBAR();
      o[2] = MFMA(v2, BFR(pk3), o[2]); k2 = KF(2); k3 = KF(3); EX2(T1, 12); ADD2(T0[12], T0[13]); pk1[2] = cvtpk(T0[12], T0[13]); PIN(pk1); SBAR();
      o[3] = MFMA(v3, BFR(pk3), o[3]); EX2(T1, 14); ADD2(T0[14], T0[15]); pk1[3] = cvtpk(T0[14], T0[15]); PIN(pk1); SBAR();
      HOOK3();
      l += sa + sb;
      S1 = T1;
      if (__builtin_expect(resc, 0)) {
#pragma unroll
        for (int d = 0; d < 4; ++d)
#pragma unroll
          for (int i = 0; i < 16; ++i) o[d][i] *= fsc;
      }
      if (first_next && g >= 0) {
        const float inv = 1.0f / swap32_add(lB);
#pragma unroll
        for (int d = 0; d < 4; ++d) {
          float y[16];
#pragma unroll
          for (int i = 0; i < 16; ++i) y[i] = o[d][i] * inv;
          store_ctx(sCtx, y, bi * 8 + 2 * rg, h, d ^ 2, r, hi);
          o[d] = f32x16{};
        }
      }
      if (grp == 1 && g == 14) {
        const unsigned src = (16u << 20) + (unsigned)(h * 524288 + (P1 + 2 * (16 - P1) + 1) * 16384 + rg * 4096), dst = lds0 + (unsigned)(L_KS2 + 16384 + rg * 4096);
        dma2<0>(sK, vL, src, dst); dma2<2048>(sK, vL, src, dst);
      }
    }
  }
  if (grp == 0) asm volatile("s_waitcnt vmcnt(0) lgkmcnt(0)\n\ts_barrier" ::: "memory");
    {
    const lds_cptr vp = (lds_cptr)lds + (lane * 16 + (grp ? L_KS2 + 16384 : L_V));
    const lds_cptr vpl = vp + grp * 8192, vpl_hi = vp + (8192 - grp * 8192);
#pragma unroll
    for (int i = 0; i < 16; ++i) l += S1[i];
    pk2 = pack8(S1, 0); pk3 = pack8(S1, 1);
#pragma unroll
    for (int d = 0; d < 4; ++d) { o[d] = MFMA(VF(d, 0), BFR(pk0), o[d]); o[d] = MFMA(VF(d, 1), BFR(pk1), o[d]); o[d] = MFMA(VF(d, 2), BFR(pk2), o[d]); o[d] = MFMA(VF(d, 3), BFR(pk3), o[d]); }
  }
  {
    l = swap32_add(l);
    char* mybuf = lds + grp * 32768 + rg * 8192;
    const char* pbuf = lds + (grp ^ 1) * 32768 + rg * 8192;
    float* ml = (float*)(lds + L_QA);
    asm volatile("s_waitcnt lgkmcnt(0)\n\ts_barrier" ::: "memory");
#pragma unroll
    for (int t = 0; t < 2; ++t)
#pragma unroll
      for (int q = 0; q < 4; ++q) {
        f32x4 v = {o[2 + t][4 * q], o[2 + t][4 * q + 1], o[2 + t][4 * q + 2], o[2 + t][4 * q + 3]};
        *(f32x4*)(mybuf + ((t * 4 + q) * 64 + lane) * 16) = v;
      }
    if (hi == 0) { ml[wid * 64 + r] = m; ml[wid * 64 + 32 + r] = l; }
    asm volatile("s_waitcnt lgkmcnt(0)\n\ts_barrier" ::: "memory");
    const int pw = wid ^ 4;
    const float m2 = ml[pw * 64 + r], l2 = ml[pw * 64 + 32 + r];
    const float mm = fmaxf(m, m2);
    const float f1 = EX(m - mm), f2 = EX(m2 - mm);
    const float inv = 1.0f / (l * f1 + l2 * f2);
    const float g1 = f1 * inv, g2 = f2 * inv;
#pragma unroll
    for (int t = 0; t < 2; ++t) {
      float y[16];
#pragma unroll
      for (int q = 0; q < 4; ++q) {
        const f32x4 v = *(const f32x4*)(pbuf + ((t * 4 + q) * 64 + lane) * 16);
#pragma unroll
        for (int e = 0; e < 4; ++e) y[4 * q + e] = o[t][4 * q + e] * g1 + v[e] * g2;
      }
      store_ctx(sCtx, y, (15 - bi) * 8 + 2 * rg, h, 2 * grp + t, r, hi);
    }
  }
#undef KF
#undef VF
}

typedef unsigned u32x2 __attribute__((ext_vector_type(2)));
DI void phaseD(char* lds, char* ws, const u16* x, const u16* wo, const u16* w2, u16* out, int bi, int wid, int lane) {
  const int tid = wid * 64 + lane;
  const int b = blockIdx.x, xcd = b & 7, jj = b >> 3, role = xcd >> 1, mt = (xcd & 1) * 8 + (jj >> 2), nt = jj & 3;
  const int c = lane & 15, g = lane >> 4, kgrp = wid >> 2, wr = (wid >> 1) & 1, wc = wid & 1;
  const unsigned lds0 = (unsigned)(uintptr_t)lds;
  const bool ish = role == 3;
  const int s0 = ish ? 0 : role * 22;
  constexpr int NS = 22;
  const __amdgpu_buffer_rsrc_t sA = ish ? mk_srd(ws + WS_H, 128u * 44u * 1024u) : mk_srd(ws + WS_CTX, 16u << 20);
  const __amdgpu_buffer_rsrc_t sW = ish ? mk_srd(w2, 1376u * 512u * 2u) : mk_srd(wo, 4096u * 512u * 2u);
  const unsigned vL = (unsigned)lane * 16u;
  const unsigned aRow = (unsigned)((mt * 8 + wid) * (ish ? 44 : 128) + s0 * 2) * 1024u;
  unsigned vW[2];
#pragma unroll
  for (int m = 0; m < 2; ++m) vW[m] = (unsigned)(((m * 4 + ((lane >> 2) & 3)) * 512 + (lane >> 4) * 32 + ((lane & 3) ^ (2 * m)) * 8) * 2);
  const unsigned wBase = (unsigned)((wid * 8 * 512 + nt * 128) * 2) + (unsigned)s0 * 65536u;
  const unsigned dw = lds0 + (unsigned)(wid * 2048);
  auto pA = [&](int s, int m) { dma16(sA, vL, aRow + (unsigned)(s * 2048 + m * 1024), dw + (unsigned)((s % 5) * 32768 + m * 1024)); };
  auto pW = [&](int s, int m) { dma16(sW, vW[m], wBase + (unsigned)s * 65536u, dw + (unsigned)((s % 5) * 32768 + 16384 + m * 1024)); };
  asm volatile("s_waitcnt lgkmcnt(0)\n\ts_barrier" ::: "memory");
#pragma unroll
  for (int s = 0; s < 5; ++s) { pW(s, 0); pW(s, 1); }
  wait_bar<10>();
  if (tid == 0) { signal(flagp(ws, F_C + bi)); signal(flagp(ws, F_C + 15 - bi)); }
  if (wid == 0) spin_ge(flagp(ws, F_C + mt), 32u);
  asm volatile("s_barrier" ::: "memory");
#pragma unroll
  for (int s = 0; s < 5; ++s) { pA(s, 0); pA(s, 1); }
  const int ab = (wr * 4 + 2 * kgrp) * 2048 + kgrp * 1024 + lane * 16, abx = (wr * 4 + 2 - 2 * kgrp) * 2048 + kgrp * 1024 + lane * 16;
  int wb[2][2];
  {
    const int kq0 = ish ? g : 4 * (g >> 1) + (g & 1), kst = ish ? 4 : 2;
#pragma unroll
    for (int par = 0; par < 2; ++par)
#pragma unroll
      for (int t = 0; t < 2; ++t) { const int kq = kgrp * 8 + kq0 + t * kst;
        wb[par][t] = 16384 + kq * 1024 + wc * 512 + ((lane >> 2) & 3) * 64 + ((par ^ (kq & 1)) * 32) + (lane & 3) * 8; }
  }
  f32x4 acc[4][4];
#pragma unroll
  for (int i = 0; i < 4; ++i)
#pragma unroll
    for (int j = 0; j < 4; ++j) acc[i][j] = f32x4{};
  bf16x8 af[2][4], wf[2][4];
  wait_bar<8>();
  {
    lds_cptr base = (lds_cptr)lds;
#pragma unroll
    for (int j = 0; j < 4; ++j) wf[0][j] = cat8(ldstr(base + wb[j & 1][0] + (j >> 1) * 256), ldstr(base + wb[j & 1][1] + (j >> 1) * 256));
#pragma unroll
    for (int i = 0; i < 4; ++i) af[0][i] = ldsr128(base + ((i < 2) ? ab : abx) + (i & 1) * 2048);
  }
#pragma unroll
  for (int s = 0; s < NS; ++s) {
    const int hf = s & 1;
    const bool nx = s + 1 < NS, pf = s + 5 < NS;
    if (nx) {
      constexpr int dummy = 0; (void)dummy;
      const int yo = (s < 3) ? (6 - 2 * s) + 4 * s : 12;
      const int tail = NS - 2 - s;
      const int n = (tail < 3) ? 4 * tail : yo;
      if (n == 12) wait_bar<12>(); else if (n == 10) wait_bar<10>(); else if (n == 8) wait_bar<8>(); else if (n == 6) wait_bar<6>(); else if (n == 4) wait_bar<4>(); else wait_bar<0>();
    }
    SBAR();
    lds_cptr base = (lds_cptr)lds + ((s + 1) % 5) * 32768;
    s16x4 wl[4], wh[4];
#define K2M(i, j) acc[i][j] = MFMA16(wf[hf][j], af[hf][i], acc[i][j])
#define PIN4(i) asm volatile("" : "+v"(acc[i][0]), "+v"(acc[i][1]), "+v"(acc[i][2]), "+v"(acc[i][3]))
    K2M(0, 0); if (nx) wl[0] = ldstr(base + wb[0][0]); SBAR();
    K2M(0, 1); if (nx) wh[0] = ldstr(base + wb[0][1]); SBAR();
    K2M(0, 2); if (nx) af[hf ^ 1][0] = ldsr128(base + ab); SBAR();
    K2M(0, 3); if (nx) wl[1] = ldstr(base + wb[1][0]); SBAR();
    PIN4(0); if (pf) pA(s + 5, 0); SBAR();
    K2M(1, 0); if (nx) wh[1] = ldstr(base + wb[1][1]); SBAR();
    K2M(1, 1); if (nx) wl[2] = ldstr(base + wb[0][0] + 256); SBAR();
    K2M(1, 2); if (nx) wh[2] = ldstr(base + wb[0][1] + 256); SBAR();
    K2M(1, 3); if (nx) wl[3] = ldstr(base + wb[1][0] + 256); SBAR();
    PIN4(1); if (pf) pA(s + 5, 1); SBAR();
    K2M(2, 0); if (nx) wh[3] = ldstr(base + wb[1][1] + 256); SBAR();
    K2M(2, 1); if (nx) af[hf ^ 1][1] = ldsr128(base + ab + 2048); SBAR();
    K2M(2, 2); if (nx) af[hf ^ 1][2] = ldsr128(base + abx); SBAR();
    K2M(2, 3); if (nx) af[hf ^ 1][3] = ldsr128(base + abx + 2048); SBAR();
    PIN4(2); if (pf) pW(s + 5, 0); SBAR();
    K2M(3, 0); K2M(3, 1); SBAR();
    if (pf) pW(s + 5, 1); SBAR();
    K2M(3, 2); K2M(3, 3); SBAR(); PIN4(3);
#undef K2M
    if (nx) {
#pragma unroll
      for (int j = 0; j < 4; ++j) wf[hf ^ 1][j] = cat8(wl[j], wh[j]);
    }
  }
  asm volatile("s_waitcnt vmcnt(0) lgkmcnt(0)\n\ts_barrier" ::: "memory");
  {
    char* mybuf = lds + wid * 8192;
    const char* pbuf = lds + (wid ^ 4) * 8192;
#pragma unroll
    for (int i = 0; i < 2; ++i)
#pragma unroll
      for (int j = 0; j < 4; ++j) *(f32x4*)(mybuf + ((i * 4 + j) * 64 + lane) * 16) = acc[2 + i][j];
    asm volatile("s_waitcnt lgkmcnt(0)\n\ts_barrier" ::: "memory");
#pragma unroll
    for (int i = 0; i < 2; ++i)
#pragma unroll
      for (int j = 0; j < 4; ++j) acc[i][j] += *(const f32x4*)(pbuf + ((i * 4 + j) * 64 + lane) * 16);
  }
  const int qd = 2 * wr + kgrp, tile = mt * 4 + nt;
  const __amdgpu_buffer_rsrc_t sP = mk_srd(ws + WS_PART, 16u << 20);
  unsigned* cnt = flagp(ws, F_P + tile);
  f32x4 own[2];
#pragma unroll
  for (int j = 0; j < 4; ++j) {
    if (j != role) {
      const unsigned off = (unsigned)((((tile * 4 + j) * 4 + role) * 8 + wid) * 2048) + vL;
#pragma unroll
      for (int i = 0; i < 2; ++i) __builtin_amdgcn_raw_buffer_store_b128(__builtin_bit_cast(u32x4, acc[i][j]), sP, off + (unsigned)(i * 1024), 0, 16);
    } else { own[0] = acc[0][j]; own[1] = acc[1][j]; }
  }
  u32x2 xv[2];
  const size_t obase = (size_t)(mt * 128 + qd * 32 + c) * 512 + nt * 128 + wc * 64 + role * 16 + 4 * g;
#pragma unroll
  for (int i = 0; i < 2; ++i) xv[i] = *(const u32x2*)(x + obase + i * 16 * 512);
  asm volatile("s_waitcnt vmcnt(0)\n\ts_barrier" ::: "memory");
  if (tid == 0) signal(cnt);
  if (wid == 0) spin_ge(cnt, 4u);
  asm volatile("s_barrier" ::: "memory");
  f32x4 p[3][2];
#pragma unroll
  for (int rr = 0; rr < 3; ++rr) {
    const int src = (role + 1 + rr) & 3;
    const unsigned off = (unsigned)((((tile * 4 + role) * 4 + src) * 8 + wid) * 2048) + vL;
#pragma unroll
    for (int i = 0; i < 2; ++i) p[rr][i] = __builtin_bit_cast(f32x4, __builtin_amdgcn_raw_buffer_load_b128(sP, off + (unsigned)(i * 1024), 0, 16));
  }
#pragma unroll
  for (int i = 0; i < 2; ++i) {
    f32x4 ya = ish ? own[i] : (role == 2 ? p[0][i] : (role == 1 ? p[1][i] : p[2][i]));
    f32x4 sa = (own[i] + p[0][i]) + (p[1][i] + p[2][i]) - ya;
    {
      const f32x4 z = {};
      sa = (ish ? z : own[i]) + (role == 2 ? z : p[0][i]) + (role == 1 ? z : p[1][i]) + (role == 0 ? z : p[2][i]);
    }
    float o4[4];
#pragma unroll
    for (int e = 0; e < 4; ++e) {
      const float y = rbf(ya[e]);
      const float gt = rbf(y * __builtin_amdgcn_rcpf(1.f + __builtin_amdgcn_exp2f(-1.4426950408889634f * y)));
      const float sg = rbf(rbf(sa[e]) * gt);
      const float xe = (e & 1) ? __uint_as_float(xv[i][e >> 1] & 0xffff0000u) : __uint_as_float(xv[i][e >> 1] << 16);
      o4[e] = xe + sg;
    }
    u32x2 w = {cvtpk(o4[0], o4[1]), cvtpk(o4[2], o4[3])};
    *(u32x2*)(out + obase + i * 16 * 512) = w;
  }
}

__global__ __launch_bounds__(512, 2) void fused_block(const u16* __restrict__ x, const u16* __restrict__ ckv, const u16* __restrict__ wq, const u16* __restrict__ wuk, const u16* __restrict__ wuv,
                                                      const u16* __restrict__ wo, const u16* __restrict__ w1, const u16* __restrict__ w2, u16* __restrict__ out, char* __restrict__ ws) {
  extern __shared__ __attribute__((aligned(16))) char lds[];
  const int tid = threadIdx.x, lane = tid & 63, wid = __builtin_amdgcn_readfirstlane(tid >> 6);
  const int b = blockIdx.x, xcd = b & 7, jj = b >> 3, h = 4 * xcd + (jj >> 3), mt = jj & 7, bi = mt;
  const __amdgpu_buffer_rsrc_t sC = mk_srd(ckv, 2048u * 512u * 2u), sUK = mk_srd(wuk, 512u * 4096u * 2u), sUV = mk_srd(wuv, 512u * 4096u * 2u);
  const int R = b * 8 + wid;
  u32x4 xr;
  { const u16* xp = x + (size_t)R * 512 + lane * 8; asm volatile("global_load_dwordx4 %0, %1, off" : "=v"(xr) : "v"(xp) : "memory"); }
  const unsigned vC = (unsigned)((lane >> 2) * 1024 + (((lane & 3) ^ (3 * ((lane >> 5) & 1))) * 16));
  const unsigned vW = (unsigned)((((lane >> 2) & 3) * 4096 + (lane >> 4) * 32 + ((lane & 3) ^ (2 * ((wid >> 1) & 1))) * 8) * 2);
  const unsigned sCbase = (unsigned)((mt * 256 + wid * 32) * 1024);
  const unsigned sWbase = (unsigned)((wid * 4 * 4096 + h * 128) * 2);
  {
    const unsigned lds0 = (unsigned)(uintptr_t)lds;
#pragma unroll
    for (int s = 0; s < 4; ++s) {
      const unsigned bb = lds0 + (unsigned)(PA_BASE + s * 32768);
#pragma unroll
      for (int m = 0; m < 2; ++m) dma16(sC, vC, sCbase + (unsigned)(m * 16 * 1024 + s * 64), bb + (unsigned)((wid * 2 + m) * 1024));
#pragma unroll
      for (int m = 0; m < 2; ++m) dma16(m ? sUV : sUK, vW, sWbase + (unsigned)((s * 32 * 4096) * 2), bb + 16384u + (unsigned)((wid * 2 + m) * 1024));
    }
  }
  PBsrc pb;
  pb.sX = mk_srd(ws + WS_XN, 2048u * 512u * 2u); pb.sQ = mk_srd(wq, 512u * 4096u * 2u); pb.s1 = mk_srd(w1, 512u * 1376u * 2u);
  pb.vL = pb.vQ = pb.v1 = 0u;
  pb.sXbase = (unsigned)(((wid < 4) ? (15 - bi) * 8 : bi * 8 - 8) + 2 * wid) * 16384u;
  pb.sQbase = sWbase; pb.s1base = h < 22 ? (unsigned)((wid * 4 * 1376 + h * 64) * 2) : OOBS;
  {
    asm volatile("s_waitcnt vmcnt(16)" : "+v"(xr) :: "memory");
    float v[8];
#pragma unroll
    for (int e = 0; e < 4; ++e) { v[2 * e] = __uint_as_float(xr[e] << 16); v[2 * e + 1] = __uint_as_float(xr[e] & 0xffff0000u); }
    float s = 0;
#pragma unroll
    for (int e = 0; e < 8; ++e) s += v[e];
#pragma unroll
    for (int o = 32; o; o >>= 1) s += __shfl_xor(s, o);
    const float mean = s * (1.f / 512.f);
    float q = 0;
#pragma unroll
    for (int e = 0; e < 8; ++e) { const float d = v[e] - mean; q += d * d; }
#pragma unroll
    for (int o = 32; o; o >>= 1) q += __shfl_xor(q, o);
    const float mb = rbf(mean), vb = rbf(q * (1.f / 512.f));
    const float den = rbf(__builtin_sqrtf(rbf(vb + 1e-5f)));
    float o8[8];
#pragma unroll
    for (int e = 0; e < 8; ++e) o8[e] = rbf(v[e] - mb) / den;
    u32x4 w = {cvtpk(o8[0], o8[1]), cvtpk(o8[2], o8[3]), cvtpk(o8[4], o8[5]), cvtpk(o8[6], o8[7])};
    st16_wt(pb.sX, (unsigned)((((R >> 4) * 16 + (lane >> 2)) * 64 + ((lane & 3) * 16 + (R & 15))) * 16), w);
  }
  const __amdgpu_buffer_rsrc_t sK = mk_srd(ws + WS_KF, 32u << 20);
  u32x4 kv[16];
  if (wid < 4) phaseA<true>(lds, ws, sC, sUK, sUV, sCbase, sWbase, pb, h, mt, wid, lane_id(), kv);
  else         phaseA<false>(lds, ws, sC, sUK, sUV, sCbase, sWbase, pb, h, mt, wid, lane_id(), kv);
  if (h < 22) phaseB<true>(lds, ws, pb, sK, h, bi, wid, lane_id(), kv);
  else        phaseB<false>(lds, ws, pb, sK, h, bi, wid, lane_id(), kv);
  phaseC(lds, ws, sK, h, bi, wid, lane_id());
  phaseD(lds, ws, x, wo, w2, out, bi, wid, lane_id());
}

extern "C" void kernel_launch(void* const* d_in, const int* in_sizes, int n_in, void* d_out, int out_size, void* d_ws, size_t ws_size, hipStream_t stream) {
  (void)in_sizes; (void)n_in; (void)out_size; (void)ws_size;
  (void)hipMemsetAsync((char*)d_ws + WS_FLAG, 0, F_WORDS * 4, stream);
  (void)hipFuncSetAttribute((const void*)fused_block, hipFuncAttributeMaxDynamicSharedMemorySize, 163840);
  fused_block<<<256, 512, 163840, stream>>>((const u16*)d_in[0], (const u16*)d_in[1], (const u16*)d_in[2], (const u16*)d_in[3], (const u16*)d_in[4], (const u16*)d_in[5],
                                            (const u16*)d_in[7], (const u16*)d_in[8], (u16*)d_out, (char*)d_ws);
}
```

```cpp
#include <hip/hip_runtime.h>
#include <hip/hip_bf16.h>
#include <stdint.h>

typedef unsigned short u16;
typedef short bf16x8 __attribute__((ext_vector_type(8)));
typedef short s16x4 __attribute__((ext_vector_type(4)));
typedef float f32x16 __attribute__((ext_vector_type(16)));
typedef float f32x4 __attribute__((ext_vector_type(4)));
typedef float f32x2 __attribute__((ext_vector_type(2)));
typedef __bf16 bf16x2_t __attribute__((ext_vector_type(2)));
typedef unsigned u32x4 __attribute__((ext_vector_type(4)));
#define LAS __attribute__((address_space(3)))
typedef LAS const char* lds_cptr;
#define DI __device__ __forceinline__
#define MFMA(a, b, c) __builtin_amdgcn_mfma_f32_32x32x16_bf16((a), (b), (c), 0, 0, 0)
#define SBAR() __builtin_amdgcn_sched_barrier(0)

constexpr size_t WS_KF = 0;
constexpr size_t WS_VF = 16u << 20;
constexpr size_t WS_CTX = 32u << 20;
constexpr size_t WS_XN = 48u << 20;
constexpr size_t WS_H = 50u << 20;
constexpr size_t WS_PART = 56u << 20;
constexpr size_t WS_FLAG = 80u << 20;

constexpr size_t WS_DBG = 96u << 20;
#define RSTAMP(i) do {} while (0)
#define STAMP(i) do {} while (0)
DI unsigned cvtpk(float lo, float hi) { f32x2 v = {lo, hi}; bf16x2_t b = __builtin_convertvector(v, bf16x2_t); return __builtin_bit_cast(unsigned, b); }
DI float bf2f(unsigned v16) { return __uint_as_float(v16 << 16); }
DI float rbf(float f) { return bf2f(cvtpk(f, f) & 0xffffu); }
DI u32x4 pack8(const f32x16& x, int s) {
  u32x4 p = {cvtpk(x[8 * s], x[8 * s + 1]), cvtpk(x[8 * s + 2], x[8 * s + 3]), cvtpk(x[8 * s + 4], x[8 * s + 5]), cvtpk(x[8 * s + 6], x[8 * s + 7])};
  return p;
}
DI __amdgpu_buffer_rsrc_t mk_srd(const void* p, unsigned bytes) { return __builtin_amdgcn_make_buffer_rsrc((void*)p, (short)0, (int)bytes, 0x00020000); }
DI void dma16(__amdgpu_buffer_rsrc_t srd, unsigned voff, unsigned soff, unsigned lds_dst) {
  unsigned keep;
  asm volatile("s_mov_b32 %0, m0\n\ts_mov_b32 m0, %1\n\ts_nop 0\n\tbuffer_load_dwordx4 %2, %3, %4 offen lds\n\ts_mov_b32 m0, %0"
               : "=&s"(keep) : "s"(lds_dst), "v"(voff), "s"(srd), "s"(soff) : "memory");
}
DI bf16x8 ldsr128(lds_cptr p) { return *(const LAS bf16x8*)p; }
typedef short v4i16_t __attribute__((ext_vector_type(4)));
DI s16x4 ldstr(lds_cptr p) { return __builtin_bit_cast(s16x4, __builtin_amdgcn_ds_read_tr16_b64_v4i16((LAS v4i16_t*)p)); }
DI bf16x8 cat8(s16x4 a, s16x4 b) { return __builtin_shufflevector(a, b, 0, 1, 2, 3, 4, 5, 6, 7); }
#define ILV() do { _Pragma("unroll") for (int i_ = 0; i_ < 8; ++i_) { __builtin_amdgcn_sched_group_barrier(0x008, 1, 0); __builtin_amdgcn_sched_group_barrier(0x100, 1, 0); } } while (0)
#define ILV2() do { _Pragma("unroll") for (int i_ = 0; i_ < 2; ++i_) { __builtin_amdgcn_sched_group_barrier(0x008, 1, 0); __builtin_amdgcn_sched_group_barrier(0x100, 1, 0); } } while (0)
#define ILVF() do { _Pragma("unroll") for (int i_ = 0; i_ < 4; ++i_) { __builtin_amdgcn_sched_group_barrier(0x008, 1, 0); __builtin_amdgcn_sched_group_barrier(0x100, 2, 0); } __builtin_amdgcn_sched_group_barrier(0x008, 4, 0); } while (0)
#define WAIT_BAR0() asm volatile("s_waitcnt vmcnt(0) lgkmcnt(0)\n\ts_barrier" ::: "memory")

template <int N> DI void wait_bar() { asm volatile("s_waitcnt vmcnt(%0) lgkmcnt(0)\n\ts_barrier" :: "i"(N) : "memory"); }
#define MFMA16(a, b, c) __builtin_amdgcn_mfma_f32_16x16x32_bf16((a), (b), (c), 0, 0, 0)
#define SGB(mask, n) __builtin_amdgcn_sched_group_barrier(mask, n, 0)
constexpr unsigned OOBS = 0x7f000000u, DROP = 0x38000u;
typedef unsigned long long u64;
extern "C" __device__ u64 hw_dispatch_id() __asm("llvm.amdgcn.dispatch.id");
constexpr int F_X = 0, F_KV = 256, F_C = 512, F_P = 1024;
DI int lane_id() { int l; asm volatile("v_mbcnt_lo_u32_b32 %0, -1, 0\n\tv_mbcnt_hi_u32_b32 %0, -1, %0" : "=v"(l)); return l; }
DI u64* flagp(char* ws, int i) { return (u64*)(ws + WS_FLAG) + i; }
DI void st16_wt(__amdgpu_buffer_rsrc_t srd, unsigned off, u32x4 w) { __builtin_amdgcn_raw_buffer_store_b128(w, srd, off, 0, 16); }
constexpr int L_V = 0, L_QA = 65536, L_QB = 98304, L_KS2 = 98304, L_KS0 = 131072;
constexpr float QSCALE = 0.08838834764831845f * 1.4426950408889634f;
constexpr float THR = 8.0f;
#define PIN(x) asm volatile("" : "+v"(x))
DI float max3f(float a, float b, float c) { float r; asm("v_max3_f32 %0, %1, %2, %3" : "=v"(r) : "v"(a), "v"(b), "v"(c)); return r; }
DI float max2f(float a, float b) { float r; asm("v_max_f32_e32 %0, %1, %2" : "=v"(r) : "v"(a), "v"(b)); return r; }
DI float fadd_s(float a, float b) { float r; asm("v_add_f32_e32 %0, %1, %2" : "=v"(r) : "v"(a), "v"(b)); return r; }
DI float swap32_max(float m) { auto rr = __builtin_amdgcn_permlane32_swap(__float_as_uint(m), __float_as_uint(m), false, false); return fmaxf(__uint_as_float(rr[0]), __uint_as_float(rr[1])); }
DI float swap32_add(float m) { auto rr = __builtin_amdgcn_permlane32_swap(__float_as_uint(m), __float_as_uint(m), false, false); return __uint_as_float(rr[0]) + __uint_as_float(rr[1]); }

constexpr int PB_STAGE = 32768;
DI constexpr int pb_slot(int s) { return (s & 3) * PB_STAGE; }
constexpr int PA_BASE = 32768;
struct PBsrc { __amdgpu_buffer_rsrc_t sX, sQ, s1; unsigned vL, vQ, v1, sXbase, sQbase, s1base; };
DI void pb_lanes(PBsrc& p, int wid, int lane) {
  const int c4 = (lane & 3) ^ (2 * ((wid >> 1) & 1));
  p.vL = (unsigned)lane * 16u;
  p.vQ = (unsigned)((((lane >> 2) & 3) * 4096 + (lane >> 4) * 32 + c4 * 8) * 2);
  p.v1 = lane < 32 ? (unsigned)((((lane >> 2) & 3) * 1376 + (lane >> 4) * 32 + c4 * 8) * 2) : OOBS;
}
DI void pb_piece(const PBsrc& p, unsigned lds0, int wid, int s, int m) {
  const unsigned b = lds0 + (unsigned)pb_slot(s);
  if (m < 2) dma16(p.sX, p.vL, p.sXbase + (unsigned)(m * 16384 + s * 1024), b + (unsigned)((wid * 2 + m) * 1024));
  else if (m == 2) dma16(p.sQ, p.vQ, p.sQbase + (unsigned)(s * 32 * 4096 * 2), b + 16384u + (unsigned)(wid * 2048));
  else dma16(p.s1, p.v1, p.s1base + (unsigned)(s * 32 * 1376 * 2), b + 16384u + 1024u + (unsigned)(wid * 2048));
}
DI void dma16_nz(int wid, __amdgpu_buffer_rsrc_t srd, unsigned voff, unsigned soff, unsigned lds_dst) {
  asm volatile("s_cmp_eq_u32 %4, 0\n\ts_cbranch_scc1 1f\n\ts_mov_b32 m0, %0\n\ts_nop 0\n\tbuffer_load_dwordx4 %1, %2, %3 offen lds\n1:"
               :: "s"(lds_dst), "v"(voff), "s"(srd), "s"(soff), "s"(wid) : "memory", "scc");
}
template <int N0, int N> DI void wait_bar2(int wid) {
  asm volatile("s_cmp_eq_u32 %0, 0\n\ts_cbranch_scc1 1f\n\ts_waitcnt vmcnt(%2)\n\ts_branch 2f\n1:\n\ts_waitcnt vmcnt(%1)\n2:\n\ts_waitcnt lgkmcnt(0)\n\ts_barrier" :: "s"(wid), "i"(N0), "i"(N) : "memory", "scc");
}
DI void signal_w0(int wid, unsigned en, u64* p, u64 tok) {
  unsigned z = 0u;
  asm volatile("s_cmp_eq_u32 %0, 0\n\ts_cbranch_scc0 1f\n\ts_cmp_eq_u32 %4, 0\n\ts_cbranch_scc1 1f\n\ts_mov_b64 exec, 1\n\tglobal_store_dwordx2 %2, %3, %1 sc1\n\ts_mov_b64 exec, -1\n1:"
               :: "s"(wid), "s"(p), "v"(z), "v"(tok), "s"(en) : "memory", "scc");
}
DI void poll_w0(int wid, const u64* p, unsigned voff) {
  asm volatile("s_cmp_eq_u32 %0, 0\n\ts_cbranch_scc0 1f\n\tglobal_load_dwordx2 v[252:253], %2, %1 sc1\n1:" :: "s"(wid), "s"(p), "v"(voff) : "memory", "scc", "v252", "v253"); }
template <bool INV, bool POLLED> DI void check_w0(int wid, const u64* p, unsigned voff, u64 tok, u64 need) {
  unsigned cnt; u64 m;
  asm volatile("s_cmp_eq_u32 %2, 0\n\ts_cbranch_scc0 9f\n\ts_mov_b32 %1, 0x400000\n\t.if %8 == 0\n\ts_branch 3f\n\t.endif\n"
               "1:\n\tv_cmp_ne_u64_e64 %0, v[252:253], %5\n\ts_and_b64 %0, %0, %6\n\ts_cbranch_scc0 2f\n\ts_sub_u32 %1, %1, 1\n\ts_cmp_eq_u32 %1, 0\n\ts_cbranch_scc1 2f\n\ts_sleep 1\n"
               "3:\n\tglobal_load_dwordx2 v[252:253], %4, %3 sc1\n\ts_waitcnt vmcnt(0)\n\ts_branch 1b\n"
               "2:\n\t.if %7\n\tbuffer_inv sc1\n\t.endif\n9:"
               : "=&s"(m), "=&s"(cnt) : "s"(wid), "s"(p), "v"(voff), "v"(tok), "s"(need), "i"(INV ? 1 : 0), "i"(POLLED ? 1 : 0) : "memory", "scc", "v252", "v253");
}
template <int NF, int NS_> DI void wait_barf(unsigned fast) {
  asm volatile("s_cmp_eq_u32 %0, 0\n\ts_cbranch_scc1 1f\n\ts_waitcnt vmcnt(%1)\n\ts_branch 2f\n1:\n\ts_waitcnt vmcnt(%2)\n2:\n\ts_waitcnt lgkmcnt(0)\n\ts_barrier" :: "s"(fast), "i"(NF), "i"(NS_) : "memory", "scc");
}
DI void st16_if(unsigned en, u32x4 w, __amdgpu_buffer_rsrc_t srd, unsigned voff, unsigned soff) {
  asm volatile("s_cmp_eq_u32 %4, 0\n\ts_cbranch_scc1 1f\n\tbuffer_store_dwordx4 %0, %1, %2, %3 offen sc1\n\ts_nop 1\n1:" :: "v"(w), "v"(voff), "s"(srd), "s"(soff), "s"(en) : "memory", "scc");
}
struct OwnPf { __amdgpu_buffer_rsrc_t srd; unsigned base, step, v, dst; int kind; };
DI void own_lanes(OwnPf& p, int lane) {
  const unsigned l16 = (unsigned)lane * 16u;
  p.v = p.kind == 0 ? (l16 >> 7) * 1024u + (l16 & 127u) : (l16 >> 8) * 8192u + (l16 & 255u);
}
DI void own_pf(const OwnPf& p, int t) {
  const unsigned off = p.kind == 0 ? p.base + (unsigned)(t >> 1) * 128u + (unsigned)(t & 1) * (1024u * 1024u) : p.base + (unsigned)t * p.step;
  dma16(p.srd, p.v, off, p.dst);
}

template <bool KT>
DI void phaseA(char* lds, char* ws, __amdgpu_buffer_rsrc_t sC, __amdgpu_buffer_rsrc_t sUK, __amdgpu_buffer_rsrc_t sUV, unsigned sCbase, unsigned sWbase,
               PBsrc pb, __amdgpu_buffer_rsrc_t sWo, __amdgpu_buffer_rsrc_t sW2, OwnPf pfo, u64 tok, int h, int mt, int wid, int lane, u32x4 (&kv)[16]) {
  own_lanes(pfo, lane);
  const int c = lane & 15, g = lane >> 4, km = wid & 1, nq = wid >> 1;
  const unsigned vC = (unsigned)((lane >> 2) * 1024 + (((lane & 3) ^ (3 * ((lane >> 5) & 1))) * 16));
  const unsigned vW = (unsigned)((((lane >> 2) & 3) * 4096 + (lane >> 4) * 32 + ((lane & 3) ^ (2 * ((wid >> 1) & 1))) * 8) * 2);
  const unsigned lds0 = (unsigned)(uintptr_t)lds;
  auto piece = [&](int s, int m) {
    if (s < 16) {
      const unsigned b = lds0 + (unsigned)(PA_BASE + (s & 3) * 32768);
      if (m < 2) dma16(sC, vC, sCbase + (unsigned)(m * 16 * 1024 + s * 64), b + (unsigned)((wid * 2 + m) * 1024));
      else       dma16((m & 1) ? sUV : sUK, vW, sWbase + (unsigned)((s * 32 * 4096) * 2), b + 16384u + (unsigned)((wid * 2 + (m & 1)) * 1024));
    } else pb_piece(pb, lds0, wid, s - 16, m);
  };
  const unsigned wv = (unsigned)(mt * 8 + wid), gw = (unsigned)blockIdx.x * 8u + (unsigned)wid;
  auto warm = [&](int t) {
    const unsigned dst = lds0 + (unsigned)(wid * 2048);
    const unsigned l16 = (unsigned)lane * 16u;
    if (t < 2) dma16(pb.sQ, (l16 >> 8) * 8192u + (l16 & 255u), (wv * 8u + (unsigned)t * 4u) * 8192u + (unsigned)(h * 256), dst);
    else if (t == 2) dma16(pb.s1, (l16 >> 7) * 2752u + (l16 & 127u), wv * 8u * 2752u + (unsigned)(h * 128), dst);
    else if (t < 5) dma16(sWo, l16, (gw * 2u + (unsigned)(t - 3)) * 1024u, dst);
    else dma16(sW2, l16, gw * 1024u, dst);
  };
  const int cb = (128 * km + c) * 64 + ((g ^ (3 * ((c >> 3) & 1))) * 16);
  int wb[2];
#pragma unroll
  for (int par = 0; par < 2; ++par) wb[par] = 16384 + g * 4096 + nq * 512 + ((lane >> 2) & 3) * 64 + ((par ^ (g & 1)) * 32) + (lane & 3) * 8;
  f32x4 acc[8][4];
#pragma unroll
  for (int i = 0; i < 8; ++i)
#pragma unroll
    for (int j = 0; j < 4; ++j) acc[i][j] = f32x4{};
  bf16x8 cf[2][4], wf[2][4];
  auto rdW = [&](int s, int buf) {
    lds_cptr base = (lds_cptr)lds + (PA_BASE + (s & 3) * 32768);
#pragma unroll
    for (int j = 0; j < 4; ++j) wf[buf][j] = cat8(ldstr(base + wb[j & 1] + (j >> 1) * 256), ldstr(base + wb[j & 1] + 2048 + (j >> 1) * 256));
  };
  auto rdC = [&](int s, int half, int buf) {
    lds_cptr base = (lds_cptr)lds + (PA_BASE + (s & 3) * 32768);
#pragma unroll
    for (int i = 0; i < 4; ++i) cf[buf][i] = ldsr128(base + cb + (half * 4 + i) * 1024);
  };
  auto mm1 = [&](int wbuf, int cbuf, int half, int i) {
#pragma unroll
    for (int j = 0; j < 4; ++j) acc[half * 4 + i][j] = KT ? MFMA16(wf[wbuf][j], cf[cbuf][i], acc[half * 4 + i][j]) : MFMA16(cf[cbuf][i], wf[wbuf][j], acc[half * 4 + i][j]);
  };
  auto pin4 = [&](int half) {
#pragma unroll
    for (int i = 0; i < 4; ++i) asm volatile("" : "+v"(acc[half * 4 + i][0]), "+v"(acc[half * 4 + i][1]), "+v"(acc[half * 4 + i][2]), "+v"(acc[half * 4 + i][3]));
  };
  const int bi = mt;
  wait_bar<16>();
  rdW(0, 0); rdC(0, 0, 0);
#pragma unroll
  for (int s = 0; s < 16; ++s) {
    const int wbuf = s & 1;
    rdC(s, 1, 1);
    mm1(wbuf, 0, 0, 0); mm1(wbuf, 0, 0, 1); mm1(wbuf, 0, 0, 2); mm1(wbuf, 0, 0, 3);
    SGB(0x008, 2); SGB(0x100, 1); SGB(0x008, 2); SGB(0x100, 1); SGB(0x008, 2); SGB(0x100, 1); SGB(0x008, 2); SGB(0x100, 1); SGB(0x008, 8); SBAR(); pin4(0);
    if (KT && s == 10) { const int l2 = lane_id(); check_w0<true, true>(wid, flagp(ws, F_X), (unsigned)((((l2 & 16) ? 15 - bi : bi) * 16 + (l2 & 15)) * 8), tok, ~0ull); }
    if (s < 15) {
      if (s == 0) wait_bar<12>(); else if (s == 1) wait_bar<13>(); else if (s == 2) wait_bar<14>(); else if (s <= 8) wait_bar<10>(); else if (s == 9) wait_bar<11>(); else if (s <= 11) wait_bar<12>(); else if (s == 12) wait_bar<10>(); else wait_bar<8>();
      SBAR();
      if (s == 12) pb_lanes(pb, wid, lane_id());
      if (KT && s == 4) signal_w0(wid, 1u, flagp(ws, F_X + blockIdx.x), tok);
      if (KT && s == 6) { const int l2 = lane_id(); poll_w0(wid, flagp(ws, F_X), (unsigned)((((l2 & 16) ? 15 - bi : bi) * 16 + (l2 & 15)) * 8)); }
      rdW(s + 1, wbuf ^ 1); rdC(s + 1, 0, 0);
      mm1(wbuf, 1, 1, 0); SGB(0x008, 1); SGB(0x100, 3); SGB(0x008, 1); SGB(0x100, 3); SGB(0x008, 1); SGB(0x100, 3); SGB(0x008, 1); SGB(0x100, 3); SBAR(); piece(s + 4, 0); SBAR();
      mm1(wbuf, 1, 1, 1); SBAR(); piece(s + 4, 1); SBAR();
      mm1(wbuf, 1, 1, 2); SBAR(); piece(s + 4, 2); SBAR();
      mm1(wbuf, 1, 1, 3); SBAR(); pin4(1); piece(s + 4, 3); if (s <= 7) own_pf(pfo, s + 8); else if (s <= 10) { warm(2 * (s - 8)); warm(2 * (s - 8) + 1); } SBAR();
    } else {
      mm1(wbuf, 1, 1, 0); SBAR(); piece(19, 0); SBAR(); mm1(wbuf, 1, 1, 1); SBAR(); piece(19, 1); SBAR(); mm1(wbuf, 1, 1, 2); SBAR(); piece(19, 2); SBAR(); mm1(wbuf, 1, 1, 3); SBAR(); piece(19, 3); SBAR();
    }
  }
  if (KT) {
#pragma unroll
    for (int i = 0; i < 8; ++i)
#pragma unroll
      for (int dp = 0; dp < 2; ++dp)
        kv[i * 2 + dp] = (u32x4){cvtpk(acc[i][2 * dp][0], acc[i][2 * dp][1]), cvtpk(acc[i][2 * dp][2], acc[i][2 * dp][3]), cvtpk(acc[i][2 * dp + 1][0], acc[i][2 * dp + 1][1]), cvtpk(acc[i][2 * dp + 1][2], acc[i][2 * dp + 1][3])};
  } else {
#pragma unroll
    for (int ip = 0; ip < 4; ++ip)
#pragma unroll
      for (int j = 0; j < 4; ++j) {
        unsigned a0 = cvtpk(acc[2 * ip][j][0], acc[2 * ip][j][1]), a1 = cvtpk(acc[2 * ip][j][2], acc[2 * ip][j][3]);
        unsigned b0 = cvtpk(acc[2 * ip + 1][j][0], acc[2 * ip + 1][j][1]), b1 = cvtpk(acc[2 * ip + 1][j][2], acc[2 * ip + 1][j][3]);
        auto r0 = __builtin_amdgcn_permlane32_swap(a0, b0, false, false);
        auto r1 = __builtin_amdgcn_permlane32_swap(a1, b1, false, false);
        kv[ip * 4 + j] = (u32x4){r0[0], r1[0], r0[1], r1[1]};
      }
  }
}

template <bool FFN>
DI void phaseB(char* lds, char* ws, PBsrc pb, __amdgpu_buffer_rsrc_t sK, u64 tok, int h, int bi, int wid, int lane, const u32x4 (&kv)[16]) {
  pb_lanes(pb, wid, lane);
  const int c = lane & 15, g = lane >> 4, rgp = wid >> 1, ch = wid & 1;
  const unsigned lds0 = (unsigned)(uintptr_t)lds;
  constexpr int NP = 4;
  auto kvpiece = [&](unsigned base, int tile, int ldsoff, int m) {
    dma16(sK, pb.vL, base + (unsigned)(h * 524288 + tile * 16384 + (wid * 2 + m) * 1024), lds0 + (unsigned)(ldsoff + (wid * 2 + m) * 1024));
  };
  const int v0slot = L_V + ((0 - (2 * bi + 2)) & 3) * 16384;
  constexpr int NJ = FFN ? 6 : 4;
  const int xb = rgp * 4096 + lane * 16;
  int wb[2];
#pragma unroll
  for (int par = 0; par < 2; ++par) wb[par] = 16384 + g * 4096 + ((lane >> 2) & 3) * 64 + ((par ^ (g & 1)) * 32) + (lane & 3) * 8;
  f32x4 acc[4][NJ];
#pragma unroll
  for (int i = 0; i < 4; ++i)
#pragma unroll
    for (int j = 0; j < NJ; ++j) acc[i][j] = f32x4{};
  bf16x8 xf[2][2], wf[2][NJ];
  auto rdW = [&](int s, int buf) {
    lds_cptr base = (lds_cptr)lds + pb_slot(s);
#pragma unroll
    for (int j = 0; j < NJ; ++j) { const int u = (j < 4) ? (2 * ch + (j >> 1)) : (4 + ch);
      wf[buf][j] = cat8(ldstr(base + wb[j & 1] + u * 256), ldstr(base + wb[j & 1] + 2048 + u * 256)); }
  };
  auto rdX = [&](int s, int half, int buf) {
    lds_cptr base = (lds_cptr)lds + pb_slot(s);
#pragma unroll
    for (int i = 0; i < 2; ++i) xf[buf][i] = ldsr128(base + xb + (half * 2 + i) * 1024);
  };
  auto mm1 = [&](int wbuf, int xbuf, int half, int i) {
#pragma unroll
    for (int j = 0; j < NJ; ++j) acc[half * 2 + i][j] = MFMA16(wf[wbuf][j], xf[xbuf][i], acc[half * 2 + i][j]);
  };
  auto pin1 = [&](int i) {
#pragma unroll
    for (int j = 0; j < NJ; j += 2) asm volatile("" : "+v"(acc[i][j]), "+v"(acc[i][j + 1]));
  };
  const int km = wid & 1, nq = wid >> 1;
  const bool kt = wid < 4;
  const unsigned kvV = (unsigned)((g >> 1) * 1024 + ((g & 1) * 32 + c) * 16);
  const unsigned kvS = kt ? (unsigned)(((h * 32 + bi * 4 + 2 * km) * 16 + 4 * nq) * 1024) : (16u << 20) + (unsigned)(((h * 32 + bi * 4 + 2 * km) * 16 + 8 * (nq - 2)) * 1024);
  const unsigned fast = (unsigned)__builtin_amdgcn_readfirstlane((int)((unsigned)(bi - 2) >> 31)), slow = (unsigned)__builtin_amdgcn_readfirstlane((int)(fast ^ 1u));
  auto kvstore = [&](int n, unsigned en) {
    const int i = n >> 1, dp = n & 1, ip = n >> 2, j = n & 3;
    const unsigned ok = (unsigned)(((i >> 2) * 16 + ((i >> 1) & 1) * 8 + 2 * dp) * 1024 + (i & 1) * 256);
    const unsigned ov = (unsigned)(((ip >> 1) * 16 + (j >> 1) * 4 + 2 * (ip & 1)) * 1024 + (j & 1) * 256);
    st16_if(en, kv[n], sK, kvV, kvS + (kt ? ok : ov));
  };
  wait_bar<12>();
  rdW(0, 0); rdX(0, 0, 0);
#pragma unroll
  for (int s = 0; s < 16; ++s) {
    const int wbuf = s & 1;
    rdX(s, 1, 1);
    mm1(wbuf, 0, 0, 0); mm1(wbuf, 0, 0, 1);
    SGB(0x008, 1); SGB(0x100, 1); SGB(0x008, 1); SGB(0x100, 1); SGB(0x008, 2 * NJ - 2); SBAR(); pin1(0); pin1(1);
    if (s == 14) check_w0<true, true>(wid, flagp(ws, F_KV + h * 8), 0u, tok, 1ull);
    if (s < 15) {
      if (s == 0) wait_bar<8>(); else if (s == 1 || s == 9) wait_bar<10>();
      else if (s <= 8) { if (s % 3 == 0) wait_barf<12, 10>(fast); else wait_barf<12, 11>(fast); }
      else if (s <= 11) wait_barf<8, 11>(fast); else if (s == 12) wait_barf<8, 10>(fast);
      else if (s == 13) wait_bar2<0, 4>(wid); else wait_bar<0>();
      SBAR();
      if (s == 10) signal_w0(wid, fast, flagp(ws, F_KV + h * 8 + bi), tok);
      if (s == 14) signal_w0(wid, slow, flagp(ws, F_KV + h * 8 + bi), tok);
      if (s == 12) poll_w0(wid, flagp(ws, F_KV + h * 8), 0u);
      rdW(s + 1, wbuf ^ 1); rdX(s + 1, 0, 0);
      mm1(wbuf, 1, 1, 0);
#pragma unroll
      for (int q = 0; q < NJ; ++q) { SGB(0x008, 1); SGB(0x100, 3); }
      SBAR(); pin1(2);
      if (s + 4 < 16) { pb_piece(pb, lds0, wid, s + 4, 0); pb_piece(pb, lds0, wid, s + 4, 1); SBAR(); }
      if (s <= 7) { kvstore(2 * s, fast); SBAR(); }
      mm1(wbuf, 1, 1, 1); SBAR(); pin1(3);
      if (s + 4 < 16) { pb_piece(pb, lds0, wid, s + 4, 2); pb_piece(pb, lds0, wid, s + 4, 3); SBAR(); }
      if (s <= 7) { kvstore(2 * s + 1, fast); SBAR(); }
      if (s <= 11) { const int n0 = s + (s + 2) / 3; kvstore(n0, slow); if (s % 3 == 0) kvstore(n0 + 1, slow); SBAR(); }
    } else {
      wait_bar2<0, 63>(wid);
      kvpiece(0u, 0, L_KS0, 0); kvpiece(0u, 0, L_KS0, 1); kvpiece(0u, 1, L_KS0 + 16384, 0); kvpiece(0u, 1, L_KS0 + 16384, 1);
      kvpiece(16u << 20, 0, v0slot, 0); kvpiece(16u << 20, 0, v0slot, 1);
      SBAR();
      mm1(wbuf, 1, 1, 0); mm1(wbuf, 1, 1, 1);
    }
  }
  char* qdst = lds + ((rgp < 2) ? L_QA : L_QB);
  const __amdgpu_buffer_rsrc_t sH = mk_srd(ws + WS_H, 128u * 44u * 1024u);
#pragma unroll
  for (int ii = 0; ii < 4; ++ii) {
    const int rb32 = 2 * (rgp & 1) + (ii >> 1), r = 16 * (ii & 1) + c;
#pragma unroll
    for (int dpp = 0; dpp < 2; ++dpp) {
      const int ks = 2 * (2 * ch + dpp) + (g >> 1);
      const f32x4 a = acc[ii][2 * dpp] * QSCALE, b = acc[ii][2 * dpp + 1] * QSCALE;
      u32x4 w = {cvtpk(a[0], a[1]), cvtpk(a[2], a[3]), cvtpk(b[0], b[1]), cvtpk(b[2], b[3])};
      *(u32x4*)(qdst + ((rb32 * 8 + ks) * 64 + (g & 1) * 32 + r) * 16) = w;
    }
    if (FFN) {
      const int rb16 = ((rgp < 2) ? (15 - bi) * 8 : bi * 8) + 4 * (rgp & 1) + ii;
      const bool valid = (h * 64 + ch * 32) < 1376;
      f32x4 a = acc[ii][NJ - 2], b = acc[ii][NJ - 1];
#pragma unroll
      for (int e = 0; e < 4; ++e) { a[e] = valid ? fmaxf(a[e], 0.f) : 0.f; b[e] = valid ? fmaxf(b[e], 0.f) : 0.f; }
      u32x4 w = {cvtpk(a[0], a[1]), cvtpk(a[2], a[3]), cvtpk(b[0], b[1]), cvtpk(b[2], b[3])};
      st16_wt(sH, (unsigned)(((rb16 * 44 + 2 * h + ch) * 64 + lane) * 16), w);
    }
  }
}

DI void store_ctx(__amdgpu_buffer_rsrc_t sCtx, const float (&y)[16], int rowblk16, int h, int db, int r, int hi) {
#pragma unroll
  for (int sx = 0; sx < 2; ++sx) {
    u32x4 w = {cvtpk(y[8 * sx], y[8 * sx + 1]), cvtpk(y[8 * sx + 2], y[8 * sx + 3]), cvtpk(y[8 * sx + 4], y[8 * sx + 5]), cvtpk(y[8 * sx + 6], y[8 * sx + 7])};
    st16_wt(sCtx, (unsigned)((((rowblk16 + (r >> 4)) * 128 + h * 4 + db) * 64 + (2 * sx + hi) * 16 + (r & 15)) * 16), w);
  }
}

template <int OFF> DI void dma2(__amdgpu_buffer_rsrc_t srd, unsigned voff, unsigned soff, unsigned lds_dst) {
  asm volatile("s_mov_b32 m0, %0\n\ts_nop 0\n\tbuffer_load_dwordx4 %1, %2, %3 offen offset:%4 lds\n\tbuffer_load_dwordx4 %1, %2, %3 offen offset:%5 lds"
               :: "s"(lds_dst), "v"(voff), "s"(srd), "s"(soff), "i"(OFF), "i"(OFF + 1024) : "memory");
}
DI unsigned rdl(unsigned v, int l) { return (unsigned)__builtin_amdgcn_readlane((int)v, l); }

DI void phaseC(char* lds, char* ws, __amdgpu_buffer_rsrc_t sK, u64 tok, int h, int bi, int wid, int lane) {
  const unsigned pvo = (unsigned)(lane & 7) * 8u;
  const __amdgpu_buffer_rsrc_t sCtx = mk_srd(ws + WS_CTX, 16u << 20);
  const int rg = wid & 3, grp = wid >> 2, r = lane & 31, hi = lane >> 5;
  const unsigned lds0 = (unsigned)(uintptr_t)lds;
  const int P1 = 2 * bi + 2;
  const unsigned vL = (unsigned)lane * 16u;
  unsigned tKs, tVs, tC;
  {
    const unsigned hbase = (unsigned)(h * 524288);
    auto tix = [&](int g) { return g < P1 ? g : P1 + 2 * (g - P1) + grp; };
    auto kslot = [&](int g) { return ((grp && g >= P1) ? L_KS2 : L_KS0) + (g & 1) * 16384; };
    auto vslot = [&](int g) { return g < P1 ? L_V + ((g - P1) & 3) * 16384 : ((grp && g == 16) ? L_KS2 + 16384 : L_V + (2 * grp + (g & 1)) * 16384); };
    const int k = lane, gk = k + 1;
    const bool ksh = gk < P1, kok = gk <= 16;
    const unsigned ko = ksh ? wid * 2048 : rg * 4096;
    tKs = kok ? (hbase + (unsigned)(tix(gk) * 16384) + ko) | (unsigned)(kslot(gk) >> 14) : (OOBS | 14u);
    const bool vsh = k < P1;
    const int gv = (vsh || !grp) ? k : k - 1;
    const bool vok = vsh || gv >= P1;
    const unsigned vo = vsh ? wid * 2048 : rg * 4096;
    tVs = vok ? ((16u << 20) + hbase + (unsigned)(tix(gv) * 16384) + vo) | (unsigned)(vslot(gv) >> 14) : (OOBS | 14u);
    const int g = lane - 1;
    const bool first = (g < 0) || (grp && g + 1 == P1);
    const bool mask = (g + 1 == 16) || (grp && (g + 1 == P1 - 2 || g + 1 == P1 - 1));
    const unsigned qsrc = (g < 0 && grp) ? (unsigned)L_QB : (unsigned)L_QA;
    tC = (unsigned)(kslot(g + 1) >> 14) | ((unsigned)((g < 0 ? L_KS0 : vslot(g)) >> 14) << 4) | ((unsigned)(kslot(g + 2) >> 14) << 8) | ((qsrc >> 14) << 12) |
         (first ? 1u << 16 : 0u) | (mask ? 1u << 17 : 0u) | ((kok && !ksh) ? 1u << 18 : 0u) | ((vok && !vsh) ? 1u << 19 : 0u) |
         ((unsigned)((((grp && g + 1 < P1) ? bi : 15 - bi) * 4 + rg - tix(g + 1) * 2) & 255) << 24);
  }
  const float rl = (float)(r - 4 * hi) + 0.5f;
  bf16x8 qf[8];
  f32x16 o[4], S1, T0, T1, negm;
  u32x4 pk0 = {}, pk1 = {}, pk2, pk3;
  float m = 0.f, l = 0.f, lB = 0.f, ma, mb;
  bf16x8 k0, k1, k2, k3, v0, v1, v2, v3;
#pragma unroll
  for (int d = 0; d < 4; ++d) o[d] = f32x16{};
  S1 = f32x16{};
#define KF(i) ldsr128(kp + (i) * 1024)
#define VF(db, ks) ldsr128(((db) < 2 ? vpl : vpl_hi) + (((db) & 1) * 4 + (ks)) * 1024)
#define EX(v) __builtin_amdgcn_exp2f(v)
#define BFR(x) __builtin_bit_cast(bf16x8, x)
#define ADD2(a0, a1) asm("v_add_f32_e32 %0, %0, %2\n\tv_add_f32_e32 %1, %1, %3" : "+v"(sa), "+v"(sb) : "v"(a0), "v"(a1))
#define MX2(X, i) asm("v_max3_f32 %0, %0, %2, %3\n\tv_max3_f32 %1, %1, %4, %5" : "+v"(ma), "+v"(mb) : "v"(X[i]), "v"(X[i + 1]), "v"(X[i + 2]), "v"(X[i + 3]))
  unsigned ksrc = 0x7f000000u, kdst = lds0 + 0x38000u, vsrc = 0x7f000000u, vdst = lds0 + 0x38000u, dfl = 0;
#define HOOK0() do { if (dfl & 1) dma2<0>(sK, vL, ksrc, kdst); SBAR(); } while (0)
#define HOOK1() do { if (dfl & 2) dma2<2048>(sK, vL, ksrc, kdst); SBAR(); } while (0)
#define HOOK2() do { if (dfl & 1) dma2<0>(sK, vL, vsrc, vdst); SBAR(); } while (0)
#define HOOK3() do { if (dfl & 4) dma2<2048>(sK, vL, vsrc, vdst); SBAR(); } while (0)
#define PLAN(k) do { const unsigned a_ = rdl(tKs, k), b_ = rdl(tVs, k); dfl = 1u | ((rdl(tC, k) >> 17) & 6u); ksrc = a_ & ~15u; vsrc = b_ & ~15u; \
    kdst = lds0 + (a_ & 15u) * 16384u + ((dfl & 2u) ? rg * 4096u : wid * 2048u); vdst = lds0 + (b_ & 15u) * 16384u + ((dfl & 4u) ? rg * 4096u : wid * 2048u); } while (0)
  #pragma unroll 1
  for (int g = -1; g < 16; ++g) {
    const unsigned tc = rdl(tC, g + 1);
    const lds_cptr kp = (lds_cptr)lds + (lane * 16 + (tc & 15u) * 16384u);
    const lds_cptr vp = (lds_cptr)lds + (lane * 16 + ((tc >> 4) & 15u) * 16384u);
    const lds_cptr vpl = vp + grp * 8192, vpl_hi = vp + (8192 - grp * 8192);
    const bool first_next = (tc >> 16) & 1u;
    dfl = 0;
    if (grp == 0 || g <= 0) {
      if (g < 0) asm volatile("s_waitcnt vmcnt(4) lgkmcnt(0)\n\ts_barrier" ::: "memory");
      else {
        const int gk = g + 2 > 16 ? 16 : g + 2, tmax = gk < P1 ? gk : 2 * gk - P1 + 1;
        asm volatile("s_waitcnt vmcnt(0)" ::: "memory");
        check_w0<false, true>(wid, flagp(ws, F_KV + h * 8), pvo, tok, (2ull << (tmax >> 2)) - 1ull);
        asm volatile("s_waitcnt lgkmcnt(0)\n\ts_barrier" ::: "memory");
        PLAN(g + 1);
      }
      poll_w0(wid, flagp(ws, F_KV + h * 8), pvo);
            k0 = KF(0); k1 = KF(1); k2 = KF(2); k3 = KF(3);
    }
    if (first_next) {
      const lds_cptr qp = (lds_cptr)lds + (lane * 16 + rg * 8192 + ((tc >> 12) & 15u) * 16384u);
#pragma unroll
      for (int ks = 0; ks < 8; ++ks) qf[ks] = ldsr128(qp + ks * 1024);
      negm = f32x16{};
    }
    PIN(negm);
    SBAR();
    {
      float sa = 0.f, sb = 0.f;
      bf16x8 k4, k5;
      T0 = MFMA(k0, qf[0], negm); k4 = KF(4); k5 = KF(5); ADD2(S1[0], S1[1]); pk2[0] = cvtpk(S1[0], S1[1]); PIN(pk2); SBAR();
      T0 = MFMA(k1, qf[1], T0); ADD2(S1[2], S1[3]); pk2[1] = cvtpk(S1[2], S1[3]); PIN(pk2); SBAR();
      T0 = MFMA(k2, qf[2], T0); k0 = KF(6); k1 = KF(7); ADD2(S1[4], S1[5]); pk2[2] = cvtpk(S1[4], S1[5]); PIN(pk2); SBAR();
      T0 = MFMA(k3, qf[3], T0); ADD2(S1[6], S1[7]); pk2[3] = cvtpk(S1[6], S1[7]); PIN(pk2); SBAR();
      HOOK0();
      T0 = MFMA(k4, qf[4], T0); k2 = KF(8); k3 = KF(9); ADD2(S1[8], S1[9]); pk3[0] = cvtpk(S1[8], S1[9]); PIN(pk3); SBAR();
      T0 = MFMA(k5, qf[5], T0); ADD2(S1[10], S1[11]); pk3[1] = cvtpk(S1[10], S1[11]); PIN(pk3); SBAR();
      T0 = MFMA(k0, qf[6], T0); k4 = KF(10); k5 = KF(11); ADD2(S1[12], S1[13]); pk3[2] = cvtpk(S1[12], S1[13]); PIN(pk3); SBAR();
      T0 = MFMA(k1, qf[7], T0); ADD2(S1[14], S1[15]); pk3[3] = cvtpk(S1[14], S1[15]); PIN(pk3); SBAR();
      HOOK1();
      l += sa + sb; PIN(pk2); PIN(pk3);
      T1 = MFMA(k2, qf[0], negm); k0 = KF(12); k1 = KF(13); SBAR();
      T1 = MFMA(k3, qf[1], T1); SBAR();
      T1 = MFMA(k4, qf[2], T1); k2 = KF(14); k3 = KF(15); SBAR();
      T1 = MFMA(k5, qf[3], T1); SBAR();
      HOOK2();
      ma = T0[0]; mb = T0[1];
      T1 = MFMA(k0, qf[4], T1); v0 = VF(0, 0); v1 = VF(1, 0); MX2(T0, 2); SBAR();
      T1 = MFMA(k1, qf[5], T1); MX2(T0, 6); SBAR();
      T1 = MFMA(k2, qf[6], T1); v2 = VF(2, 0); v3 = VF(3, 0); MX2(T0, 10); SBAR();
      T1 = MFMA(k3, qf[7], T1); ma = max3f(ma, T0[14], T0[15]); SBAR();
      HOOK3();
    }
    if (grp == 1 && g >= 0) {
      asm volatile("s_waitcnt vmcnt(0) lgkmcnt(0)\n\ts_barrier" ::: "memory");
      v0 = VF(0, 0); v1 = VF(1, 0); v2 = VF(2, 0); v3 = VF(3, 0);
      if (g < 15) PLAN(g + 2); else dfl = 0;
          } else dfl = 0;
    {
      const lds_cptr kp = (lds_cptr)lds + (lane * 16 + ((tc >> 8) & 15u) * 16384u);
      bf16x8 v4, v5;
      o[0] = MFMA(v0, BFR(pk0), o[0]); v4 = VF(0, 1); v5 = VF(1, 1); SBAR();
      o[1] = MFMA(v1, BFR(pk0), o[1]); SBAR();
      o[2] = MFMA(v2, BFR(pk0), o[2]); v0 = VF(2, 1); v1 = VF(3, 1); MX2(T1, 0); MX2(T1, 4); SBAR();
      o[3] = MFMA(v3, BFR(pk0), o[3]); MX2(T1, 8); MX2(T1, 12); SBAR();
      ma = max2f(ma, mb);
      bool resc = false; float fsc = 1.f;
      if (__builtin_expect(((tc >> 16) & 3u) != 0u || __any(ma > THR), 0)) {
        if ((tc >> 17) & 1u) {
          const float lfB = ((float)(((int)tc >> 24) * 32 + 64) + rl) * 1.0e30f;
#pragma unroll
          for (int i = 0; i < 16; ++i) { const int kk = (i & 3) + 8 * (i >> 2); float t0_, t1_;
            asm("v_add_f32_e32 %2, %5, %4\n\tv_add_f32_e32 %3, %6, %4\n\tv_min_f32_e32 %0, %0, %2\n\tv_min_f32_e32 %1, %1, %3"
                : "+v"(T0[i]), "+v"(T1[i]), "=&v"(t0_), "=&v"(t1_) : "v"(lfB), "i"(__builtin_bit_cast(int, -(float)(kk + 64) * 1.0e30f)), "i"(__builtin_bit_cast(int, -(float)(kk + 96) * 1.0e30f))); }
        }
        ma = T0[0]; mb = T0[1];
        MX2(T0, 2); MX2(T0, 6); MX2(T0, 10); ma = max3f(ma, T0[14], T0[15]); MX2(T1, 0); MX2(T1, 4); MX2(T1, 8); MX2(T1, 12);
        const float a = swap32_max(max2f(ma, mb));
        float dl;
        if (first_next) { dl = fmaxf(a, -1.0e29f); lB = l; l = 0.f; m = dl; }
        else { dl = fmaxf(a, 0.f); m += dl; fsc = EX(-dl); l *= fsc; resc = true; }
#pragma unroll
        for (int i = 0; i < 16; ++i) { T0[i] -= dl; T1[i] -= dl; negm[i] = -m; }
      }
      SBAR();
      float sa = 0.f, sb = 0.f;
#define EX4(X, i) do { X[i] = EX(X[i]); X[i + 1] = EX(X[i + 1]); X[i + 2] = EX(X[i + 2]); X[i + 3] = EX(X[i + 3]); PIN(X); } while (0)
#define EX2(X, i) do { X[i] = EX(X[i]); X[i + 1] = EX(X[i + 1]); PIN(X); } while (0)
      HOOK0();
      o[0] = MFMA(v4, BFR(pk1), o[0]); v2 = VF(0, 2); v3 = VF(1, 2); EX4(T0, 0); SBAR();
      o[1] = MFMA(v5, BFR(pk1), o[1]); EX4(T0, 4); SBAR();
      o[2] = MFMA(v0, BFR(pk1), o[2]); v4 = VF(2, 2); v5 = VF(3, 2); EX4(T0, 8); SBAR();
      o[3] = MFMA(v1, BFR(pk1), o[3]); EX4(T0, 12); SBAR();
      HOOK1();
      o[0] = MFMA(v2, BFR(pk2), o[0]); v0 = VF(0, 3); v1 = VF(1, 3); EX2(T1, 0); ADD2(T0[0], T0[1]); pk0[0] = cvtpk(T0[0], T0[1]); PIN(pk0); SBAR();
      o[1] = MFMA(v3, BFR(pk2), o[1]); EX2(T1, 2); ADD2(T0[2], T0[3]); pk0[1] = cvtpk(T0[2], T0[3]); PIN(pk0); SBAR();
      o[2] = MFMA(v4, BFR(pk2), o[2]); v2 = VF(2, 3); v3 = VF(3, 3); EX2(T1, 4); ADD2(T0[4], T0[5]); pk0[2] = cvtpk(T0[4], T0[5]); PIN(pk0); SBAR();
      o[3] = MFMA(v5, BFR(pk2), o[3]); EX2(T1, 6); ADD2(T0[6], T0[7]); pk0[3] = cvtpk(T0[6], T0[7]); PIN(pk0); SBAR();
      HOOK2();
      o[0] = MFMA(v0, BFR(pk3), o[0]); k0 = KF(0); k1 = KF(1); EX2(T1, 8); ADD2(T0[8], T0[9]); pk1[0] = cvtpk(T0[8], T0[9]); PIN(pk1); SBAR();
      o[1] = MFMA(v1, BFR(pk3), o[1]); EX2(T1, 10); ADD2(T0[10], T0[11]); pk1[1] = cvtpk(T0[10], T0[11]); PIN(pk1); SBAR();
      o[2] = MFMA(v2, BFR(pk3), o[2]); k2 = KF(2); k3 = KF(3); EX2(T1, 12); ADD2(T0[12], T0[13]); pk1[2] = cvtpk(T0[12], T0[13]); PIN(pk1); SBAR();
      o[3] = MFMA(v3, BFR(pk3), o[3]); EX2(T1, 14); ADD2(T0[14], T0[15]); pk1[3] = cvtpk(T0[14], T0[15]); PIN(pk1); SBAR();
      HOOK3();
      l += sa + sb;
      S1 = T1;
      if (__builtin_expect(resc, 0)) {
#pragma unroll
        for (int d = 0; d < 4; ++d)
#pragma unroll
          for (int i = 0; i < 16; ++i) o[d][i] *= fsc;
      }
      if (first_next && g >= 0) {
        const float inv = 1.0f / swap32_add(lB);
#pragma unroll
        for (int d = 0; d < 4; ++d) {
          float y[16];
#pragma unroll
          for (int i = 0; i < 16; ++i) y[i] = o[d][i] * inv;
          store_ctx(sCtx, y, bi * 8 + 2 * rg, h, d ^ 2, r, hi);
          o[d] = f32x16{};
        }
      }
      if (grp == 1 && g == 14) {
        const unsigned src = (16u << 20) + (unsigned)(h * 524288 + (P1 + 2 * (16 - P1) + 1) * 16384 + rg * 4096), dst = lds0 + (unsigned)(L_KS2 + 16384 + rg * 4096);
        dma2<0>(sK, vL, src, dst); dma2<2048>(sK, vL, src, dst);
      }
    }
  }
  if (grp == 0) asm volatile("s_waitcnt vmcnt(0) lgkmcnt(0)\n\ts_barrier" ::: "memory");
    {
    const lds_cptr vp = (lds_cptr)lds + (lane * 16 + (grp ? L_KS2 + 16384 : L_V));
    const lds_cptr vpl = vp + grp * 8192, vpl_hi = vp + (8192 - grp * 8192);
#pragma unroll
    for (int i = 0; i < 16; ++i) l += S1[i];
    pk2 = pack8(S1, 0); pk3 = pack8(S1, 1);
#pragma unroll
    for (int d = 0; d < 4; ++d) { o[d] = MFMA(VF(d, 0), BFR(pk0), o[d]); o[d] = MFMA(VF(d, 1), BFR(pk1), o[d]); o[d] = MFMA(VF(d, 2), BFR(pk2), o[d]); o[d] = MFMA(VF(d, 3), BFR(pk3), o[d]); }
  }
  {
    l = swap32_add(l);
    char* mybuf = lds + grp * 32768 + rg * 8192;
    const char* pbuf = lds + (grp ^ 1) * 32768 + rg * 8192;
    float* ml = (float*)(lds + L_QA);
    asm volatile("s_waitcnt lgkmcnt(0)\n\ts_barrier" ::: "memory");
#pragma unroll
    for (int t = 0; t < 2; ++t)
#pragma unroll
      for (int q = 0; q < 4; ++q) {
        f32x4 v = {o[2 + t][4 * q], o[2 + t][4 * q + 1], o[2 + t][4 * q + 2], o[2 + t][4 * q + 3]};
        *(f32x4*)(mybuf + ((t * 4 + q) * 64 + lane) * 16) = v;
      }
    if (hi == 0) { ml[wid * 64 + r] = m; ml[wid * 64 + 32 + r] = l; }
    asm volatile("s_waitcnt lgkmcnt(0)\n\ts_barrier" ::: "memory");
    const int pw = wid ^ 4;
    const float m2 = ml[pw * 64 + r], l2 = ml[pw * 64 + 32 + r];
    const float mm = fmaxf(m, m2);
    const float f1 = EX(m - mm), f2 = EX(m2 - mm);
    const float inv = 1.0f / (l * f1 + l2 * f2);
    const float g1 = f1 * inv, g2 = f2 * inv;
#pragma unroll
    for (int t = 0; t < 2; ++t) {
      float y[16];
#pragma unroll
      for (int q = 0; q < 4; ++q) {
        const f32x4 v = *(const f32x4*)(pbuf + ((t * 4 + q) * 64 + lane) * 16);
#pragma unroll
        for (int e = 0; e < 4; ++e) y[4 * q + e] = o[t][4 * q + e] * g1 + v[e] * g2;
      }
      store_ctx(sCtx, y, (15 - bi) * 8 + 2 * rg, h, 2 * grp + t, r, hi);
    }
  }
#undef KF
#undef VF
}

typedef unsigned u32x2 __attribute__((ext_vector_type(2)));
DI void phaseD(char* lds, char* ws, const u16* x, const u16* wo, const u16* w2, u16* out, u64 tok, int h, int bi, int wid, int lane) {
  const int b = blockIdx.x, xcd = b & 7, jj = b >> 3, role = xcd >> 1, mt = (xcd & 1) * 8 + (jj >> 2), nt = jj & 3;
  const int c = lane & 15, g = lane >> 4, kgrp = wid >> 2, wr = (wid >> 1) & 1, wc = wid & 1;
  const unsigned lds0 = (unsigned)(uintptr_t)lds;
  const bool ish = role == 3;
  const int s0 = ish ? 0 : role * 22;
  constexpr int NS = 22;
  const __amdgpu_buffer_rsrc_t sA = ish ? mk_srd(ws + WS_H, 128u * 44u * 1024u) : mk_srd(ws + WS_CTX, 16u << 20);
  const __amdgpu_buffer_rsrc_t sW = ish ? mk_srd(w2, 1376u * 512u * 2u) : mk_srd(wo, 4096u * 512u * 2u);
  const unsigned vL = (unsigned)lane * 16u;
  const unsigned aRow = (unsigned)((mt * 8 + wid) * (ish ? 44 : 128) + s0 * 2) * 1024u;
  unsigned vW[2];
#pragma unroll
  for (int m = 0; m < 2; ++m) vW[m] = (unsigned)(((m * 4 + ((lane >> 2) & 3)) * 512 + (lane >> 4) * 32 + ((lane & 3) ^ (2 * m)) * 8) * 2);
  const unsigned wBase = (unsigned)((wid * 8 * 512 + nt * 128) * 2) + (unsigned)s0 * 65536u;
  const unsigned dw = lds0 + (unsigned)(wid * 2048);
  auto pA = [&](int s, int m) { dma16(sA, vL, aRow + (unsigned)(s * 2048 + m * 1024), dw + (unsigned)((s % 5) * 32768 + m * 1024)); };
  auto pW = [&](int s, int m) { dma16(sW, vW[m], wBase + (unsigned)s * 65536u, dw + (unsigned)((s % 5) * 32768 + 16384 + m * 1024)); };
  asm volatile("s_waitcnt lgkmcnt(0)\n\ts_barrier" ::: "memory");
#pragma unroll
  for (int s = 0; s < 5; ++s) { pW(s, 0); pW(s, 1); }
  wait_bar<10>();
  signal_w0(wid, 1u, flagp(ws, F_C + bi * 32 + h), tok); signal_w0(wid, 1u, flagp(ws, F_C + (15 - bi) * 32 + h), tok);
  { check_w0<false, false>(wid, flagp(ws, F_C + mt * 32), (unsigned)(lane & 31) * 8u, tok, ~0ull); }
  asm volatile("s_barrier" ::: "memory");
#pragma unroll
  for (int s = 0; s < 5; ++s) { pA(s, 0); pA(s, 1); }
  const int ab = (wr * 4 + 2 * kgrp) * 2048 + kgrp * 1024 + lane * 16, abx = (wr * 4 + 2 - 2 * kgrp) * 2048 + kgrp * 1024 + lane * 16;
  int wb[2][2];
  {
    const int kq0 = ish ? g : 4 * (g >> 1) + (g & 1), kst = ish ? 4 : 2;
#pragma unroll
    for (int par = 0; par < 2; ++par)
#pragma unroll
      for (int t = 0; t < 2; ++t) { const int kq = kgrp * 8 + kq0 + t * kst;
        wb[par][t] = 16384 + kq * 1024 + wc * 512 + ((lane >> 2) & 3) * 64 + ((par ^ (kq & 1)) * 32) + (lane & 3) * 8; }
  }
  f32x4 acc[4][4];
#pragma unroll
  for (int i = 0; i < 4; ++i)
#pragma unroll
    for (int j = 0; j < 4; ++j) acc[i][j] = f32x4{};
  bf16x8 af[2][4], wf[2][4];
  wait_bar<8>();
  {
    lds_cptr base = (lds_cptr)lds;
#pragma unroll
    for (int j = 0; j < 4; ++j) wf[0][j] = cat8(ldstr(base + wb[j & 1][0] + (j >> 1) * 256), ldstr(base + wb[j & 1][1] + (j >> 1) * 256));
#pragma unroll
    for (int i = 0; i < 4; ++i) af[0][i] = ldsr128(base + ((i < 2) ? ab : abx) + (i & 1) * 2048);
  }
#pragma unroll
  for (int s = 0; s < NS; ++s) {
    const int hf = s & 1;
    const bool nx = s + 1 < NS, pf = s + 5 < NS;
    if (nx) {
      constexpr int dummy = 0; (void)dummy;
      const int yo = (s < 3) ? (6 - 2 * s) + 4 * s : 12;
      const int tail = NS - 2 - s;
      const int n = (tail < 3) ? 4 * tail : yo;
      if (n == 12) wait_bar<12>(); else if (n == 10) wait_bar<10>(); else if (n == 8) wait_bar<8>(); else if (n == 6) wait_bar<6>(); else if (n == 4) wait_bar<4>(); else wait_bar<0>();
    }
    SBAR();
    lds_cptr base = (lds_cptr)lds + ((s + 1) % 5) * 32768;
    s16x4 wl[4], wh[4];
#define K2M(i, j) acc[i][j] = MFMA16(wf[hf][j], af[hf][i], acc[i][j])
#define PIN4(i) asm volatile("" : "+v"(acc[i][0]), "+v"(acc[i][1]), "+v"(acc[i][2]), "+v"(acc[i][3]))
    K2M(0, 0); if (nx) wl[0] = ldstr(base + wb[0][0]); SBAR();
    K2M(0, 1); if (nx) wh[0] = ldstr(base + wb[0][1]); SBAR();
    K2M(0, 2); if (nx) af[hf ^ 1][0] = ldsr128(base + ab); SBAR();
    K2M(0, 3); if (nx) wl[1] = ldstr(base + wb[1][0]); SBAR();
    PIN4(0); if (pf) pA(s + 5, 0); SBAR();
    K2M(1, 0); if (nx) wh[1] = ldstr(base + wb[1][1]); SBAR();
    K2M(1, 1); if (nx) wl[2] = ldstr(base + wb[0][0] + 256); SBAR();
    K2M(1, 2); if (nx) wh[2] = ldstr(base + wb[0][1] + 256); SBAR();
    K2M(1, 3); if (nx) wl[3] = ldstr(base + wb[1][0] + 256); SBAR();
    PIN4(1); if (pf) pA(s + 5, 1); SBAR();
    K2M(2, 0); if (nx) wh[3] = ldstr(base + wb[1][1] + 256); SBAR();
    K2M(2, 1); if (nx) af[hf ^ 1][1] = ldsr128(base + ab + 2048); SBAR();
    K2M(2, 2); if (nx) af[hf ^ 1][2] = ldsr128(base + abx); SBAR();
    K2M(2, 3); if (nx) af[hf ^ 1][3] = ldsr128(base + abx + 2048); SBAR();
    PIN4(2); if (pf) pW(s + 5, 0); SBAR();
    K2M(3, 0); K2M(3, 1); SBAR();
    if (pf) pW(s + 5, 1); SBAR();
    K2M(3, 2); K2M(3, 3); SBAR(); PIN4(3);
#undef K2M
    if (nx) {
#pragma unroll
      for (int j = 0; j < 4; ++j) wf[hf ^ 1][j] = cat8(wl[j], wh[j]);
    }
  }
  asm volatile("s_waitcnt vmcnt(0) lgkmcnt(0)\n\ts_barrier" ::: "memory");
  {
    char* mybuf = lds + wid * 8192;
    const char* pbuf = lds + (wid ^ 4) * 8192;
#pragma unroll
    for (int i = 0; i < 2; ++i)
#pragma unroll
      for (int j = 0; j < 4; ++j) *(f32x4*)(mybuf + ((i * 4 + j) * 64 + lane) * 16) = acc[2 + i][j];
    asm volatile("s_waitcnt lgkmcnt(0)\n\ts_barrier" ::: "memory");
#pragma unroll
    for (int i = 0; i < 2; ++i)
#pragma unroll
      for (int j = 0; j < 4; ++j) acc[i][j] += *(const f32x4*)(pbuf + ((i * 4 + j) * 64 + lane) * 16);
  }
  const int qd = 2 * wr + kgrp, tile = mt * 4 + nt;
  const __amdgpu_buffer_rsrc_t sP = mk_srd(ws + WS_PART, 16u << 20);
  f32x4 own[2];
#pragma unroll
  for (int j = 0; j < 4; ++j) {
    if (j != role) {
      const unsigned off = (unsigned)((((tile * 4 + j) * 4 + role) * 8 + wid) * 2048) + vL;
#pragma unroll
      for (int i = 0; i < 2; ++i) __builtin_amdgcn_raw_buffer_store_b128(__builtin_bit_cast(u32x4, acc[i][j]), sP, off + (unsigned)(i * 1024), 0, 16);
    } else { own[0] = acc[0][j]; own[1] = acc[1][j]; }
  }
  u32x2 xv[2];
  const size_t obase = (size_t)(mt * 128 + qd * 32 + c) * 512 + nt * 128 + wc * 64 + role * 16 + 4 * g;
#pragma unroll
  for (int i = 0; i < 2; ++i) xv[i] = *(const u32x2*)(x + obase + i * 16 * 512);
  asm volatile("s_waitcnt vmcnt(0)\n\ts_barrier" ::: "memory");
  signal_w0(wid, 1u, flagp(ws, F_P + tile * 4 + role), tok);
  { check_w0<false, false>(wid, flagp(ws, F_P + tile * 4), (unsigned)(lane & 3) * 8u, tok, ~0ull); }
  asm volatile("s_barrier" ::: "memory");
  f32x4 p[3][2];
#pragma unroll
  for (int rr = 0; rr < 3; ++rr) {
    const int src = (role + 1 + rr) & 3;
    const unsigned off = (unsigned)((((tile * 4 + role) * 4 + src) * 8 + wid) * 2048) + vL;
#pragma unroll
    for (int i = 0; i < 2; ++i) p[rr][i] = __builtin_bit_cast(f32x4, __builtin_amdgcn_raw_buffer_load_b128(sP, off + (unsigned)(i * 1024), 0, 16));
  }
#pragma unroll
  for (int i = 0; i < 2; ++i) {
    f32x4 ya = ish ? own[i] : (role == 2 ? p[0][i] : (role == 1 ? p[1][i] : p[2][i]));
    f32x4 sa = (own[i] + p[0][i]) + (p[1][i] + p[2][i]) - ya;
    {
      const f32x4 z = {};
      sa = (ish ? z : own[i]) + (role == 2 ? z : p[0][i]) + (role == 1 ? z : p[1][i]) + (role == 0 ? z : p[2][i]);
    }
    float o4[4];
#pragma unroll
    for (int e = 0; e < 4; ++e) {
      const float y = rbf(ya[e]);
      const float gt = rbf(y * __builtin_amdgcn_rcpf(1.f + __builtin_amdgcn_exp2f(-1.4426950408889634f * y)));
      const float sg = rbf(rbf(sa[e]) * gt);
      const float xe = (e & 1) ? __uint_as_float(xv[i][e >> 1] & 0xffff0000u) : __uint_as_float(xv[i][e >> 1] << 16);
      o4[e] = xe + sg;
    }
    u32x2 w = {cvtpk(o4[0], o4[1]), cvtpk(o4[2], o4[3])};
    *(u32x2*)(out + obase + i * 16 * 512) = w;
  }
}

__global__ __launch_bounds__(512, 2) __attribute__((amdgpu_num_vgpr(252))) void fused_block(const u16* __restrict__ x, const u16* __restrict__ ckv, const u16* __restrict__ wq, const u16* __restrict__ wuk, const u16* __restrict__ wuv,
                                                      const u16* __restrict__ wo, const u16* __restrict__ w1, const u16* __restrict__ w2, u16* __restrict__ out, char* __restrict__ ws) {
  extern __shared__ __attribute__((aligned(16))) char lds[];
  const int tid = threadIdx.x, lane = tid & 63, wid = __builtin_amdgcn_readfirstlane(tid >> 6);
  const int b = blockIdx.x, xcd = b & 7, jj = b >> 3, h = 4 * xcd + (jj >> 3), mt = jj & 7, bi = mt;
  const __amdgpu_buffer_rsrc_t sC = mk_srd(ckv, 2048u * 512u * 2u), sUK = mk_srd(wuk, 512u * 4096u * 2u), sUV = mk_srd(wuv, 512u * 4096u * 2u);
  {
    unsigned long long pcv; unsigned len;
    asm volatile("s_getpc_b64 %0\n.Lfb_pc:\n\ts_mov_b32 %1, .Lfb_end-.Lfb_pc" : "=s"(pcv), "=s"(len));
    const char* pc = (const char*)(pcv & ~63ull);
    dma16(mk_srd(pc, len), (unsigned)lane * 16u, (unsigned)(jj * 8 + wid) * 1024u, (unsigned)(uintptr_t)lds + (unsigned)wid * 2048u);
  }
  const int R = b * 8 + wid;
  u32x4 xr;
  { const u16* xp = x + (size_t)R * 512 + lane * 8; asm volatile("global_load_dwordx4 %0, %1, off" : "=v"(xr) : "v"(xp) : "memory"); }
  const unsigned vC = (unsigned)((lane >> 2) * 1024 + (((lane & 3) ^ (3 * ((lane >> 5) & 1))) * 16));
  const unsigned vW = (unsigned)((((lane >> 2) & 3) * 4096 + (lane >> 4) * 32 + ((lane & 3) ^ (2 * ((wid >> 1) & 1))) * 8) * 2);
  const unsigned sCbase = (unsigned)((mt * 256 + wid * 32) * 1024);
  const unsigned sWbase = (unsigned)((wid * 4 * 4096 + h * 128) * 2);
  {
    const unsigned lds0 = (unsigned)(uintptr_t)lds;
#pragma unroll
    for (int s = 0; s < 4; ++s) {
      const unsigned bb = lds0 + (unsigned)(PA_BASE + s * 32768);
#pragma unroll
      for (int m = 0; m < 2; ++m) dma16(sC, vC, sCbase + (unsigned)(m * 16 * 1024 + s * 64), bb + (unsigned)((wid * 2 + m) * 1024));
#pragma unroll
      for (int m = 0; m < 2; ++m) dma16(m ? sUV : sUK, vW, sWbase + (unsigned)((s * 32 * 4096) * 2), bb + 16384u + (unsigned)((wid * 2 + m) * 1024));
    }
  }
  const u64 tok = ((hw_dispatch_id() + 1ull) * 0x9E3779B97F4A7C15ull) ^ ((u64)(uintptr_t)__builtin_amdgcn_dispatch_ptr() << 7) ^ 0x5bd1e9955bd1e995ull;
  OwnPf pfo;
  { const int id = jj * 8 + wid;
    pfo.kind = id < 128 ? 0 : 1;
    const int w = id - 128;
    pfo.srd = id < 128 ? sC : ((w & 8) ? sUV : sUK);
    pfo.base = id < 128 ? (unsigned)id * 8192u : (id < 192 ? (unsigned)((w & 7) * 4 * 8192 + (4 * xcd + (w >> 4)) * 256) : OOBS);
    pfo.step = 32u * 8192u;
    pfo.dst = (unsigned)(uintptr_t)lds + (unsigned)wid * 2048u;
    own_lanes(pfo, lane);
  }
  PBsrc pb;
  pb.sX = mk_srd(ws + WS_XN, 2048u * 512u * 2u); pb.sQ = mk_srd(wq, 512u * 4096u * 2u); pb.s1 = mk_srd(w1, 512u * 1376u * 2u);
  pb.vL = pb.vQ = pb.v1 = 0u;
  pb.sXbase = (unsigned)(((wid < 4) ? (15 - bi) * 8 : bi * 8 - 8) + 2 * wid) * 16384u;
  pb.sQbase = sWbase; pb.s1base = h < 22 ? (unsigned)((wid * 4 * 1376 + h * 64) * 2) : OOBS;
  {
    asm volatile("s_waitcnt vmcnt(16)" : "+v"(xr) :: "memory");
    float v[8];
#pragma unroll
    for (int e = 0; e < 4; ++e) { v[2 * e] = __uint_as_float(xr[e] << 16); v[2 * e + 1] = __uint_as_float(xr[e] & 0xffff0000u); }
    float s = 0;
#pragma unroll
    for (int e = 0; e < 8; ++e) s += v[e];
#pragma unroll
    for (int o = 32; o; o >>= 1) s += __shfl_xor(s, o);
    const float mean = s * (1.f / 512.f);
    float q = 0;
#pragma unroll
    for (int e = 0; e < 8; ++e) { const float d = v[e] - mean; q += d * d; }
#pragma unroll
    for (int o = 32; o; o >>= 1) q += __shfl_xor(q, o);
    const float mb = rbf(mean), vb = rbf(q * (1.f / 512.f));
    const float den = rbf(__builtin_sqrtf(rbf(vb + 1e-5f)));
    float o8[8];
#pragma unroll
    for (int e = 0; e < 8; ++e) o8[e] = rbf(v[e] - mb) / den;
    u32x4 w = {cvtpk(o8[0], o8[1]), cvtpk(o8[2], o8[3]), cvtpk(o8[4], o8[5]), cvtpk(o8[6], o8[7])};
    st16_wt(pb.sX, (unsigned)((((R >> 4) * 16 + (lane >> 2)) * 64 + ((lane & 3) * 16 + (R & 15))) * 16), w);
  }
  own_pf(pfo, 4); own_pf(pfo, 5); own_pf(pfo, 6); own_pf(pfo, 7);
  const __amdgpu_buffer_rsrc_t sK = mk_srd(ws + WS_KF, 32u << 20);
  u32x4 kv[16];
  const __amdgpu_buffer_rsrc_t sWo = mk_srd(wo, 4096u * 512u * 2u), sW2 = mk_srd(w2, 1376u * 512u * 2u);
  if (wid < 4) phaseA<true>(lds, ws, sC, sUK, sUV, sCbase, sWbase, pb, sWo, sW2, pfo, tok, h, mt, wid, lane_id(), kv);
  else         phaseA<false>(lds, ws, sC, sUK, sUV, sCbase, sWbase, pb, sWo, sW2, pfo, tok, h, mt, wid, lane_id(), kv);
  if (h < 22) phaseB<true>(lds, ws, pb, sK, tok, h, bi, wid, lane_id(), kv);
  else        phaseB<false>(lds, ws, pb, sK, tok, h, bi, wid, lane_id(), kv);
  phaseC(lds, ws, sK, tok, h, bi, wid, lane_id());
  phaseD(lds, ws, x, wo, w2, out, tok, h, bi, wid, lane_id());
  asm volatile(".Lfb_end:");
}

extern "C" void kernel_launch(void* const* d_in, const int* in_sizes, int n_in, void* d_out, int out_size, void* d_ws, size_t ws_size, hipStream_t stream) {
  (void)in_sizes; (void)n_in; (void)out_size; (void)ws_size;
  (void)hipFuncSetAttribute((const void*)fused_block, hipFuncAttributeMaxDynamicSharedMemorySize, 163840);
  fused_block<<<256, 512, 163840, stream>>>((const u16*)d_in[0], (const u16*)d_in[1], (const u16*)d_in[2], (const u16*)d_in[3], (const u16*)d_in[4], (const u16*)d_in[5],
                                            (const u16*)d_in[7], (const u16*)d_in[8], (u16*)d_out, (char*)d_ws);
}
```

```cpp
#include <hip/hip_runtime.h>
#include <hip/hip_bf16.h>
#include <stdint.h>

typedef unsigned short u16;
typedef short bf16x8 __attribute__((ext_vector_type(8)));
typedef short s16x4 __attribute__((ext_vector_type(4)));
typedef float f32x16 __attribute__((ext_vector_type(16)));
typedef float f32x4 __attribute__((ext_vector_type(4)));
typedef float f32x2 __attribute__((ext_vector_type(2)));
typedef __bf16 bf16x2_t __attribute__((ext_vector_type(2)));
typedef unsigned u32x4 __attribute__((ext_vector_type(4)));
#define LAS __attribute__((address_space(3)))
typedef LAS const char* lds_cptr;
#define DI __device__ __forceinline__
#define MFMA(a, b, c) __builtin_amdgcn_mfma_f32_32x32x16_bf16((a), (b), (c), 0, 0, 0)
#define SBAR() __builtin_amdgcn_sched_barrier(0)

constexpr size_t WS_KF = 0;
constexpr size_t WS_VF = 16u << 20;
constexpr size_t WS_CTX = 32u << 20;
constexpr size_t WS_XN = 48u << 20;
constexpr size_t WS_H = 50u << 20;
constexpr size_t WS_PART = 56u << 20;
constexpr size_t WS_FLAG = 80u << 20;

constexpr size_t WS_DBG = 96u << 20;
#define RSTAMP(i) do {} while (0)
#define STAMP(i) do {} while (0)
DI unsigned cvtpk(float lo, float hi) { f32x2 v = {lo, hi}; bf16x2_t b = __builtin_convertvector(v, bf16x2_t); return __builtin_bit_cast(unsigned, b); }
DI float bf2f(unsigned v16) { return __uint_as_float(v16 << 16); }
DI float rbf(float f) { return bf2f(cvtpk(f, f) & 0xffffu); }
DI u32x4 pack8(const f32x16& x, int s) {
  u32x4 p = {cvtpk(x[8 * s], x[8 * s + 1]), cvtpk(x[8 * s + 2], x[8 * s + 3]), cvtpk(x[8 * s + 4], x[8 * s + 5]), cvtpk(x[8 * s + 6], x[8 * s + 7])};
  return p;
}
DI __amdgpu_buffer_rsrc_t mk_srd(const void* p, unsigned bytes) { return __builtin_amdgcn_make_buffer_rsrc((void*)p, (short)0, (int)bytes, 0x00020000); }
DI void dma16(__amdgpu_buffer_rsrc_t srd, unsigned voff, unsigned soff, unsigned lds_dst) {
  unsigned keep;
  asm volatile("s_mov_b32 %0, m0\n\ts_mov_b32 m0, %1\n\ts_nop 0\n\tbuffer_load_dwordx4 %2, %3, %4 offen lds\n\ts_mov_b32 m0, %0"
               : "=&s"(keep) : "s"(lds_dst), "v"(voff), "s"(srd), "s"(soff) : "memory");
}
DI bf16x8 ldsr128(lds_cptr p) { return *(const LAS bf16x8*)p; }
typedef short v4i16_t __attribute__((ext_vector_type(4)));
DI s16x4 ldstr(lds_cptr p) { return __builtin_bit_cast(s16x4, __builtin_amdgcn_ds_read_tr16_b64_v4i16((LAS v4i16_t*)p)); }
DI bf16x8 cat8(s16x4 a, s16x4 b) { return __builtin_shufflevector(a, b, 0, 1, 2, 3, 4, 5, 6, 7); }
#define ILV() do { _Pragma("unroll") for (int i_ = 0; i_ < 8; ++i_) { __builtin_amdgcn_sched_group_barrier(0x008, 1, 0); __builtin_amdgcn_sched_group_barrier(0x100, 1, 0); } } while (0)
#define ILV2() do { _Pragma("unroll") for (int i_ = 0; i_ < 2; ++i_) { __builtin_amdgcn_sched_group_barrier(0x008, 1, 0); __builtin_amdgcn_sched_group_barrier(0x100, 1, 0); } } while (0)
#define ILVF() do { _Pragma("unroll") for (int i_ = 0; i_ < 4; ++i_) { __builtin_amdgcn_sched_group_barrier(0x008, 1, 0); __builtin_amdgcn_sched_group_barrier(0x100, 2, 0); } __builtin_amdgcn_sched_group_barrier(0x008, 4, 0); } while (0)
#define WAIT_BAR0() asm volatile("s_waitcnt vmcnt(0) lgkmcnt(0)\n\ts_barrier" ::: "memory")

template <int N> DI void wait_bar() { asm volatile("s_waitcnt vmcnt(%0) lgkmcnt(0)\n\ts_barrier" :: "i"(N) : "memory"); }
#define MFMA16(a, b, c) __builtin_amdgcn_mfma_f32_16x16x32_bf16((a), (b), (c), 0, 0, 0)
#define SGB(mask, n) __builtin_amdgcn_sched_group_barrier(mask, n, 0)
constexpr unsigned OOBS = 0x7f000000u, DROP = 0x38000u;
typedef unsigned long long u64;
extern "C" __device__ u64 hw_dispatch_id() __asm("llvm.amdgcn.dispatch.id");
constexpr int F_X = 0, F_KV = 256, F_C = 512, F_P = 1024;
DI int lane_id() { int l; asm volatile("v_mbcnt_lo_u32_b32 %0, -1, 0\n\tv_mbcnt_hi_u32_b32 %0, -1, %0" : "=v"(l)); return l; }
DI u64* flagp(char* ws, int i) { return (u64*)(ws + WS_FLAG) + i; }
DI void st16_wt(__amdgpu_buffer_rsrc_t srd, unsigned off, u32x4 w) { __builtin_amdgcn_raw_buffer_store_b128(w, srd, off, 0, 16); }
constexpr int L_V = 0, L_QA = 65536, L_QB = 98304, L_KS2 = 98304, L_KS0 = 131072;
constexpr float QSCALE = 0.08838834764831845f * 1.4426950408889634f;
constexpr float THR = 8.0f;
#define PIN(x) asm volatile("" : "+v"(x))
DI float max3f(float a, float b, float c) { float r; asm("v_max3_f32 %0, %1, %2, %3" : "=v"(r) : "v"(a), "v"(b), "v"(c)); return r; }
DI float max2f(float a, float b) { float r; asm("v_max_f32_e32 %0, %1, %2" : "=v"(r) : "v"(a), "v"(b)); return r; }
DI float fadd_s(float a, float b) { float r; asm("v_add_f32_e32 %0, %1, %2" : "=v"(r) : "v"(a), "v"(b)); return r; }
DI float swap32_max(float m) { auto rr = __builtin_amdgcn_permlane32_swap(__float_as_uint(m), __float_as_uint(m), false, false); return fmaxf(__uint_as_float(rr[0]), __uint_as_float(rr[1])); }
DI float swap32_add(float m) { auto rr = __builtin_amdgcn_permlane32_swap(__float_as_uint(m), __float_as_uint(m), false, false); return __uint_as_float(rr[0]) + __uint_as_float(rr[1]); }

constexpr int PB_STAGE = 32768;
DI constexpr int pb_slot(int s) { return (s & 3) * PB_STAGE; }
constexpr int PA_BASE = 32768;
struct PBsrc { __amdgpu_buffer_rsrc_t sX, sQ, s1; unsigned vL, vQ, v1, sXbase, sQbase, s1base; };
DI void pb_lanes(PBsrc& p, int wid, int lane) {
  const int c4 = (lane & 3) ^ (2 * ((wid >> 1) & 1));
  p.vL = (unsigned)lane * 16u;
  p.vQ = (unsigned)((((lane >> 2) & 3) * 4096 + (lane >> 4) * 32 + c4 * 8) * 2);
  p.v1 = lane < 32 ? (unsigned)((((lane >> 2) & 3) * 1376 + (lane >> 4) * 32 + c4 * 8) * 2) : OOBS;
}
DI void pb_piece(const PBsrc& p, unsigned lds0, int wid, int s, int m) {
  const unsigned b = lds0 + (unsigned)pb_slot(s);
  if (m < 2) dma16(p.sX, p.vL, p.sXbase + (unsigned)(m * 16384 + s * 1024), b + (unsigned)((wid * 2 + m) * 1024));
  else if (m == 2) dma16(p.sQ, p.vQ, p.sQbase + (unsigned)(s * 32 * 4096 * 2), b + 16384u + (unsigned)(wid * 2048));
  else dma16(p.s1, p.v1, p.s1base + (unsigned)(s * 32 * 1376 * 2), b + 16384u + 1024u + (unsigned)(wid * 2048));
}
DI void dma16_nz(int wid, __amdgpu_buffer_rsrc_t srd, unsigned voff, unsigned soff, unsigned lds_dst) {
  asm volatile("s_cmp_eq_u32 %4, 0\n\ts_cbranch_scc1 1f\n\ts_mov_b32 m0, %0\n\ts_nop 0\n\tbuffer_load_dwordx4 %1, %2, %3 offen lds\n1:"
               :: "s"(lds_dst), "v"(voff), "s"(srd), "s"(soff), "s"(wid) : "memory", "scc");
}
template <int N0, int N> DI void wait_bar2(int wid) {
  asm volatile("s_cmp_eq_u32 %0, 0\n\ts_cbranch_scc1 1f\n\ts_waitcnt vmcnt(%2)\n\ts_branch 2f\n1:\n\ts_waitcnt vmcnt(%1)\n2:\n\ts_waitcnt lgkmcnt(0)\n\ts_barrier" :: "s"(wid), "i"(N0), "i"(N) : "memory", "scc");
}
DI void signal_w0(int wid, unsigned en, u64* p, u64 tok) {
  unsigned z = 0u;
  asm volatile("s_cmp_eq_u32 %0, 0\n\ts_cbranch_scc0 1f\n\ts_cmp_eq_u32 %4, 0\n\ts_cbranch_scc1 1f\n\ts_mov_b64 exec, 1\n\tglobal_store_dwordx2 %2, %3, %1 sc1\n\ts_mov_b64 exec, -1\n1:"
               :: "s"(wid), "s"(p), "v"(z), "v"(tok), "s"(en) : "memory", "scc");
}
DI void poll_w0(int wid, const u64* p, unsigned voff) {
  asm volatile("s_cmp_eq_u32 %0, 0\n\ts_cbranch_scc0 1f\n\tglobal_load_dwordx2 v[252:253], %2, %1 sc1\n1:" :: "s"(wid), "s"(p), "v"(voff) : "memory", "scc", "v252", "v253"); }
template <bool INV, bool POLLED> DI void check_w0(int wid, const u64* p, unsigned voff, u64 tok, u64 need) {
  unsigned cnt; u64 m;
  asm volatile("s_cmp_eq_u32 %2, 0\n\ts_cbranch_scc0 9f\n\ts_mov_b32 %1, 0x40000\n\t.if %8 == 0\n\ts_branch 3f\n\t.endif\n"
               "1:\n\tv_cmp_ne_u64_e64 %0, v[252:253], %5\n\ts_and_b64 %0, %0, %6\n\ts_cbranch_scc0 2f\n\ts_sub_u32 %1, %1, 1\n\ts_cmp_eq_u32 %1, 0\n\ts_cbranch_scc1 2f\n\ts_sleep 1\n"
               "3:\n\tglobal_load_dwordx2 v[252:253], %4, %3 sc1\n\ts_waitcnt vmcnt(0)\n\ts_branch 1b\n"
               "2:\n\t.if %7\n\tbuffer_inv sc1\n\t.endif\n9:"
               : "=&s"(m), "=&s"(cnt) : "s"(wid), "s"(p), "v"(voff), "v"(tok), "s"(need), "i"(INV ? 1 : 0), "i"(POLLED ? 1 : 0) : "memory", "scc", "v252", "v253");
}
template <int NF, int NS_> DI void wait_barf(unsigned fast) {
  asm volatile("s_cmp_eq_u32 %0, 0\n\ts_cbranch_scc1 1f\n\ts_waitcnt vmcnt(%1)\n\ts_branch 2f\n1:\n\ts_waitcnt vmcnt(%2)\n2:\n\ts_waitcnt lgkmcnt(0)\n\ts_barrier" :: "s"(fast), "i"(NF), "i"(NS_) : "memory", "scc");
}
DI void st16_if(unsigned en, u32x4 w, __amdgpu_buffer_rsrc_t srd, unsigned voff, unsigned soff) {
  asm volatile("s_cmp_eq_u32 %4, 0\n\ts_cbranch_scc1 1f\n\tbuffer_store_dwordx4 %0, %1, %2, %3 offen sc1\n\ts_nop 1\n1:" :: "v"(w), "v"(voff), "s"(srd), "s"(soff), "s"(en) : "memory", "scc");
}
DI void touch(__amdgpu_buffer_rsrc_t srd, unsigned voff, unsigned soff) {
  asm volatile("buffer_load_dword v254, %0, %1, %2 offen" :: "v"(voff), "s"(srd), "s"(soff) : "memory", "v254");
}

template <bool KT>
DI void phaseA(char* lds, char* ws, __amdgpu_buffer_rsrc_t sC, __amdgpu_buffer_rsrc_t sUK, __amdgpu_buffer_rsrc_t sUV, unsigned sCbase, unsigned sWbase,
               PBsrc pb, u64 tok, int h, int mt, int wid, int lane, u32x4 (&kv)[16]) {
  const int c = lane & 15, g = lane >> 4, km = wid & 1, nq = wid >> 1;
  const unsigned vC = (unsigned)((lane >> 2) * 1024 + (((lane & 3) ^ (3 * ((lane >> 5) & 1))) * 16));
  const unsigned vW = (unsigned)((((lane >> 2) & 3) * 4096 + (lane >> 4) * 32 + ((lane & 3) ^ (2 * ((wid >> 1) & 1))) * 8) * 2);
  const unsigned lds0 = (unsigned)(uintptr_t)lds;
  auto piece = [&](int s, int m) {
    if (s < 16) {
      const unsigned b = lds0 + (unsigned)(PA_BASE + (s & 3) * 32768);
      if (m < 2) dma16(sC, vC, sCbase + (unsigned)(m * 16 * 1024 + s * 64), b + (unsigned)((wid * 2 + m) * 1024));
      else       dma16((m & 1) ? sUV : sUK, vW, sWbase + (unsigned)((s * 32 * 4096) * 2), b + 16384u + (unsigned)((wid * 2 + (m & 1)) * 1024));
    } else pb_piece(pb, lds0, wid, s - 16, m);
  };
  auto warmB = [&]() {
    const int l2 = lane_id(), id = (int)(blockIdx.x >> 3) * 8 + wid, xg = (int)(blockIdx.x & 7);
    const bool q = id < 64;
    const unsigned vo = (unsigned)(l2 >> 3) * (q ? 8192u : 2752u) + (unsigned)(l2 & 7) * 128u;
    const unsigned so = q ? (unsigned)(id * 8 * 8192 + xg * 1024) : (id < 128 ? (unsigned)((id - 64) * 8 * 2752 + xg * 512) : OOBS);
    touch(q ? pb.sQ : pb.s1, vo, so);
  };
  const int cb = (128 * km + c) * 64 + ((g ^ (3 * ((c >> 3) & 1))) * 16);
  int wb[2];
#pragma unroll
  for (int par = 0; par < 2; ++par) wb[par] = 16384 + g * 4096 + nq * 512 + ((lane >> 2) & 3) * 64 + ((par ^ (g & 1)) * 32) + (lane & 3) * 8;
  f32x4 acc[8][4];
#pragma unroll
  for (int i = 0; i < 8; ++i)
#pragma unroll
    for (int j = 0; j < 4; ++j) acc[i][j] = f32x4{};
  bf16x8 cf[2][4], wf[2][4];
  auto rdW = [&](int s, int buf) {
    lds_cptr base = (lds_cptr)lds + (PA_BASE + (s & 3) * 32768);
#pragma unroll
    for (int j = 0; j < 4; ++j) wf[buf][j] = cat8(ldstr(base + wb[j & 1] + (j >> 1) * 256), ldstr(base + wb[j & 1] + 2048 + (j >> 1) * 256));
  };
  auto rdC = [&](int s, int half, int buf) {
    lds_cptr base = (lds_cptr)lds + (PA_BASE + (s & 3) * 32768);
#pragma unroll
    for (int i = 0; i < 4; ++i) cf[buf][i] = ldsr128(base + cb + (half * 4 + i) * 1024);
  };
  auto mm1 = [&](int wbuf, int cbuf, int half, int i) {
#pragma unroll
    for (int j = 0; j < 4; ++j) acc[half * 4 + i][j] = KT ? MFMA16(wf[wbuf][j], cf[cbuf][i], acc[half * 4 + i][j]) : MFMA16(cf[cbuf][i], wf[wbuf][j], acc[half * 4 + i][j]);
  };
  auto pin4 = [&](int half) {
#pragma unroll
    for (int i = 0; i < 4; ++i) asm volatile("" : "+v"(acc[half * 4 + i][0]), "+v"(acc[half * 4 + i][1]), "+v"(acc[half * 4 + i][2]), "+v"(acc[half * 4 + i][3]));
  };
  const int bi = mt;
  wait_bar<15>();
  rdW(0, 0); rdC(0, 0, 0);
#pragma unroll
  for (int s = 0; s < 16; ++s) {
    const int wbuf = s & 1;
    rdC(s, 1, 1);
    mm1(wbuf, 0, 0, 0); mm1(wbuf, 0, 0, 1); mm1(wbuf, 0, 0, 2); mm1(wbuf, 0, 0, 3);
    SGB(0x008, 2); SGB(0x100, 1); SGB(0x008, 2); SGB(0x100, 1); SGB(0x008, 2); SGB(0x100, 1); SGB(0x008, 2); SGB(0x100, 1); SGB(0x008, 8); SBAR(); pin4(0);
    if (KT && s == 10) { const int l2 = lane_id(); check_w0<true, true>(wid, flagp(ws, F_X), (unsigned)((((l2 & 16) ? 15 - bi : bi) * 16 + (l2 & 15)) * 8), tok, ~0ull); }
    if (s < 15) {
      if (s == 0) wait_bar<11>(); else if (s <= 2) wait_bar<11>(); else if (s == 5 || s == 6 || s == 7) wait_bar<9>(); else wait_bar<8>();
      SBAR();
      if (s == 12) pb_lanes(pb, wid, lane_id());
      if (KT && s == 4) signal_w0(wid, 1u, flagp(ws, F_X + blockIdx.x), tok);
      if (KT && s == 6) { const int l2 = lane_id(); poll_w0(wid, flagp(ws, F_X), (unsigned)((((l2 & 16) ? 15 - bi : bi) * 16 + (l2 & 15)) * 8)); }
      rdW(s + 1, wbuf ^ 1); rdC(s + 1, 0, 0);
      mm1(wbuf, 1, 1, 0); SGB(0x008, 1); SGB(0x100, 3); SGB(0x008, 1); SGB(0x100, 3); SGB(0x008, 1); SGB(0x100, 3); SGB(0x008, 1); SGB(0x100, 3); SBAR(); piece(s + 4, 0); SBAR();
      mm1(wbuf, 1, 1, 1); SBAR(); piece(s + 4, 1); SBAR();
      mm1(wbuf, 1, 1, 2); SBAR(); piece(s + 4, 2); SBAR();
      mm1(wbuf, 1, 1, 3); SBAR(); pin4(1); piece(s + 4, 3); if (s == 4) warmB(); SBAR();
    } else {
      mm1(wbuf, 1, 1, 0); SBAR(); piece(19, 0); SBAR(); mm1(wbuf, 1, 1, 1); SBAR(); piece(19, 1); SBAR(); mm1(wbuf, 1, 1, 2); SBAR(); piece(19, 2); SBAR(); mm1(wbuf, 1, 1, 3); SBAR(); piece(19, 3); SBAR();
    }
  }
  if (KT) {
#pragma unroll
    for (int i = 0; i < 8; ++i)
#pragma unroll
      for (int dp = 0; dp < 2; ++dp)
        kv[i * 2 + dp] = (u32x4){cvtpk(acc[i][2 * dp][0], acc[i][2 * dp][1]), cvtpk(acc[i][2 * dp][2], acc[i][2 * dp][3]), cvtpk(acc[i][2 * dp + 1][0], acc[i][2 * dp + 1][1]), cvtpk(acc[i][2 * dp + 1][2], acc[i][2 * dp + 1][3])};
  } else {
#pragma unroll
    for (int ip = 0; ip < 4; ++ip)
#pragma unroll
      for (int j = 0; j < 4; ++j) {
        unsigned a0 = cvtpk(acc[2 * ip][j][0], acc[2 * ip][j][1]), a1 = cvtpk(acc[2 * ip][j][2], acc[2 * ip][j][3]);
        unsigned b0 = cvtpk(acc[2 * ip + 1][j][0], acc[2 * ip + 1][j][1]), b1 = cvtpk(acc[2 * ip + 1][j][2], acc[2 * ip + 1][j][3]);
        auto r0 = __builtin_amdgcn_permlane32_swap(a0, b0, false, false);
        auto r1 = __builtin_amdgcn_permlane32_swap(a1, b1, false, false);
        kv[ip * 4 + j] = (u32x4){r0[0], r1[0], r0[1], r1[1]};
      }
  }
}

template <bool FFN>
DI void phaseB(char* lds, char* ws, PBsrc pb, __amdgpu_buffer_rsrc_t sK, u64 tok, int h, int bi, int wid, int lane, const u32x4 (&kv)[16]) {
  pb_lanes(pb, wid, lane);
  const int c = lane & 15, g = lane >> 4, rgp = wid >> 1, ch = wid & 1;
  const unsigned lds0 = (unsigned)(uintptr_t)lds;
  constexpr int NP = 4;
  auto kvpiece = [&](unsigned base, int tile, int ldsoff, int m) {
    dma16(sK, pb.vL, base + (unsigned)(h * 524288 + tile * 16384 + (wid * 2 + m) * 1024), lds0 + (unsigned)(ldsoff + (wid * 2 + m) * 1024));
  };
  const int v0slot = L_V + ((0 - (2 * bi + 2)) & 3) * 16384;
  constexpr int NJ = FFN ? 6 : 4;
  const int xb = rgp * 4096 + lane * 16;
  int wb[2];
#pragma unroll
  for (int par = 0; par < 2; ++par) wb[par] = 16384 + g * 4096 + ((lane >> 2) & 3) * 64 + ((par ^ (g & 1)) * 32) + (lane & 3) * 8;
  f32x4 acc[4][NJ];
#pragma unroll
  for (int i = 0; i < 4; ++i)
#pragma unroll
    for (int j = 0; j < NJ; ++j) acc[i][j] = f32x4{};
  bf16x8 xf[2][2], wf[2][NJ];
  auto rdW = [&](int s, int buf) {
    lds_cptr base = (lds_cptr)lds + pb_slot(s);
#pragma unroll
    for (int j = 0; j < NJ; ++j) { const int u = (j < 4) ? (2 * ch + (j >> 1)) : (4 + ch);
      wf[buf][j] = cat8(ldstr(base + wb[j & 1] + u * 256), ldstr(base + wb[j & 1] + 2048 + u * 256)); }
  };
  auto rdX = [&](int s, int half, int buf) {
    lds_cptr base = (lds_cptr)lds + pb_slot(s);
#pragma unroll
    for (int i = 0; i < 2; ++i) xf[buf][i] = ldsr128(base + xb + (half * 2 + i) * 1024);
  };
  auto mm1 = [&](int wbuf, int xbuf, int half, int i) {
#pragma unroll
    for (int j = 0; j < NJ; ++j) acc[half * 2 + i][j] = MFMA16(wf[wbuf][j], xf[xbuf][i], acc[half * 2 + i][j]);
  };
  auto pin1 = [&](int i) {
#pragma unroll
    for (int j = 0; j < NJ; j += 2) asm volatile("" : "+v"(acc[i][j]), "+v"(acc[i][j + 1]));
  };
  const int km = wid & 1, nq = wid >> 1;
  const bool kt = wid < 4;
  const unsigned kvV = (unsigned)((g >> 1) * 1024 + ((g & 1) * 32 + c) * 16);
  const unsigned kvS = kt ? (unsigned)(((h * 32 + bi * 4 + 2 * km) * 16 + 4 * nq) * 1024) : (16u << 20) + (unsigned)(((h * 32 + bi * 4 + 2 * km) * 16 + 8 * (nq - 2)) * 1024);
  const unsigned fast = (unsigned)__builtin_amdgcn_readfirstlane((int)((unsigned)(bi - 2) >> 31)), slow = (unsigned)__builtin_amdgcn_readfirstlane((int)(fast ^ 1u));
  auto kvstore = [&](int n, unsigned en) {
    const int i = n >> 1, dp = n & 1, ip = n >> 2, j = n & 3;
    const unsigned ok = (unsigned)(((i >> 2) * 16 + ((i >> 1) & 1) * 8 + 2 * dp) * 1024 + (i & 1) * 256);
    const unsigned ov = (unsigned)(((ip >> 1) * 16 + (j >> 1) * 4 + 2 * (ip & 1)) * 1024 + (j & 1) * 256);
    st16_if(en, kv[n], sK, kvV, kvS + (kt ? ok : ov));
  };
  wait_bar<12>();
  rdW(0, 0); rdX(0, 0, 0);
#pragma unroll
  for (int s = 0; s < 16; ++s) {
    const int wbuf = s & 1;
    rdX(s, 1, 1);
    mm1(wbuf, 0, 0, 0); mm1(wbuf, 0, 0, 1);
    SGB(0x008, 1); SGB(0x100, 1); SGB(0x008, 1); SGB(0x100, 1); SGB(0x008, 2 * NJ - 2); SBAR(); pin1(0); pin1(1);
    if (s == 14) check_w0<true, true>(wid, flagp(ws, F_KV + h * 8), 0u, tok, 1ull);
    if (s < 15) {
      if (s == 0) wait_bar<8>(); else if (s == 1 || s == 9) wait_bar<10>();
      else if (s <= 8) { if (s % 3 == 0) wait_barf<12, 10>(fast); else wait_barf<12, 11>(fast); }
      else if (s <= 11) wait_barf<8, 11>(fast); else if (s == 12) wait_barf<8, 10>(fast);
      else if (s == 13) wait_bar2<0, 4>(wid); else wait_bar<0>();
      SBAR();
      if (s == 10) signal_w0(wid, fast, flagp(ws, F_KV + h * 8 + bi), tok);
      if (s == 14) signal_w0(wid, slow, flagp(ws, F_KV + h * 8 + bi), tok);
      if (s == 12) poll_w0(wid, flagp(ws, F_KV + h * 8), 0u);
      rdW(s + 1, wbuf ^ 1); rdX(s + 1, 0, 0);
      mm1(wbuf, 1, 1, 0);
#pragma unroll
      for (int q = 0; q < NJ; ++q) { SGB(0x008, 1); SGB(0x100, 3); }
      SBAR(); pin1(2);
      if (s + 4 < 16) { pb_piece(pb, lds0, wid, s + 4, 0); pb_piece(pb, lds0, wid, s + 4, 1); SBAR(); }
      if (s <= 7) { kvstore(2 * s, fast); SBAR(); }
      mm1(wbuf, 1, 1, 1); SBAR(); pin1(3);
      if (s + 4 < 16) { pb_piece(pb, lds0, wid, s + 4, 2); pb_piece(pb, lds0, wid, s + 4, 3); SBAR(); }
      if (s <= 7) { kvstore(2 * s + 1, fast); SBAR(); }
      if (s <= 11) { const int n0 = s + (s + 2) / 3; kvstore(n0, slow); if (s % 3 == 0) kvstore(n0 + 1, slow); SBAR(); }
    } else {
      wait_bar2<0, 63>(wid);
      kvpiece(0u, 0, L_KS0, 0); kvpiece(0u, 0, L_KS0, 1); kvpiece(0u, 1, L_KS0 + 16384, 0); kvpiece(0u, 1, L_KS0 + 16384, 1);
      kvpiece(16u << 20, 0, v0slot, 0); kvpiece(16u << 20, 0, v0slot, 1);
      SBAR();
      mm1(wbuf, 1, 1, 0); mm1(wbuf, 1, 1, 1);
    }
  }
  char* qdst = lds + ((rgp < 2) ? L_QA : L_QB);
  const __amdgpu_buffer_rsrc_t sH = mk_srd(ws + WS_H, 128u * 44u * 1024u);
#pragma unroll
  for (int ii = 0; ii < 4; ++ii) {
    const int rb32 = 2 * (rgp & 1) + (ii >> 1), r = 16 * (ii & 1) + c;
#pragma unroll
    for (int dpp = 0; dpp < 2; ++dpp) {
      const int ks = 2 * (2 * ch + dpp) + (g >> 1);
      const f32x4 a = acc[ii][2 * dpp] * QSCALE, b = acc[ii][2 * dpp + 1] * QSCALE;
      u32x4 w = {cvtpk(a[0], a[1]), cvtpk(a[2], a[3]), cvtpk(b[0], b[1]), cvtpk(b[2], b[3])};
      *(u32x4*)(qdst + ((rb32 * 8 + ks) * 64 + (g & 1) * 32 + r) * 16) = w;
    }
    if (FFN) {
      const int rb16 = ((rgp < 2) ? (15 - bi) * 8 : bi * 8) + 4 * (rgp & 1) + ii;
      const bool valid = (h * 64 + ch * 32) < 1376;
      f32x4 a = acc[ii][NJ - 2], b = acc[ii][NJ - 1];
#pragma unroll
      for (int e = 0; e < 4; ++e) { a[e] = valid ? fmaxf(a[e], 0.f) : 0.f; b[e] = valid ? fmaxf(b[e], 0.f) : 0.f; }
      u32x4 w = {cvtpk(a[0], a[1]), cvtpk(a[2], a[3]), cvtpk(b[0], b[1]), cvtpk(b[2], b[3])};
      st16_wt(sH, (unsigned)(((rb16 * 44 + 2 * h + ch) * 64 + lane) * 16), w);
    }
  }
}

DI void store_ctx(__amdgpu_buffer_rsrc_t sCtx, const float (&y)[16], int rowblk16, int h, int db, int r, int hi) {
#pragma unroll
  for (int sx = 0; sx < 2; ++sx) {
    u32x4 w = {cvtpk(y[8 * sx], y[8 * sx + 1]), cvtpk(y[8 * sx + 2], y[8 * sx + 3]), cvtpk(y[8 * sx + 4], y[8 * sx + 5]), cvtpk(y[8 * sx + 6], y[8 * sx + 7])};
    st16_wt(sCtx, (unsigned)((((rowblk16 + (r >> 4)) * 128 + h * 4 + db) * 64 + (2 * sx + hi) * 16 + (r & 15)) * 16), w);
  }
}

template <int OFF> DI void dma2(__amdgpu_buffer_rsrc_t srd, unsigned voff, unsigned soff, unsigned lds_dst) {
  asm volatile("s_mov_b32 m0, %0\n\ts_nop 0\n\tbuffer_load_dwordx4 %1, %2, %3 offen offset:%4 lds\n\tbuffer_load_dwordx4 %1, %2, %3 offen offset:%5 lds"
               :: "s"(lds_dst), "v"(voff), "s"(srd), "s"(soff), "i"(OFF), "i"(OFF + 1024) : "memory");
}
DI unsigned rdl(unsigned v, int l) { return (unsigned)__builtin_amdgcn_readlane((int)v, l); }

DI void phaseC(char* lds, char* ws, __amdgpu_buffer_rsrc_t sK, const u16* wo, const u16* w2, u64 tok, int h, int bi, int wid, int lane) {
  const unsigned pvo = (unsigned)(lane & 7) * 8u;
  const __amdgpu_buffer_rsrc_t sCtx = mk_srd(ws + WS_CTX, 16u << 20);
  const int rg = wid & 3, grp = wid >> 2, r = lane & 31, hi = lane >> 5;
  const unsigned lds0 = (unsigned)(uintptr_t)lds;
  const int P1 = 2 * bi + 2;
  const unsigned vL = (unsigned)lane * 16u;
  unsigned tKs, tVs, tC;
  {
    const unsigned hbase = (unsigned)(h * 524288);
    auto tix = [&](int g) { return g < P1 ? g : P1 + 2 * (g - P1) + grp; };
    auto kslot = [&](int g) { return ((grp && g >= P1) ? L_KS2 : L_KS0) + (g & 1) * 16384; };
    auto vslot = [&](int g) { return g < P1 ? L_V + ((g - P1) & 3) * 16384 : ((grp && g == 16) ? L_KS2 + 16384 : L_V + (2 * grp + (g & 1)) * 16384); };
    const int k = lane, gk = k + 1;
    const bool ksh = gk < P1, kok = gk <= 16;
    const unsigned ko = ksh ? wid * 2048 : rg * 4096;
    tKs = kok ? (hbase + (unsigned)(tix(gk) * 16384) + ko) | (unsigned)(kslot(gk) >> 14) : (OOBS | 14u);
    const bool vsh = k < P1;
    const int gv = (vsh || !grp) ? k : k - 1;
    const bool vok = vsh || gv >= P1;
    const unsigned vo = vsh ? wid * 2048 : rg * 4096;
    tVs = vok ? ((16u << 20) + hbase + (unsigned)(tix(gv) * 16384) + vo) | (unsigned)(vslot(gv) >> 14) : (OOBS | 14u);
    const int g = lane - 1;
    const bool first = (g < 0) || (grp && g + 1 == P1);
    const bool mask = (g + 1 == 16) || (grp && (g + 1 == P1 - 2 || g + 1 == P1 - 1));
    const unsigned qsrc = (g < 0 && grp) ? (unsigned)L_QB : (unsigned)L_QA;
    tC = (unsigned)(kslot(g + 1) >> 14) | ((unsigned)((g < 0 ? L_KS0 : vslot(g)) >> 14) << 4) | ((unsigned)(kslot(g + 2) >> 14) << 8) | ((qsrc >> 14) << 12) |
         (first ? 1u << 16 : 0u) | (mask ? 1u << 17 : 0u) | ((kok && !ksh) ? 1u << 18 : 0u) | ((vok && !vsh) ? 1u << 19 : 0u) |
         ((unsigned)((((grp && g + 1 < P1) ? bi : 15 - bi) * 4 + rg - tix(g + 1) * 2) & 255) << 24);
  }
  const float rl = (float)(r - 4 * hi) + 0.5f;
  bf16x8 qf[8];
  f32x16 o[4], S1, T0, T1, negm;
  u32x4 pk0 = {}, pk1 = {}, pk2, pk3;
  float m = 0.f, l = 0.f, lB = 0.f, ma, mb;
  bf16x8 k0, k1, k2, k3, v0, v1, v2, v3;
#pragma unroll
  for (int d = 0; d < 4; ++d) o[d] = f32x16{};
  S1 = f32x16{};
#define KF(i) ldsr128(kp + (i) * 1024)
#define VF(db, ks) ldsr128(((db) < 2 ? vpl : vpl_hi) + (((db) & 1) * 4 + (ks)) * 1024)
#define EX(v) __builtin_amdgcn_exp2f(v)
#define BFR(x) __builtin_bit_cast(bf16x8, x)
#define ADD2(a0, a1) asm("v_add_f32_e32 %0, %0, %2\n\tv_add_f32_e32 %1, %1, %3" : "+v"(sa), "+v"(sb) : "v"(a0), "v"(a1))
#define MX2(X, i) asm("v_max3_f32 %0, %0, %2, %3\n\tv_max3_f32 %1, %1, %4, %5" : "+v"(ma), "+v"(mb) : "v"(X[i]), "v"(X[i + 1]), "v"(X[i + 2]), "v"(X[i + 3]))
  unsigned ksrc = 0x7f000000u, kdst = lds0 + 0x38000u, vsrc = 0x7f000000u, vdst = lds0 + 0x38000u, dfl = 0;
#define HOOK0() do { if (dfl & 1) dma2<0>(sK, vL, ksrc, kdst); SBAR(); } while (0)
#define HOOK1() do { if (dfl & 2) dma2<2048>(sK, vL, ksrc, kdst); SBAR(); } while (0)
#define HOOK2() do { if (dfl & 1) dma2<0>(sK, vL, vsrc, vdst); SBAR(); } while (0)
#define HOOK3() do { if (dfl & 4) dma2<2048>(sK, vL, vsrc, vdst); SBAR(); } while (0)
#define PLAN(k) do { const unsigned a_ = rdl(tKs, k), b_ = rdl(tVs, k); dfl = 1u | ((rdl(tC, k) >> 17) & 6u); ksrc = a_ & ~15u; vsrc = b_ & ~15u; \
    kdst = lds0 + (a_ & 15u) * 16384u + ((dfl & 2u) ? rg * 4096u : wid * 2048u); vdst = lds0 + (b_ & 15u) * 16384u + ((dfl & 4u) ? rg * 4096u : wid * 2048u); } while (0)
  #pragma unroll 1
  for (int g = -1; g < 16; ++g) {
    const unsigned tc = rdl(tC, g + 1);
    const lds_cptr kp = (lds_cptr)lds + (lane * 16 + (tc & 15u) * 16384u);
    const lds_cptr vp = (lds_cptr)lds + (lane * 16 + ((tc >> 4) & 15u) * 16384u);
    const lds_cptr vpl = vp + grp * 8192, vpl_hi = vp + (8192 - grp * 8192);
    const bool first_next = (tc >> 16) & 1u;
    dfl = 0;
    if (grp == 0 || g <= 0) {
      if (g < 0) asm volatile("s_waitcnt vmcnt(4) lgkmcnt(0)\n\ts_barrier" ::: "memory");
      else {
        const int gk = g + 2 > 16 ? 16 : g + 2, tmax = gk < P1 ? gk : 2 * gk - P1 + 1;
        asm volatile("s_waitcnt vmcnt(0)" ::: "memory");
        check_w0<false, true>(wid, flagp(ws, F_KV + h * 8), pvo, tok, (2ull << (tmax >> 2)) - 1ull);
        asm volatile("s_waitcnt lgkmcnt(0)\n\ts_barrier" ::: "memory");
        PLAN(g + 1);
      }
      poll_w0(wid, flagp(ws, F_KV + h * 8), pvo);
            k0 = KF(0); k1 = KF(1); k2 = KF(2); k3 = KF(3);
    }
    if (first_next) {
      const lds_cptr qp = (lds_cptr)lds + (lane * 16 + rg * 8192 + ((tc >> 12) & 15u) * 16384u);
#pragma unroll
      for (int ks = 0; ks < 8; ++ks) qf[ks] = ldsr128(qp + ks * 1024);
      negm = f32x16{};
    }
    PIN(negm);
    SBAR();
    {
      float sa = 0.f, sb = 0.f;
      bf16x8 k4, k5;
      T0 = MFMA(k0, qf[0], negm); k4 = KF(4); k5 = KF(5); ADD2(S1[0], S1[1]); pk2[0] = cvtpk(S1[0], S1[1]); PIN(pk2); SBAR();
      T0 = MFMA(k1, qf[1], T0); ADD2(S1[2], S1[3]); pk2[1] = cvtpk(S1[2], S1[3]); PIN(pk2); SBAR();
      T0 = MFMA(k2, qf[2], T0); k0 = KF(6); k1 = KF(7); ADD2(S1[4], S1[5]); pk2[2] = cvtpk(S1[4], S1[5]); PIN(pk2); SBAR();
      T0 = MFMA(k3, qf[3], T0); ADD2(S1[6], S1[7]); pk2[3] = cvtpk(S1[6], S1[7]); PIN(pk2); SBAR();
      HOOK0();
      T0 = MFMA(k4, qf[4], T0); k2 = KF(8); k3 = KF(9); ADD2(S1[8], S1[9]); pk3[0] = cvtpk(S1[8], S1[9]); PIN(pk3); SBAR();
      T0 = MFMA(k5, qf[5], T0); ADD2(S1[10], S1[11]); pk3[1] = cvtpk(S1[10], S1[11]); PIN(pk3); SBAR();
      T0 = MFMA(k0, qf[6], T0); k4 = KF(10); k5 = KF(11); ADD2(S1[12], S1[13]); pk3[2] = cvtpk(S1[12], S1[13]); PIN(pk3); SBAR();
      T0 = MFMA(k1, qf[7], T0); ADD2(S1[14], S1[15]); pk3[3] = cvtpk(S1[14], S1[15]); PIN(pk3); SBAR();
      HOOK1();
      l += sa + sb; PIN(pk2); PIN(pk3);
      T1 = MFMA(k2, qf[0], negm); k0 = KF(12); k1 = KF(13); SBAR();
      T1 = MFMA(k3, qf[1], T1); SBAR();
      T1 = MFMA(k4, qf[2], T1); k2 = KF(14); k3 = KF(15); SBAR();
      T1 = MFMA(k5, qf[3], T1); SBAR();
      HOOK2();
      ma = T0[0]; mb = T0[1];
      T1 = MFMA(k0, qf[4], T1); v0 = VF(0, 0); v1 = VF(1, 0); MX2(T0, 2); SBAR();
      T1 = MFMA(k1, qf[5], T1); MX2(T0, 6); SBAR();
      T1 = MFMA(k2, qf[6], T1); v2 = VF(2, 0); v3 = VF(3, 0); MX2(T0, 10); SBAR();
      T1 = MFMA(k3, qf[7], T1); ma = max3f(ma, T0[14], T0[15]); SBAR();
      HOOK3();
    }
    if (grp == 1 && g >= 0) {
      asm volatile("s_waitcnt vmcnt(0) lgkmcnt(0)\n\ts_barrier" ::: "memory");
      v0 = VF(0, 0); v1 = VF(1, 0); v2 = VF(2, 0); v3 = VF(3, 0);
      if (g < 15) PLAN(g + 2); else dfl = 0;
          } else dfl = 0;
    {
      const lds_cptr kp = (lds_cptr)lds + (lane * 16 + ((tc >> 8) & 15u) * 16384u);
      bf16x8 v4, v5;
      o[0] = MFMA(v0, BFR(pk0), o[0]); v4 = VF(0, 1); v5 = VF(1, 1); SBAR();
      o[1] = MFMA(v1, BFR(pk0), o[1]); SBAR();
      o[2] = MFMA(v2, BFR(pk0), o[2]); v0 = VF(2, 1); v1 = VF(3, 1); MX2(T1, 0); MX2(T1, 4); SBAR();
      o[3] = MFMA(v3, BFR(pk0), o[3]); MX2(T1, 8); MX2(T1, 12); SBAR();
      ma = max2f(ma, mb);
      bool resc = false; float fsc = 1.f;
      if (__builtin_expect(((tc >> 16) & 3u) != 0u || __any(ma > THR), 0)) {
        if ((tc >> 17) & 1u) {
          const float lfB = ((float)(((int)tc >> 24) * 32 + 64) + rl) * 1.0e30f;
#pragma unroll
          for (int i = 0; i < 16; ++i) { const int kk = (i & 3) + 8 * (i >> 2); float t0_, t1_;
            asm("v_add_f32_e32 %2, %5, %4\n\tv_add_f32_e32 %3, %6, %4\n\tv_min_f32_e32 %0, %0, %2\n\tv_min_f32_e32 %1, %1, %3"
                : "+v"(T0[i]), "+v"(T1[i]), "=&v"(t0_), "=&v"(t1_) : "v"(lfB), "i"(__builtin_bit_cast(int, -(float)(kk + 64) * 1.0e30f)), "i"(__builtin_bit_cast(int, -(float)(kk + 96) * 1.0e30f))); }
        }
        ma = T0[0]; mb = T0[1];
        MX2(T0, 2); MX2(T0, 6); MX2(T0, 10); ma = max3f(ma, T0[14], T0[15]); MX2(T1, 0); MX2(T1, 4); MX2(T1, 8); MX2(T1, 12);
        const float a = swap32_max(max2f(ma, mb));
        float dl;
        if (first_next) { dl = fmaxf(a, -1.0e29f); lB = l; l = 0.f; m = dl; }
        else { dl = fmaxf(a, 0.f); m += dl; fsc = EX(-dl); l *= fsc; resc = true; }
#pragma unroll
        for (int i = 0; i < 16; ++i) { T0[i] -= dl; T1[i] -= dl; negm[i] = -m; }
      }
      SBAR();
      float sa = 0.f, sb = 0.f;
#define EX4(X, i) do { X[i] = EX(X[i]); X[i + 1] = EX(X[i + 1]); X[i + 2] = EX(X[i + 2]); X[i + 3] = EX(X[i + 3]); PIN(X); } while (0)
#define EX2(X, i) do { X[i] = EX(X[i]); X[i + 1] = EX(X[i + 1]); PIN(X); } while (0)
      HOOK0();
      o[0] = MFMA(v4, BFR(pk1), o[0]); v2 = VF(0, 2); v3 = VF(1, 2); EX4(T0, 0); SBAR();
      o[1] = MFMA(v5, BFR(pk1), o[1]); EX4(T0, 4); SBAR();
      o[2] = MFMA(v0, BFR(pk1), o[2]); v4 = VF(2, 2); v5 = VF(3, 2); EX4(T0, 8); SBAR();
      o[3] = MFMA(v1, BFR(pk1), o[3]); EX4(T0, 12); SBAR();
      HOOK1();
      o[0] = MFMA(v2, BFR(pk2), o[0]); v0 = VF(0, 3); v1 = VF(1, 3); EX2(T1, 0); ADD2(T0[0], T0[1]); pk0[0] = cvtpk(T0[0], T0[1]); PIN(pk0); SBAR();
      o[1] = MFMA(v3, BFR(pk2), o[1]); EX2(T1, 2); ADD2(T0[2], T0[3]); pk0[1] = cvtpk(T0[2], T0[3]); PIN(pk0); SBAR();
      o[2] = MFMA(v4, BFR(pk2), o[2]); v2 = VF(2, 3); v3 = VF(3, 3); EX2(T1, 4); ADD2(T0[4], T0[5]); pk0[2] = cvtpk(T0[4], T0[5]); PIN(pk0); SBAR();
      o[3] = MFMA(v5, BFR(pk2), o[3]); EX2(T1, 6); ADD2(T0[6], T0[7]); pk0[3] = cvtpk(T0[6], T0[7]); PIN(pk0); SBAR();
      HOOK2();
      o[0] = MFMA(v0, BFR(pk3), o[0]); k0 = KF(0); k1 = KF(1); EX2(T1, 8); ADD2(T0[8], T0[9]); pk1[0] = cvtpk(T0[8], T0[9]); PIN(pk1); SBAR();
      o[1] = MFMA(v1, BFR(pk3), o[1]); EX2(T1, 10); ADD2(T0[10], T0[11]); pk1[1] = cvtpk(T0[10], T0[11]); PIN(pk1); SBAR();
      o[2] = MFMA(v2, BFR(pk3), o[2]); k2 = KF(2); k3 = KF(3); EX2(T1, 12); ADD2(T0[12], T0[13]); pk1[2] = cvtpk(T0[12], T0[13]); PIN(pk1); SBAR();
      o[3] = MFMA(v3, BFR(pk3), o[3]); EX2(T1, 14); ADD2(T0[14], T0[15]); pk1[3] = cvtpk(T0[14], T0[15]); PIN(pk1); SBAR();
      HOOK3();
      l += sa + sb;
      S1 = T1;
      if (__builtin_expect(resc, 0)) {
#pragma unroll
        for (int d = 0; d < 4; ++d)
#pragma unroll
          for (int i = 0; i < 16; ++i) o[d][i] *= fsc;
      }
      if (first_next && g >= 0) {
        const float inv = 1.0f / swap32_add(lB);
#pragma unroll
        for (int d = 0; d < 4; ++d) {
          float y[16];
#pragma unroll
          for (int i = 0; i < 16; ++i) y[i] = o[d][i] * inv;
          store_ctx(sCtx, y, bi * 8 + 2 * rg, h, d ^ 2, r, hi);
          o[d] = f32x16{};
        }
      }
      if (g == 10) {
        const int role = (int)(blockIdx.x & 7) >> 1, id = (int)(blockIdx.x >> 3) * 8 + wid;
        touch(role == 3 ? mk_srd(w2, 1376u * 512u * 2u) : mk_srd(wo, 4096u * 512u * 2u), (unsigned)lane * 128u, id < 176 ? (unsigned)((role == 3 ? 0 : role * 22 * 65536) + id * 8192) : OOBS);
      }
      if (grp == 1 && g == 14) {
        const unsigned src = (16u << 20) + (unsigned)(h * 524288 + (P1 + 2 * (16 - P1) + 1) * 16384 + rg * 4096), dst = lds0 + (unsigned)(L_KS2 + 16384 + rg * 4096);
        dma2<0>(sK, vL, src, dst); dma2<2048>(sK, vL, src, dst);
      }
    }
  }
  if (grp == 0) asm volatile("s_waitcnt vmcnt(0) lgkmcnt(0)\n\ts_barrier" ::: "memory");
    {
    const lds_cptr vp = (lds_cptr)lds + (lane * 16 + (grp ? L_KS2 + 16384 : L_V));
    const lds_cptr vpl = vp + grp * 8192, vpl_hi = vp + (8192 - grp * 8192);
#pragma unroll
    for (int i = 0; i < 16; ++i) l += S1[i];
    pk2 = pack8(S1, 0); pk3 = pack8(S1, 1);
#pragma unroll
    for (int d = 0; d < 4; ++d) { o[d] = MFMA(VF(d, 0), BFR(pk0), o[d]); o[d] = MFMA(VF(d, 1), BFR(pk1), o[d]); o[d] = MFMA(VF(d, 2), BFR(pk2), o[d]); o[d] = MFMA(VF(d, 3), BFR(pk3), o[d]); }
  }
  {
    l = swap32_add(l);
    char* mybuf = lds + grp * 32768 + rg * 8192;
    const char* pbuf = lds + (grp ^ 1) * 32768 + rg * 8192;
    float* ml = (float*)(lds + L_QA);
    asm volatile("s_waitcnt lgkmcnt(0)\n\ts_barrier" ::: "memory");
#pragma unroll
    for (int t = 0; t < 2; ++t)
#pragma unroll
      for (int q = 0; q < 4; ++q) {
        f32x4 v = {o[2 + t][4 * q], o[2 + t][4 * q + 1], o[2 + t][4 * q + 2], o[2 + t][4 * q + 3]};
        *(f32x4*)(mybuf + ((t * 4 + q) * 64 + lane) * 16) = v;
      }
    if (hi == 0) { ml[wid * 64 + r] = m; ml[wid * 64 + 32 + r] = l; }
    asm volatile("s_waitcnt lgkmcnt(0)\n\ts_barrier" ::: "memory");
    const int pw = wid ^ 4;
    const float m2 = ml[pw * 64 + r], l2 = ml[pw * 64 + 32 + r];
    const float mm = fmaxf(m, m2);
    const float f1 = EX(m - mm), f2 = EX(m2 - mm);
    const float inv = 1.0f / (l * f1 + l2 * f2);
    const float g1 = f1 * inv, g2 = f2 * inv;
#pragma unroll
    for (int t = 0; t < 2; ++t) {
      float y[16];
#pragma unroll
      for (int q = 0; q < 4; ++q) {
        const f32x4 v = *(const f32x4*)(pbuf + ((t * 4 + q) * 64 + lane) * 16);
#pragma unroll
        for (int e = 0; e < 4; ++e) y[4 * q + e] = o[t][4 * q + e] * g1 + v[e] * g2;
      }
      store_ctx(sCtx, y, (15 - bi) * 8 + 2 * rg, h, 2 * grp + t, r, hi);
    }
  }
#undef KF
#undef VF
}

typedef unsigned u32x2 __attribute__((ext_vector_type(2)));
DI void phaseD(char* lds, char* ws, const u16* x, const u16* wo, const u16* w2, u16* out, u64 tok, int h, int bi, int wid, int lane) {
  const int b = blockIdx.x, xcd = b & 7, jj = b >> 3, role = xcd >> 1, mt = (xcd & 1) * 8 + (jj >> 2), nt = jj & 3;
  const int c = lane & 15, g = lane >> 4, kgrp = wid >> 2, wr = (wid >> 1) & 1, wc = wid & 1;
  const unsigned lds0 = (unsigned)(uintptr_t)lds;
  const bool ish = role == 3;
  const int s0 = ish ? 0 : role * 22;
  constexpr int NS = 22;
  const __amdgpu_buffer_rsrc_t sA = ish ? mk_srd(ws + WS_H, 128u * 44u * 1024u) : mk_srd(ws + WS_CTX, 16u << 20);
  const __amdgpu_buffer_rsrc_t sW = ish ? mk_srd(w2, 1376u * 512u * 2u) : mk_srd(wo, 4096u * 512u * 2u);
  const unsigned vL = (unsigned)lane * 16u;
  const unsigned aRow = (unsigned)((mt * 8 + wid) * (ish ? 44 : 128) + s0 * 2) * 1024u;
  unsigned vW[2];
#pragma unroll
  for (int m = 0; m < 2; ++m) vW[m] = (unsigned)(((m * 4 + ((lane >> 2) & 3)) * 512 + (lane >> 4) * 32 + ((lane & 3) ^ (2 * m)) * 8) * 2);
  const unsigned wBase = (unsigned)((wid * 8 * 512 + nt * 128) * 2) + (unsigned)s0 * 65536u;
  const unsigned dw = lds0 + (unsigned)(wid * 2048);
  const unsigned aLate = role == 2 ? OOBS : aRow;
  auto pA = [&](int s, int m) { dma16(sA, vL, (s < 20 ? aRow : aLate) + (unsigned)(s * 2048 + m * 1024), dw + (unsigned)((s % 5) * 32768 + m * 1024)); };
  auto pW = [&](int s, int m) { dma16(sW, vW[m], wBase + (unsigned)s * 65536u, dw + (unsigned)((s % 5) * 32768 + 16384 + m * 1024)); };
  asm volatile("s_waitcnt lgkmcnt(0)\n\ts_barrier" ::: "memory");
#pragma unroll
  for (int s = 0; s < 5; ++s) { pW(s, 0); pW(s, 1); }
  wait_bar<10>();
  signal_w0(wid, 1u, flagp(ws, F_C + bi * 32 + h), tok); signal_w0(wid, 1u, flagp(ws, F_C + (15 - bi) * 32 + h), tok);
  { check_w0<false, false>(wid, flagp(ws, F_C + mt * 32), (unsigned)(lane & 31) * 8u, tok, ~0ull); }
  asm volatile("s_barrier" ::: "memory");
#pragma unroll
  for (int s = 0; s < 5; ++s) { pA(s, 0); pA(s, 1); }
  const int ab = (wr * 4 + 2 * kgrp) * 2048 + kgrp * 1024 + lane * 16, abx = (wr * 4 + 2 - 2 * kgrp) * 2048 + kgrp * 1024 + lane * 16;
  int wb[2][2];
  {
    const int kq0 = ish ? g : 4 * (g >> 1) + (g & 1), kst = ish ? 4 : 2;
#pragma unroll
    for (int par = 0; par < 2; ++par)
#pragma unroll
      for (int t = 0; t < 2; ++t) { const int kq = kgrp * 8 + kq0 + t * kst;
        wb[par][t] = 16384 + kq * 1024 + wc * 512 + ((lane >> 2) & 3) * 64 + ((par ^ (kq & 1)) * 32) + (lane & 3) * 8; }
  }
  f32x4 acc[4][4];
#pragma unroll
  for (int i = 0; i < 4; ++i)
#pragma unroll
    for (int j = 0; j < 4; ++j) acc[i][j] = f32x4{};
  bf16x8 af[2][4], wf[2][4];
  wait_bar<8>();
  {
    lds_cptr base = (lds_cptr)lds;
#pragma unroll
    for (int j = 0; j < 4; ++j) wf[0][j] = cat8(ldstr(base + wb[j & 1][0] + (j >> 1) * 256), ldstr(base + wb[j & 1][1] + (j >> 1) * 256));
#pragma unroll
    for (int i = 0; i < 4; ++i) af[0][i] = ldsr128(base + ((i < 2) ? ab : abx) + (i & 1) * 2048);
  }
#pragma unroll
  for (int s = 0; s < NS; ++s) {
    const int hf = s & 1;
    const bool nx = s + 1 < NS, pf = s + 5 < NS;
    if (nx) {
      constexpr int dummy = 0; (void)dummy;
      const int yo = (s < 3) ? (6 - 2 * s) + 4 * s : 12;
      const int tail = NS - 2 - s;
      const int n = (tail < 3) ? 4 * tail : yo;
      if (n == 12) wait_bar<12>(); else if (n == 10) wait_bar<10>(); else if (n == 8) wait_bar<8>(); else if (n == 6) wait_bar<6>(); else if (n == 4) wait_bar<4>(); else wait_bar<0>();
    }
    SBAR();
    lds_cptr base = (lds_cptr)lds + ((s + 1) % 5) * 32768;
    s16x4 wl[4], wh[4];
#define K2M(i, j) acc[i][j] = MFMA16(wf[hf][j], af[hf][i], acc[i][j])
#define PIN4(i) asm volatile("" : "+v"(acc[i][0]), "+v"(acc[i][1]), "+v"(acc[i][2]), "+v"(acc[i][3]))
    K2M(0, 0); if (nx) wl[0] = ldstr(base + wb[0][0]); SBAR();
    K2M(0, 1); if (nx) wh[0] = ldstr(base + wb[0][1]); SBAR();
    K2M(0, 2); if (nx) af[hf ^ 1][0] = ldsr128(base + ab); SBAR();
    K2M(0, 3); if (nx) wl[1] = ldstr(base + wb[1][0]); SBAR();
    PIN4(0); if (pf) pA(s + 5, 0); SBAR();
    K2M(1, 0); if (nx) wh[1] = ldstr(base + wb[1][1]); SBAR();
    K2M(1, 1); if (nx) wl[2] = ldstr(base + wb[0][0] + 256); SBAR();
    K2M(1, 2); if (nx) wh[2] = ldstr(base + wb[0][1] + 256); SBAR();
    K2M(1, 3); if (nx) wl[3] = ldstr(base + wb[1][0] + 256); SBAR();
    PIN4(1); if (pf) pA(s + 5, 1); SBAR();
    K2M(2, 0); if (nx) wh[3] = ldstr(base + wb[1][1] + 256); SBAR();
    K2M(2, 1); if (nx) af[hf ^ 1][1] = ldsr128(base + ab + 2048); SBAR();
    K2M(2, 2); if (nx) af[hf ^ 1][2] = ldsr128(base + abx); SBAR();
    K2M(2, 3); if (nx) af[hf ^ 1][3] = ldsr128(base + abx + 2048); SBAR();
    PIN4(2); if (pf) pW(s + 5, 0); SBAR();
    K2M(3, 0); K2M(3, 1); SBAR();
    if (pf) pW(s + 5, 1); SBAR();
    K2M(3, 2); K2M(3, 3); SBAR(); PIN4(3);
#undef K2M
    if (nx) {
#pragma unroll
      for (int j = 0; j < 4; ++j) wf[hf ^ 1][j] = cat8(wl[j], wh[j]);
    }
  }
  asm volatile("s_waitcnt vmcnt(0) lgkmcnt(0)\n\ts_barrier" ::: "memory");
  {
    char* mybuf = lds + wid * 8192;
    const char* pbuf = lds + (wid ^ 4) * 8192;
#pragma unroll
    for (int i = 0; i < 2; ++i)
#pragma unroll
      for (int j = 0; j < 4; ++j) *(f32x4*)(mybuf + ((i * 4 + j) * 64 + lane) * 16) = acc[2 + i][j];
    asm volatile("s_waitcnt lgkmcnt(0)\n\ts_barrier" ::: "memory");
#pragma unroll
    for (int i = 0; i < 2; ++i)
#pragma unroll
      for (int j = 0; j < 4; ++j) acc[i][j] += *(const f32x4*)(pbuf + ((i * 4 + j) * 64 + lane) * 16);
  }
  const int qd = 2 * wr + kgrp, tile = mt * 4 + nt;
  const __amdgpu_buffer_rsrc_t sP = mk_srd(ws + WS_PART, 16u << 20);
  f32x4 own[2];
#pragma unroll
  for (int j = 0; j < 4; ++j) {
    if (j != role) {
      const u32x4 w = {cvtpk(acc[0][j][0], acc[0][j][1]), cvtpk(acc[0][j][2], acc[0][j][3]), cvtpk(acc[1][j][0], acc[1][j][1]), cvtpk(acc[1][j][2], acc[1][j][3])};
      st16_wt(sP, (unsigned)((((tile * 4 + j) * 4 + role) * 8 + wid) * 1024) + vL, w);
    } else { own[0] = acc[0][j]; own[1] = acc[1][j]; }
  }
  u32x2 xv[2];
  const size_t obase = (size_t)(mt * 128 + qd * 32 + c) * 512 + nt * 128 + wc * 64 + role * 16 + 4 * g;
#pragma unroll
  for (int i = 0; i < 2; ++i) xv[i] = *(const u32x2*)(x + obase + i * 16 * 512);
  asm volatile("s_waitcnt vmcnt(0)\n\ts_barrier" ::: "memory");
  signal_w0(wid, 1u, flagp(ws, F_P + tile * 4 + role), tok);
  { check_w0<false, false>(wid, flagp(ws, F_P + tile * 4), (unsigned)(lane & 3) * 8u, tok, ~0ull); }
  asm volatile("s_barrier" ::: "memory");
  f32x4 p[3][2];
#pragma unroll
  for (int rr = 0; rr < 3; ++rr) {
    const int src = (role + 1 + rr) & 3;
    const u32x4 w = __builtin_amdgcn_raw_buffer_load_b128(sP, (unsigned)((((tile * 4 + role) * 4 + src) * 8 + wid) * 1024) + vL, 0, 16);
#pragma unroll
    for (int i = 0; i < 2; ++i) p[rr][i] = (f32x4){__uint_as_float(w[2 * i] << 16), __uint_as_float(w[2 * i] & 0xffff0000u), __uint_as_float(w[2 * i + 1] << 16), __uint_as_float(w[2 * i + 1] & 0xffff0000u)};
  }
#pragma unroll
  for (int i = 0; i < 2; ++i) {
    f32x4 ya = ish ? own[i] : (role == 2 ? p[0][i] : (role == 1 ? p[1][i] : p[2][i]));
    f32x4 sa = (own[i] + p[0][i]) + (p[1][i] + p[2][i]) - ya;
    {
      const f32x4 z = {};
      sa = (ish ? z : own[i]) + (role == 2 ? z : p[0][i]) + (role == 1 ? z : p[1][i]) + (role == 0 ? z : p[2][i]);
    }
    float o4[4];
#pragma unroll
    for (int e = 0; e < 4; ++e) {
      const float y = rbf(ya[e]);
      const float gt = rbf(y * __builtin_amdgcn_rcpf(1.f + __builtin_amdgcn_exp2f(-1.4426950408889634f * y)));
      const float sg = rbf(rbf(sa[e]) * gt);
      const float xe = (e & 1) ? __uint_as_float(xv[i][e >> 1] & 0xffff0000u) : __uint_as_float(xv[i][e >> 1] << 16);
      o4[e] = xe + sg;
    }
    u32x2 w = {cvtpk(o4[0], o4[1]), cvtpk(o4[2], o4[3])};
    *(u32x2*)(out + obase + i * 16 * 512) = w;
  }
}

__global__ __launch_bounds__(512, 2) __attribute__((amdgpu_num_vgpr(252))) void fused_block(const u16* __restrict__ x, const u16* __restrict__ ckv, const u16* __restrict__ wq, const u16* __restrict__ wuk, const u16* __restrict__ wuv,
                                                      const u16* __restrict__ wo, const u16* __restrict__ w1, const u16* __restrict__ w2, u16* __restrict__ out, char* __restrict__ ws) {
  extern __shared__ __attribute__((aligned(16))) char lds[];
  const int tid = threadIdx.x, lane = tid & 63, wid = __builtin_amdgcn_readfirstlane(tid >> 6);
  const int b = blockIdx.x, xcd = b & 7, jj = b >> 3, h = 4 * xcd + (jj >> 3), mt = jj & 7, bi = mt;
  const __amdgpu_buffer_rsrc_t sC = mk_srd(ckv, 2048u * 512u * 2u), sUK = mk_srd(wuk, 512u * 4096u * 2u), sUV = mk_srd(wuv, 512u * 4096u * 2u);
  const int R = b * 8 + wid;
  u32x4 xr;
  const unsigned vC = (unsigned)((lane >> 2) * 1024 + (((lane & 3) ^ (3 * ((lane >> 5) & 1))) * 16));
  const unsigned vW = (unsigned)((((lane >> 2) & 3) * 4096 + (lane >> 4) * 32 + ((lane & 3) ^ (2 * ((wid >> 1) & 1))) * 8) * 2);
  const unsigned sCbase = (unsigned)((mt * 256 + wid * 32) * 1024);
  const unsigned sWbase = (unsigned)((wid * 4 * 4096 + h * 128) * 2);
  auto st0 = [&](int s) {
    const unsigned bb = (unsigned)(uintptr_t)lds + (unsigned)(PA_BASE + s * 32768);
#pragma unroll
    for (int m = 0; m < 2; ++m) dma16(sC, vC, sCbase + (unsigned)(m * 16 * 1024 + s * 64), bb + (unsigned)((wid * 2 + m) * 1024));
#pragma unroll
    for (int m = 0; m < 2; ++m) dma16(m ? sUV : sUK, vW, sWbase + (unsigned)((s * 32 * 4096) * 2), bb + 16384u + (unsigned)((wid * 2 + m) * 1024));
  };
  { const u16* xp = x + (size_t)R * 512 + lane * 8; asm volatile("global_load_dwordx4 %0, %1, off" : "=v"(xr) : "v"(xp) : "memory"); }
  st0(0); st0(1);
  const u64 tok = ((hw_dispatch_id() + 1ull) * 0x9E3779B97F4A7C15ull) ^ ((u64)(uintptr_t)__builtin_amdgcn_dispatch_ptr() << 7) ^ 0x5bd1e9955bd1e995ull;
  PBsrc pb;
  pb.sX = mk_srd(ws + WS_XN, 2048u * 512u * 2u); pb.sQ = mk_srd(wq, 512u * 4096u * 2u); pb.s1 = mk_srd(w1, 512u * 1376u * 2u);
  pb.vL = pb.vQ = pb.v1 = 0u;
  pb.sXbase = (unsigned)(((wid < 4) ? (15 - bi) * 8 : bi * 8 - 8) + 2 * wid) * 16384u;
  pb.sQbase = sWbase; pb.s1base = h < 22 ? (unsigned)((wid * 4 * 1376 + h * 64) * 2) : OOBS;
  {
    asm volatile("s_waitcnt vmcnt(8)" : "+v"(xr) :: "memory");
    st0(2); st0(3);
    float v[8];
#pragma unroll
    for (int e = 0; e < 4; ++e) { v[2 * e] = __uint_as_float(xr[e] << 16); v[2 * e + 1] = __uint_as_float(xr[e] & 0xffff0000u); }
    float s = 0;
#pragma unroll
    for (int e = 0; e < 8; ++e) s += v[e];
#pragma unroll
    for (int o = 32; o; o >>= 1) s += __shfl_xor(s, o);
    const float mean = s * (1.f / 512.f);
    float q = 0;
#pragma unroll
    for (int e = 0; e < 8; ++e) { const float d = v[e] - mean; q += d * d; }
#pragma unroll
    for (int o = 32; o; o >>= 1) q += __shfl_xor(q, o);
    const float mb = rbf(mean), vb = rbf(q * (1.f / 512.f));
    const float den = rbf(__builtin_sqrtf(rbf(vb + 1e-5f)));
    float o8[8];
#pragma unroll
    for (int e = 0; e < 8; ++e) o8[e] = rbf(v[e] - mb) / den;
    u32x4 w = {cvtpk(o8[0], o8[1]), cvtpk(o8[2], o8[3]), cvtpk(o8[4], o8[5]), cvtpk(o8[6], o8[7])};
    st16_wt(pb.sX, (unsigned)((((R >> 4) * 16 + (lane >> 2)) * 64 + ((lane & 3) * 16 + (R & 15))) * 16), w);
  }
  {
    const int id = jj * 8 + wid;
    touch(sC, (unsigned)lane * 128u, (unsigned)id * 8192u);
    unsigned long long pcv; unsigned len;
    asm volatile("s_getpc_b64 %0\n.Lfb_pc:\n\ts_mov_b32 %1, .Lfb_end-.Lfb_pc" : "=s"(pcv), "=s"(len));
    const bool w = id < 128;
    touch(w ? ((id & 64) ? sUV : sUK) : mk_srd((const char*)(pcv & ~127ull), len), w ? (unsigned)(lane >> 3) * 8192u + (unsigned)(lane & 7) * 128u : (unsigned)lane * 128u,
          w ? (unsigned)((id & 63) * 8 * 8192 + xcd * 1024) : (unsigned)(id - 128) * 8192u);
  }
  const __amdgpu_buffer_rsrc_t sK = mk_srd(ws + WS_KF, 32u << 20);
  u32x4 kv[16];
  if (wid < 4) phaseA<true>(lds, ws, sC, sUK, sUV, sCbase, sWbase, pb, tok, h, mt, wid, lane_id(), kv);
  else         phaseA<false>(lds, ws, sC, sUK, sUV, sCbase, sWbase, pb, tok, h, mt, wid, lane_id(), kv);
  if (h < 22) phaseB<true>(lds, ws, pb, sK, tok, h, bi, wid, lane_id(), kv);
  else        phaseB<false>(lds, ws, pb, sK, tok, h, bi, wid, lane_id(), kv);
  phaseC(lds, ws, sK, wo, w2, tok, h, bi, wid, lane_id());
  phaseD(lds, ws, x, wo, w2, out, tok, h, bi, wid, lane_id());
  asm volatile(".Lfb_end:");
}

extern "C" void kernel_launch(void* const* d_in, const int* in_sizes, int n_in, void* d_out, int out_size, void* d_ws, size_t ws_size, hipStream_t stream) {
  (void)in_sizes; (void)n_in; (void)out_size; (void)ws_size;
  (void)hipFuncSetAttribute((const void*)fused_block, hipFuncAttributeMaxDynamicSharedMemorySize, 163840);
  fused_block<<<256, 512, 163840, stream>>>((const u16*)d_in[0], (const u16*)d_in[1], (const u16*)d_in[2], (const u16*)d_in[3], (const u16*)d_in[4], (const u16*)d_in[5],
                                            (const u16*)d_in[7], (const u16*)d_in[8], (u16*)d_out, (char*)d_ws);
}
```

```cpp
#include <hip/hip_runtime.h>
#include <hip/hip_bf16.h>
#include <stdint.h>

typedef unsigned short u16;
typedef short bf16x8 __attribute__((ext_vector_type(8)));
typedef short s16x4 __attribute__((ext_vector_type(4)));
typedef float f32x16 __attribute__((ext_vector_type(16)));
typedef float f32x4 __attribute__((ext_vector_type(4)));
typedef float f32x2 __attribute__((ext_vector_type(2)));
typedef __bf16 bf16x2_t __attribute__((ext_vector_type(2)));
typedef unsigned u32x4 __attribute__((ext_vector_type(4)));
#define LAS __attribute__((address_space(3)))
typedef LAS const char* lds_cptr;
#define DI __device__ __forceinline__
#define MFMA(a, b, c) __builtin_amdgcn_mfma_f32_32x32x16_bf16((a), (b), (c), 0, 0, 0)
#define SBAR() __builtin_amdgcn_sched_barrier(0)

constexpr size_t WS_KF = 0;
constexpr size_t WS_VF = 16u << 20;
constexpr size_t WS_CTX = 32u << 20;
constexpr size_t WS_XN = 48u << 20;
constexpr size_t WS_H = 50u << 20;
constexpr size_t WS_PART = 56u << 20;
constexpr size_t WS_FLAG = 80u << 20;

DI unsigned cvtpk(float lo, float hi) { f32x2 v = {lo, hi}; bf16x2_t b = __builtin_convertvector(v, bf16x2_t); return __builtin_bit_cast(unsigned, b); }
DI float bf2f(unsigned v16) { return __uint_as_float(v16 << 16); }
DI float rbf(float f) { return bf2f(cvtpk(f, f) & 0xffffu); }
DI u32x4 pack8(const f32x16& x, int s) {
  u32x4 p = {cvtpk(x[8 * s], x[8 * s + 1]), cvtpk(x[8 * s + 2], x[8 * s + 3]), cvtpk(x[8 * s + 4], x[8 * s + 5]), cvtpk(x[8 * s + 6], x[8 * s + 7])};
  return p;
}
DI __amdgpu_buffer_rsrc_t mk_srd(const void* p, unsigned bytes) { return __builtin_amdgcn_make_buffer_rsrc((void*)p, (short)0, (int)bytes, 0x00020000); }
DI void dma16(__amdgpu_buffer_rsrc_t srd, unsigned voff, unsigned soff, unsigned lds_dst) {
  unsigned keep;
  asm volatile("s_mov_b32 %0, m0\n\ts_mov_b32 m0, %1\n\ts_nop 0\n\tbuffer_load_dwordx4 %2, %3, %4 offen lds\n\ts_mov_b32 m0, %0"
               : "=&s"(keep) : "s"(lds_dst), "v"(voff), "s"(srd), "s"(soff) : "memory");
}
DI bf16x8 ldsr128(lds_cptr p) { return *(const LAS bf16x8*)p; }
typedef short v4i16_t __attribute__((ext_vector_type(4)));
DI s16x4 ldstr(lds_cptr p) { return __builtin_bit_cast(s16x4, __builtin_amdgcn_ds_read_tr16_b64_v4i16((LAS v4i16_t*)p)); }
DI bf16x8 cat8(s16x4 a, s16x4 b) { return __builtin_shufflevector(a, b, 0, 1, 2, 3, 4, 5, 6, 7); }


template <int N> DI void wait_bar() { asm volatile("s_waitcnt vmcnt(%0) lgkmcnt(0)\n\ts_barrier" :: "i"(N) : "memory"); }
#define MFMA16(a, b, c) __builtin_amdgcn_mfma_f32_16x16x32_bf16((a), (b), (c), 0, 0, 0)
#define SGB(mask, n) __builtin_amdgcn_sched_group_barrier(mask, n, 0)
constexpr unsigned OOBS = 0x7f000000u, DROP = 0x38000u;
typedef unsigned long long u64;
extern "C" __device__ u64 hw_dispatch_id() __asm("llvm.amdgcn.dispatch.id");
constexpr int F_X = 0, F_KV = 256, F_C = 512, F_P = 1024;
DI int lane_id() { int l; asm volatile("v_mbcnt_lo_u32_b32 %0, -1, 0\n\tv_mbcnt_hi_u32_b32 %0, -1, %0" : "=v"(l)); return l; }
DI u64* flagp(char* ws, int i) { return (u64*)(ws + WS_FLAG) + i; }
DI void st16_wt(__amdgpu_buffer_rsrc_t srd, unsigned off, u32x4 w) { __builtin_amdgcn_raw_buffer_store_b128(w, srd, off, 0, 16); }
constexpr int L_V = 0, L_QA = 65536, L_QB = 98304, L_KS2 = 98304, L_KS0 = 131072;
constexpr float QSCALE = 0.08838834764831845f * 1.4426950408889634f;
constexpr float THR = 8.0f;
#define PIN(x) asm volatile("" : "+v"(x))
DI float max3f(float a, float b, float c) { float r; asm("v_max3_f32 %0, %1, %2, %3" : "=v"(r) : "v"(a), "v"(b), "v"(c)); return r; }
DI float max2f(float a, float b) { float r; asm("v_max_f32_e32 %0, %1, %2" : "=v"(r) : "v"(a), "v"(b)); return r; }
DI float fadd_s(float a, float b) { float r; asm("v_add_f32_e32 %0, %1, %2" : "=v"(r) : "v"(a), "v"(b)); return r; }
DI float swap32_max(float m) { auto rr = __builtin_amdgcn_permlane32_swap(__float_as_uint(m), __float_as_uint(m), false, false); return fmaxf(__uint_as_float(rr[0]), __uint_as_float(rr[1])); }
DI float swap32_add(float m) { auto rr = __builtin_amdgcn_permlane32_swap(__float_as_uint(m), __float_as_uint(m), false, false); return __uint_as_float(rr[0]) + __uint_as_float(rr[1]); }


constexpr int PB_STAGE = 32768;
DI constexpr int pb_slot(int s) { return (s & 3) * PB_STAGE; }
constexpr int PA_BASE = 32768;
struct PBsrc { __amdgpu_buffer_rsrc_t sX, sQ, s1; unsigned vL, vQ, v1, sXbase, sQbase, s1base; };
DI void pb_lanes(PBsrc& p, int wid, int lane) {
  const int c4 = (lane & 3) ^ (2 * ((wid >> 1) & 1));
  p.vL = (unsigned)lane * 16u;
  p.vQ = (unsigned)((((lane >> 2) & 3) * 4096 + (lane >> 4) * 32 + c4 * 8) * 2);
  p.v1 = lane < 32 ? (unsigned)((((lane >> 2) & 3) * 1376 + (lane >> 4) * 32 + c4 * 8) * 2) : OOBS;
}
DI void pb_piece(const PBsrc& p, unsigned lds0, int wid, int s, int m) {
  const unsigned b = lds0 + (unsigned)pb_slot(s);
  if (m < 2) dma16(p.sX, p.vL, p.sXbase + (unsigned)(m * 16384 + s * 1024), b + (unsigned)((wid * 2 + m) * 1024));
  else if (m == 2) dma16(p.sQ, p.vQ, p.sQbase + (unsigned)(s * 32 * 4096 * 2), b + 16384u + (unsigned)(wid * 2048));
  else dma16(p.s1, p.v1, p.s1base + (unsigned)(s * 32 * 1376 * 2), b + 16384u + 1024u + (unsigned)(wid * 2048));
}
DI void dma16_nz(int wid, __amdgpu_buffer_rsrc_t srd, unsigned voff, unsigned soff, unsigned lds_dst) {
  asm volatile("s_cmp_eq_u32 %4, 0\n\ts_cbranch_scc1 1f\n\ts_mov_b32 m0, %0\n\ts_nop 0\n\tbuffer_load_dwordx4 %1, %2, %3 offen lds\n1:"
               :: "s"(lds_dst), "v"(voff), "s"(srd), "s"(soff), "s"(wid) : "memory", "scc");
}
template <int N0, int N> DI void wait_bar2(int wid) {
  asm volatile("s_cmp_eq_u32 %0, 0\n\ts_cbranch_scc1 1f\n\ts_waitcnt vmcnt(%2)\n\ts_branch 2f\n1:\n\ts_waitcnt vmcnt(%1)\n2:\n\ts_waitcnt lgkmcnt(0)\n\ts_barrier" :: "s"(wid), "i"(N0), "i"(N) : "memory", "scc");
}
DI void signal_w0(int wid, unsigned en, u64* p, u64 tok) {
  unsigned z = 0u;
  asm volatile("s_cmp_eq_u32 %0, 0\n\ts_cbranch_scc0 1f\n\ts_cmp_eq_u32 %4, 0\n\ts_cbranch_scc1 1f\n\ts_mov_b64 exec, 1\n\tglobal_store_dwordx2 %2, %3, %1 sc1\n\ts_mov_b64 exec, -1\n1:"
               :: "s"(wid), "s"(p), "v"(z), "v"(tok), "s"(en) : "memory", "scc");
}
DI void poll_w0(int wid, const u64* p, unsigned voff) {
  asm volatile("s_cmp_eq_u32 %0, 0\n\ts_cbranch_scc0 1f\n\tglobal_load_dwordx2 v[252:253], %2, %1 sc1\n1:" :: "s"(wid), "s"(p), "v"(voff) : "memory", "scc", "v252", "v253"); }
template <bool INV, bool POLLED> DI void check_w0(int wid, const u64* p, unsigned voff, u64 tok, u64 need) {
  unsigned cnt; u64 m;
  asm volatile("s_cmp_eq_u32 %2, 0\n\ts_cbranch_scc0 9f\n\ts_mov_b32 %1, 0x40000\n\t.if %8 == 0\n\ts_branch 3f\n\t.endif\n"
               "1:\n\tv_cmp_ne_u64_e64 %0, v[252:253], %5\n\ts_and_b64 %0, %0, %6\n\ts_cbranch_scc0 2f\n\ts_sub_u32 %1, %1, 1\n\ts_cmp_eq_u32 %1, 0\n\ts_cbranch_scc1 2f\n\ts_sleep 1\n"
               "3:\n\tglobal_load_dwordx2 v[252:253], %4, %3 sc1\n\ts_waitcnt vmcnt(0)\n\ts_branch 1b\n"
               "2:\n\t.if %7\n\tbuffer_inv sc1\n\t.endif\n9:"
               : "=&s"(m), "=&s"(cnt) : "s"(wid), "s"(p), "v"(voff), "v"(tok), "s"(need), "i"(INV ? 1 : 0), "i"(POLLED ? 1 : 0) : "memory", "scc", "v252", "v253");
}
template <int NF, int NS_> DI void wait_barf(unsigned fast) {
  asm volatile("s_cmp_eq_u32 %0, 0\n\ts_cbranch_scc1 1f\n\ts_waitcnt vmcnt(%1)\n\ts_branch 2f\n1:\n\ts_waitcnt vmcnt(%2)\n2:\n\ts_waitcnt lgkmcnt(0)\n\ts_barrier" :: "s"(fast), "i"(NF), "i"(NS_) : "memory", "scc");
}
DI void st16_if(unsigned en, u32x4 w, __amdgpu_buffer_rsrc_t srd, unsigned voff, unsigned soff) {
  asm volatile("s_cmp_eq_u32 %4, 0\n\ts_cbranch_scc1 1f\n\tbuffer_store_dwordx4 %0, %1, %2, %3 offen sc1\n\ts_nop 1\n1:" :: "v"(w), "v"(voff), "s"(srd), "s"(soff), "s"(en) : "memory", "scc");
}
DI void touch(__amdgpu_buffer_rsrc_t srd, unsigned voff, unsigned soff) {
  asm volatile("buffer_load_dword v254, %0, %1, %2 offen" :: "v"(voff), "s"(srd), "s"(soff) : "memory", "v254");
}

template <bool KT>
DI void phaseA(char* lds, char* ws, __amdgpu_buffer_rsrc_t sC, __amdgpu_buffer_rsrc_t sUK, __amdgpu_buffer_rsrc_t sUV, unsigned sCbase, unsigned sWbase,
               PBsrc pb, u64 tok, int h, int mt, int wid, int lane, u32x4 (&kv)[16]) {
  const int c = lane & 15, g = lane >> 4, km = wid & 1, nq = wid >> 1;
  const unsigned vC = (unsigned)((lane >> 2) * 1024 + (((lane & 3) ^ (3 * ((lane >> 5) & 1))) * 16));
  const unsigned vW = (unsigned)((((lane >> 2) & 3) * 4096 + (lane >> 4) * 32 + ((lane & 3) ^ (2 * ((wid >> 1) & 1))) * 8) * 2);
  const unsigned lds0 = (unsigned)(uintptr_t)lds;
  auto piece = [&](int s, int m) {
    if (s < 16) {
      const unsigned b = lds0 + (unsigned)(PA_BASE + (s & 3) * 32768);
      if (m < 2) dma16(sC, vC, sCbase + (unsigned)(m * 16 * 1024 + s * 64), b + (unsigned)((wid * 2 + m) * 1024));
      else       dma16((m & 1) ? sUV : sUK, vW, sWbase + (unsigned)((s * 32 * 4096) * 2), b + 16384u + (unsigned)((wid * 2 + (m & 1)) * 1024));
    } else pb_piece(pb, lds0, wid, s - 16, m);
  };
  auto warmB = [&]() {
    const int l2 = lane_id(), id = (int)(blockIdx.x >> 3) * 8 + wid, xg = (int)(blockIdx.x & 7);
    const bool q = id < 64;
    const unsigned vo = (unsigned)(l2 >> 3) * (q ? 8192u : 2752u) + (unsigned)(l2 & 7) * 128u;
    const unsigned so = q ? (unsigned)(id * 8 * 8192 + xg * 1024) : (id < 128 ? (unsigned)((id - 64) * 8 * 2752 + xg * 512) : OOBS);
    touch(q ? pb.sQ : pb.s1, vo, so);
  };
  const int cb = (128 * km + c) * 64 + ((g ^ (3 * ((c >> 3) & 1))) * 16);
  int wb[2];
#pragma unroll
  for (int par = 0; par < 2; ++par) wb[par] = 16384 + g * 4096 + nq * 512 + ((lane >> 2) & 3) * 64 + ((par ^ (g & 1)) * 32) + (lane & 3) * 8;
  f32x4 acc[8][4];
#pragma unroll
  for (int i = 0; i < 8; ++i)
#pragma unroll
    for (int j = 0; j < 4; ++j) acc[i][j] = f32x4{};
  bf16x8 cf[2][4], wf[2][4];
  auto rdW = [&](int s, int buf) {
    lds_cptr base = (lds_cptr)lds + (PA_BASE + (s & 3) * 32768);
#pragma unroll
    for (int j = 0; j < 4; ++j) wf[buf][j] = cat8(ldstr(base + wb[j & 1] + (j >> 1) * 256), ldstr(base + wb[j & 1] + 2048 + (j >> 1) * 256));
  };
  auto rdC = [&](int s, int half, int buf) {
    lds_cptr base = (lds_cptr)lds + (PA_BASE + (s & 3) * 32768);
#pragma unroll
    for (int i = 0; i < 4; ++i) cf[buf][i] = ldsr128(base + cb + (half * 4 + i) * 1024);
  };
  auto mm1 = [&](int wbuf, int cbuf, int half, int i) {
#pragma unroll
    for (int j = 0; j < 4; ++j) acc[half * 4 + i][j] = KT ? MFMA16(wf[wbuf][j], cf[cbuf][i], acc[half * 4 + i][j]) : MFMA16(cf[cbuf][i], wf[wbuf][j], acc[half * 4 + i][j]);
  };
  auto pin4 = [&](int half) {
#pragma unroll
    for (int i = 0; i < 4; ++i) asm volatile("" : "+v"(acc[half * 4 + i][0]), "+v"(acc[half * 4 + i][1]), "+v"(acc[half * 4 + i][2]), "+v"(acc[half * 4 + i][3]));
  };
  const int bi = mt;
  wait_bar<16>();
  rdW(0, 0); rdC(0, 0, 0);
#pragma unroll
  for (int s = 0; s < 16; ++s) {
    const int wbuf = s & 1;
    rdC(s, 1, 1);
    mm1(wbuf, 0, 0, 0); mm1(wbuf, 0, 0, 1); mm1(wbuf, 0, 0, 2); mm1(wbuf, 0, 0, 3);
    SGB(0x008, 2); SGB(0x100, 1); SGB(0x008, 2); SGB(0x100, 1); SGB(0x008, 2); SGB(0x100, 1); SGB(0x008, 2); SGB(0x100, 1); SGB(0x008, 8); SBAR(); pin4(0);
    if (KT && s == 10) { const int l2 = lane_id(); check_w0<true, true>(wid, flagp(ws, F_X), (unsigned)((((l2 & 16) ? 15 - bi : bi) * 16 + (l2 & 15)) * 8), tok, ~0ull); }
    if (s < 15) {
      if (s <= 2) wait_bar<12>(); else if (s == 5 || s == 6 || s == 7) wait_bar<9>(); else wait_bar<8>();
      SBAR();
      if (s == 12) pb_lanes(pb, wid, lane_id());
      if (KT && s == 4) signal_w0(wid, 1u, flagp(ws, F_X + blockIdx.x), tok);
      if (KT && s == 6) { const int l2 = lane_id(); poll_w0(wid, flagp(ws, F_X), (unsigned)((((l2 & 16) ? 15 - bi : bi) * 16 + (l2 & 15)) * 8)); }
      rdW(s + 1, wbuf ^ 1); rdC(s + 1, 0, 0);
      mm1(wbuf, 1, 1, 0); SGB(0x008, 1); SGB(0x100, 3); SGB(0x008, 1); SGB(0x100, 3); SGB(0x008, 1); SGB(0x100, 3); SGB(0x008, 1); SGB(0x100, 3); SBAR(); piece(s + 4, 0); SBAR();
      mm1(wbuf, 1, 1, 1); SBAR(); piece(s + 4, 1); SBAR();
      mm1(wbuf, 1, 1, 2); SBAR(); piece(s + 4, 2); SBAR();
      mm1(wbuf, 1, 1, 3); SBAR(); pin4(1); piece(s + 4, 3); if (s == 4) warmB(); SBAR();
    } else {
      mm1(wbuf, 1, 1, 0); SBAR(); piece(19, 0); SBAR(); mm1(wbuf, 1, 1, 1); SBAR(); piece(19, 1); SBAR(); mm1(wbuf, 1, 1, 2); SBAR(); piece(19, 2); SBAR(); mm1(wbuf, 1, 1, 3); SBAR(); piece(19, 3); SBAR();
    }
  }
  if (KT) {
#pragma unroll
    for (int i = 0; i < 8; ++i)
#pragma unroll
      for (int dp = 0; dp < 2; ++dp)
        kv[i * 2 + dp] = (u32x4){cvtpk(acc[i][2 * dp][0], acc[i][2 * dp][1]), cvtpk(acc[i][2 * dp][2], acc[i][2 * dp][3]), cvtpk(acc[i][2 * dp + 1][0], acc[i][2 * dp + 1][1]), cvtpk(acc[i][2 * dp + 1][2], acc[i][2 * dp + 1][3])};
  } else {
#pragma unroll
    for (int ip = 0; ip < 4; ++ip)
#pragma unroll
      for (int j = 0; j < 4; ++j) {
        unsigned a0 = cvtpk(acc[2 * ip][j][0], acc[2 * ip][j][1]), a1 = cvtpk(acc[2 * ip][j][2], acc[2 * ip][j][3]);
        unsigned b0 = cvtpk(acc[2 * ip + 1][j][0], acc[2 * ip + 1][j][1]), b1 = cvtpk(acc[2 * ip + 1][j][2], acc[2 * ip + 1][j][3]);
        auto r0 = __builtin_amdgcn_permlane32_swap(a0, b0, false, false);
        auto r1 = __builtin_amdgcn_permlane32_swap(a1, b1, false, false);
        kv[ip * 4 + j] = (u32x4){r0[0], r1[0], r0[1], r1[1]};
      }
  }
}

template <bool FFN>
DI void phaseB(char* lds, char* ws, PBsrc pb, __amdgpu_buffer_rsrc_t sK, u64 tok, int h, int bi, int wid, int lane, const u32x4 (&kv)[16]) {
  pb_lanes(pb, wid, lane);
  const int c = lane & 15, g = lane >> 4, rgp = wid >> 1, ch = wid & 1;
  const unsigned lds0 = (unsigned)(uintptr_t)lds;
  constexpr int NP = 4;
  auto kvpiece = [&](unsigned base, int tile, int ldsoff, int m) {
    dma16(sK, pb.vL, base + (unsigned)(h * 524288 + tile * 16384 + (wid * 2 + m) * 1024), lds0 + (unsigned)(ldsoff + (wid * 2 + m) * 1024));
  };
  const int v0slot = L_V + ((0 - (2 * bi + 2)) & 3) * 16384;
  constexpr int NJ = FFN ? 6 : 4;
  const int xb = rgp * 4096 + lane * 16;
  int wb[2];
#pragma unroll
  for (int par = 0; par < 2; ++par) wb[par] = 16384 + g * 4096 + ((lane >> 2) & 3) * 64 + ((par ^ (g & 1)) * 32) + (lane & 3) * 8;
  f32x4 acc[4][NJ];
#pragma unroll
  for (int i = 0; i < 4; ++i)
#pragma unroll
    for (int j = 0; j < NJ; ++j) acc[i][j] = f32x4{};
  bf16x8 xf[2][2], wf[2][NJ];
  auto rdW = [&](int s, int buf) {
    lds_cptr base = (lds_cptr)lds + pb_slot(s);
#pragma unroll
    for (int j = 0; j < NJ; ++j) { const int u = (j < 4) ? (2 * ch + (j >> 1)) : (4 + ch);
      wf[buf][j] = cat8(ldstr(base + wb[j & 1] + u * 256), ldstr(base + wb[j & 1] + 2048 + u * 256)); }
  };
  auto rdX = [&](int s, int half, int buf) {
    lds_cptr base = (lds_cptr)lds + pb_slot(s);
#pragma unroll
    for (int i = 0; i < 2; ++i) xf[buf][i] = ldsr128(base + xb + (half * 2 + i) * 1024);
  };
  auto mm1 = [&](int wbuf, int xbuf, int half, int i) {
#pragma unroll
    for (int j = 0; j < NJ; ++j) acc[half * 2 + i][j] = MFMA16(wf[wbuf][j], xf[xbuf][i], acc[half * 2 + i][j]);
  };
  auto pin1 = [&](int i) {
#pragma unroll
    for (int j = 0; j < NJ; j += 2) asm volatile("" : "+v"(acc[i][j]), "+v"(acc[i][j + 1]));
  };
  const int km = wid & 1, nq = wid >> 1;
  const bool kt = wid < 4;
  const unsigned kvV = (unsigned)((g >> 1) * 1024 + ((g & 1) * 32 + c) * 16);
  const unsigned kvS = kt ? (unsigned)(((h * 32 + bi * 4 + 2 * km) * 16 + 4 * nq) * 1024) : (16u << 20) + (unsigned)(((h * 32 + bi * 4 + 2 * km) * 16 + 8 * (nq - 2)) * 1024);
  const unsigned fast = (unsigned)__builtin_amdgcn_readfirstlane((int)((unsigned)(bi - 2) >> 31)), slow = (unsigned)__builtin_amdgcn_readfirstlane((int)(fast ^ 1u));
  auto kvstore = [&](int n, unsigned en) {
    const int i = n >> 1, dp = n & 1, ip = n >> 2, j = n & 3;
    const unsigned ok = (unsigned)(((i >> 2) * 16 + ((i >> 1) & 1) * 8 + 2 * dp) * 1024 + (i & 1) * 256);
    const unsigned ov = (unsigned)(((ip >> 1) * 16 + (j >> 1) * 4 + 2 * (ip & 1)) * 1024 + (j & 1) * 256);
    st16_if(en, kv[n], sK, kvV, kvS + (kt ? ok : ov));
  };
  wait_bar<12>();
  rdW(0, 0); rdX(0, 0, 0);
#pragma unroll
  for (int s = 0; s < 16; ++s) {
    const int wbuf = s & 1;
    rdX(s, 1, 1);
    mm1(wbuf, 0, 0, 0); mm1(wbuf, 0, 0, 1);
    SGB(0x008, 1); SGB(0x100, 1); SGB(0x008, 1); SGB(0x100, 1); SGB(0x008, 2 * NJ - 2); SBAR(); pin1(0); pin1(1);
    if (s == 14) check_w0<true, true>(wid, flagp(ws, F_KV + h * 8), 0u, tok, 1ull);
    if (s < 15) {
      if (s == 0) wait_bar<8>(); else if (s == 1 || s == 9) wait_bar<10>();
      else if (s <= 8) { if (s % 3 == 0) wait_barf<12, 10>(fast); else wait_barf<12, 11>(fast); }
      else if (s <= 11) wait_barf<8, 11>(fast); else if (s == 12) wait_barf<8, 10>(fast);
      else if (s == 13) wait_bar2<0, 4>(wid); else wait_bar<0>();
      SBAR();
      if (s == 10) signal_w0(wid, fast, flagp(ws, F_KV + h * 8 + bi), tok);
      if (s == 14) signal_w0(wid, slow, flagp(ws, F_KV + h * 8 + bi), tok);
      if (s == 12) poll_w0(wid, flagp(ws, F_KV + h * 8), 0u);
      rdW(s + 1, wbuf ^ 1); rdX(s + 1, 0, 0);
      mm1(wbuf, 1, 1, 0);
#pragma unroll
      for (int q = 0; q < NJ; ++q) { SGB(0x008, 1); SGB(0x100, 3); }
      SBAR(); pin1(2);
      if (s + 4 < 16) { pb_piece(pb, lds0, wid, s + 4, 0); pb_piece(pb, lds0, wid, s + 4, 1); SBAR(); }
      if (s <= 7) { kvstore(2 * s, fast); SBAR(); }
      mm1(wbuf, 1, 1, 1); SBAR(); pin1(3);
      if (s + 4 < 16) { pb_piece(pb, lds0, wid, s + 4, 2); pb_piece(pb, lds0, wid, s + 4, 3); SBAR(); }
      if (s <= 7) { kvstore(2 * s + 1, fast); SBAR(); }
      if (s <= 11) { const int n0 = s + (s + 2) / 3; kvstore(n0, slow); if (s % 3 == 0) kvstore(n0 + 1, slow); SBAR(); }
    } else {
      wait_bar2<0, 63>(wid);
      kvpiece(0u, 0, L_KS0, 0); kvpiece(0u, 0, L_KS0, 1); kvpiece(0u, 1, L_KS0 + 16384, 0); kvpiece(0u, 1, L_KS0 + 16384, 1);
      kvpiece(16u << 20, 0, v0slot, 0); kvpiece(16u << 20, 0, v0slot, 1);
      SBAR();
      mm1(wbuf, 1, 1, 0); mm1(wbuf, 1, 1, 1);
    }
  }
  char* qdst = lds + ((rgp < 2) ? L_QA : L_QB);
  const __amdgpu_buffer_rsrc_t sH = mk_srd(ws + WS_H, 128u * 44u * 1024u);
#pragma unroll
  for (int ii = 0; ii < 4; ++ii) {
    const int rb32 = 2 * (rgp & 1) + (ii >> 1), r = 16 * (ii & 1) + c;
#pragma unroll
    for (int dpp = 0; dpp < 2; ++dpp) {
      const int ks = 2 * (2 * ch + dpp) + (g >> 1);
      const f32x4 a = acc[ii][2 * dpp] * QSCALE, b = acc[ii][2 * dpp + 1] * QSCALE;
      u32x4 w = {cvtpk(a[0], a[1]), cvtpk(a[2], a[3]), cvtpk(b[0], b[1]), cvtpk(b[2], b[3])};
      *(u32x4*)(qdst + ((rb32 * 8 + ks) * 64 + (g & 1) * 32 + r) * 16) = w;
    }
    if (FFN) {
      const int rb16 = ((rgp < 2) ? (15 - bi) * 8 : bi * 8) + 4 * (rgp & 1) + ii;
      const bool valid = (h * 64 + ch * 32) < 1376;
      f32x4 a = acc[ii][NJ - 2], b = acc[ii][NJ - 1];
#pragma unroll
      for (int e = 0; e < 4; ++e) { a[e] = valid ? fmaxf(a[e], 0.f) : 0.f; b[e] = valid ? fmaxf(b[e], 0.f) : 0.f; }
      u32x4 w = {cvtpk(a[0], a[1]), cvtpk(a[2], a[3]), cvtpk(b[0], b[1]), cvtpk(b[2], b[3])};
      st16_wt(sH, (unsigned)(((rb16 * 44 + 2 * h + ch) * 64 + lane) * 16), w);
    }
  }
}

DI void store_ctx(__amdgpu_buffer_rsrc_t sCtx, const float (&y)[16], int rowblk16, int h, int db, int r, int hi) {
#pragma unroll
  for (int sx = 0; sx < 2; ++sx) {
    u32x4 w = {cvtpk(y[8 * sx], y[8 * sx + 1]), cvtpk(y[8 * sx + 2], y[8 * sx + 3]), cvtpk(y[8 * sx + 4], y[8 * sx + 5]), cvtpk(y[8 * sx + 6], y[8 * sx + 7])};
    st16_wt(sCtx, (unsigned)((((rowblk16 + (r >> 4)) * 128 + h * 4 + db) * 64 + (2 * sx + hi) * 16 + (r & 15)) * 16), w);
  }
}

template <int OFF> DI void dma2(__amdgpu_buffer_rsrc_t srd, unsigned voff, unsigned soff, unsigned lds_dst) {
  asm volatile("s_mov_b32 m0, %0\n\ts_nop 0\n\tbuffer_load_dwordx4 %1, %2, %3 offen offset:%4 lds\n\tbuffer_load_dwordx4 %1, %2, %3 offen offset:%5 lds"
               :: "s"(lds_dst), "v"(voff), "s"(srd), "s"(soff), "i"(OFF), "i"(OFF + 1024) : "memory");
}
DI unsigned rdl(unsigned v, int l) { return (unsigned)__builtin_amdgcn_readlane((int)v, l); }

DI void phaseC(char* lds, char* ws, __amdgpu_buffer_rsrc_t sK, const u16* wo, const u16* w2, u64 tok, int h, int bi, int wid, int lane) {
  const unsigned pvo = (unsigned)(lane & 7) * 8u;
  const __amdgpu_buffer_rsrc_t sCtx = mk_srd(ws + WS_CTX, 16u << 20);
  const int rg = wid & 3, grp = wid >> 2, r = lane & 31, hi = lane >> 5;
  const unsigned lds0 = (unsigned)(uintptr_t)lds;
  const int P1 = 2 * bi + 2;
  const unsigned vL = (unsigned)lane * 16u;
  unsigned tKs, tVs, tC;
  {
    const unsigned hbase = (unsigned)(h * 524288);
    auto tix = [&](int g) { return g < P1 ? g : P1 + 2 * (g - P1) + grp; };
    auto kslot = [&](int g) { return ((grp && g >= P1) ? L_KS2 : L_KS0) + (g & 1) * 16384; };
    auto vslot = [&](int g) { return g < P1 ? L_V + ((g - P1) & 3) * 16384 : ((grp && g == 16) ? L_KS2 + 16384 : L_V + (2 * grp + (g & 1)) * 16384); };
    const int k = lane, gk = k + 1;
    const bool ksh = gk < P1, kok = gk <= 16;
    const unsigned ko = ksh ? wid * 2048 : rg * 4096;
    tKs = kok ? (hbase + (unsigned)(tix(gk) * 16384) + ko) | (unsigned)(kslot(gk) >> 14) : (OOBS | 14u);
    const bool vsh = k < P1;
    const int gv = (vsh || !grp) ? k : k - 1;
    const bool vok = vsh || gv >= P1;
    const unsigned vo = vsh ? wid * 2048 : rg * 4096;
    tVs = vok ? ((16u << 20) + hbase + (unsigned)(tix(gv) * 16384) + vo) | (unsigned)(vslot(gv) >> 14) : (OOBS | 14u);
    const int g = lane - 1;
    const bool first = (g < 0) || (grp && g + 1 == P1);
    const bool mask = (g + 1 == 16) || (grp && (g + 1 == P1 - 2 || g + 1 == P1 - 1));
    const unsigned qsrc = (g < 0 && grp) ? (unsigned)L_QB : (unsigned)L_QA;
    tC = (unsigned)(kslot(g + 1) >> 14) | ((unsigned)((g < 0 ? L_KS0 : vslot(g)) >> 14) << 4) | ((unsigned)(kslot(g + 2) >> 14) << 8) | ((qsrc >> 14) << 12) |
         (first ? 1u << 16 : 0u) | (mask ? 1u << 17 : 0u) | ((kok && !ksh) ? 1u << 18 : 0u) | ((vok && !vsh) ? 1u << 19 : 0u) |
         ((unsigned)((((grp && g + 1 < P1) ? bi : 15 - bi) * 4 + rg - tix(g + 1) * 2) & 255) << 24);
  }
  const float rl = (float)(r - 4 * hi) + 0.5f;
  bf16x8 qf[8];
  f32x16 o[4], S1, T0, T1, negm;
  u32x4 pk0 = {}, pk1 = {}, pk2, pk3;
  float m = 0.f, l = 0.f, lB = 0.f, ma, mb;
  bf16x8 k0, k1, k2, k3, v0, v1, v2, v3;
#pragma unroll
  for (int d = 0; d < 4; ++d) o[d] = f32x16{};
  S1 = f32x16{};
#define KF(i) ldsr128(kp + (i) * 1024)
#define VF(db, ks) ldsr128(((db) < 2 ? vpl : vpl_hi) + (((db) & 1) * 4 + (ks)) * 1024)
#define EX(v) __builtin_amdgcn_exp2f(v)
#define BFR(x) __builtin_bit_cast(bf16x8, x)
#define ADD2(a0, a1) asm("v_add_f32_e32 %0, %0, %2\n\tv_add_f32_e32 %1, %1, %3" : "+v"(sa), "+v"(sb) : "v"(a0), "v"(a1))
#define MX2(X, i) asm("v_max3_f32 %0, %0, %2, %3\n\tv_max3_f32 %1, %1, %4, %5" : "+v"(ma), "+v"(mb) : "v"(X[i]), "v"(X[i + 1]), "v"(X[i + 2]), "v"(X[i + 3]))
  unsigned ksrc = 0x7f000000u, kdst = lds0 + 0x38000u, vsrc = 0x7f000000u, vdst = lds0 + 0x38000u, dfl = 0;
#define HOOK0() do { if (dfl & 1) dma2<0>(sK, vL, ksrc, kdst); SBAR(); } while (0)
#define HOOK1() do { if (dfl & 2) dma2<2048>(sK, vL, ksrc, kdst); SBAR(); } while (0)
#define HOOK2() do { if (dfl & 1) dma2<0>(sK, vL, vsrc, vdst); SBAR(); } while (0)
#define HOOK3() do { if (dfl & 4) dma2<2048>(sK, vL, vsrc, vdst); SBAR(); } while (0)
#define PLAN(k) do { const unsigned a_ = rdl(tKs, k), b_ = rdl(tVs, k); dfl = 1u | ((rdl(tC, k) >> 17) & 6u); ksrc = a_ & ~15u; vsrc = b_ & ~15u; \
    kdst = lds0 + (a_ & 15u) * 16384u + ((dfl & 2u) ? rg * 4096u : wid * 2048u); vdst = lds0 + (b_ & 15u) * 16384u + ((dfl & 4u) ? rg * 4096u : wid * 2048u); } while (0)
  #pragma unroll 1
  for (int g = -1; g < 16; ++g) {
    const unsigned tc = rdl(tC, g + 1);
    const lds_cptr kp = (lds_cptr)lds + (lane * 16 + (tc & 15u) * 16384u);
    const lds_cptr vp = (lds_cptr)lds + (lane * 16 + ((tc >> 4) & 15u) * 16384u);
    const lds_cptr vpl = vp + grp * 8192, vpl_hi = vp + (8192 - grp * 8192);
    const bool first_next = (tc >> 16) & 1u;
    dfl = 0;
    if (grp == 0 || g <= 0) {
      if (g < 0) asm volatile("s_waitcnt vmcnt(4) lgkmcnt(0)\n\ts_barrier" ::: "memory");
      else {
        const int gk = g + 2 > 16 ? 16 : g + 2, tmax = gk < P1 ? gk : 2 * gk - P1 + 1;
        asm volatile("s_waitcnt vmcnt(0)" ::: "memory");
        check_w0<false, true>(wid, flagp(ws, F_KV + h * 8), pvo, tok, (2ull << (tmax >> 2)) - 1ull);
        asm volatile("s_waitcnt lgkmcnt(0)\n\ts_barrier" ::: "memory");
        PLAN(g + 1);
      }
      poll_w0(wid, flagp(ws, F_KV + h * 8), pvo);
            k0 = KF(0); k1 = KF(1); k2 = KF(2); k3 = KF(3);
    }
    if (first_next) {
      const lds_cptr qp = (lds_cptr)lds + (lane * 16 + rg * 8192 + ((tc >> 12) & 15u) * 16384u);
#pragma unroll
      for (int ks = 0; ks < 8; ++ks) qf[ks] = ldsr128(qp + ks * 1024);
      negm = f32x16{};
    }
    PIN(negm);
    SBAR();
    {
      float sa = 0.f, sb = 0.f;
      bf16x8 k4, k5;
      T0 = MFMA(k0, qf[0], negm); k4 = KF(4); k5 = KF(5); ADD2(S1[0], S1[1]); pk2[0] = cvtpk(S1[0], S1[1]); PIN(pk2); SBAR();
      T0 = MFMA(k1, qf[1], T0); ADD2(S1[2], S1[3]); pk2[1] = cvtpk(S1[2], S1[3]); PIN(pk2); SBAR();
      T0 = MFMA(k2, qf[2], T0); k0 = KF(6); k1 = KF(7); ADD2(S1[4], S1[5]); pk2[2] = cvtpk(S1[4], S1[5]); PIN(pk2); SBAR();
      T0 = MFMA(k3, qf[3], T0); ADD2(S1[6], S1[7]); pk2[3] = cvtpk(S1[6], S1[7]); PIN(pk2); SBAR();
      HOOK0();
      T0 = MFMA(k4, qf[4], T0); k2 = KF(8); k3 = KF(9); ADD2(S1[8], S1[9]); pk3[0] = cvtpk(S1[8], S1[9]); PIN(pk3); SBAR();
      T0 = MFMA(k5, qf[5], T0); ADD2(S1[10], S1[11]); pk3[1] = cvtpk(S1[10], S1[11]); PIN(pk3); SBAR();
      T0 = MFMA(k0, qf[6], T0); k4 = KF(10); k5 = KF(11); ADD2(S1[12], S1[13]); pk3[2] = cvtpk(S1[12], S1[13]); PIN(pk3); SBAR();
      T0 = MFMA(k1, qf[7], T0); ADD2(S1[14], S1[15]); pk3[3] = cvtpk(S1[14], S1[15]); PIN(pk3); SBAR();
      HOOK1();
      l += sa + sb; PIN(pk2); PIN(pk3);
      T1 = MFMA(k2, qf[0], negm); k0 = KF(12); k1 = KF(13); SBAR();
      T1 = MFMA(k3, qf[1], T1); SBAR();
      T1 = MFMA(k4, qf[2], T1); k2 = KF(14); k3 = KF(15); SBAR();
      T1 = MFMA(k5, qf[3], T1); SBAR();
      HOOK2();
      ma = T0[0]; mb = T0[1];
      T1 = MFMA(k0, qf[4], T1); v0 = VF(0, 0); v1 = VF(1, 0); MX2(T0, 2); SBAR();
      T1 = MFMA(k1, qf[5], T1); MX2(T0, 6); SBAR();
      T1 = MFMA(k2, qf[6], T1); v2 = VF(2, 0); v3 = VF(3, 0); MX2(T0, 10); SBAR();
      T1 = MFMA(k3, qf[7], T1); ma = max3f(ma, T0[14], T0[15]); SBAR();
      HOOK3();
    }
    if (grp == 1 && g >= 0) {
      asm volatile("s_waitcnt vmcnt(0) lgkmcnt(0)\n\ts_barrier" ::: "memory");
      v0 = VF(0, 0); v1 = VF(1, 0); v2 = VF(2, 0); v3 = VF(3, 0);
      if (g < 15) PLAN(g + 2); else dfl = 0;
          } else dfl = 0;
    {
      const lds_cptr kp = (lds_cptr)lds + (lane * 16 + ((tc >> 8) & 15u) * 16384u);
      bf16x8 v4, v5;
      o[0] = MFMA(v0, BFR(pk0), o[0]); v4 = VF(0, 1); v5 = VF(1, 1); SBAR();
      o[1] = MFMA(v1, BFR(pk0), o[1]); SBAR();
      o[2] = MFMA(v2, BFR(pk0), o[2]); v0 = VF(2, 1); v1 = VF(3, 1); MX2(T1, 0); MX2(T1, 4); SBAR();
      o[3] = MFMA(v3, BFR(pk0), o[3]); MX2(T1, 8); MX2(T1, 12); SBAR();
      ma = max2f(ma, mb);
      bool resc = false; float fsc = 1.f;
      if (__builtin_expect(((tc >> 16) & 3u) != 0u || __any(ma > THR), 0)) {
        if ((tc >> 17) & 1u) {
          const float lfB = ((float)(((int)tc >> 24) * 32 + 64) + rl) * 1.0e30f;
#pragma unroll
          for (int i = 0; i < 16; ++i) { const int kk = (i & 3) + 8 * (i >> 2); float t0_, t1_;
            asm("v_add_f32_e32 %2, %5, %4\n\tv_add_f32_e32 %3, %6, %4\n\tv_min_f32_e32 %0, %0, %2\n\tv_min_f32_e32 %1, %1, %3"
                : "+v"(T0[i]), "+v"(T1[i]), "=&v"(t0_), "=&v"(t1_) : "v"(lfB), "i"(__builtin_bit_cast(int, -(float)(kk + 64) * 1.0e30f)), "i"(__builtin_bit_cast(int, -(float)(kk + 96) * 1.0e30f))); }
        }
        ma = T0[0]; mb = T0[1];
        MX2(T0, 2); MX2(T0, 6); MX2(T0, 10); ma = max3f(ma, T0[14], T0[15]); MX2(T1, 0); MX2(T1, 4); MX2(T1, 8); MX2(T1, 12);
        const float a = swap32_max(max2f(ma, mb));
        float dl;
        if (first_next) { dl = fmaxf(a, -1.0e29f); lB = l; l = 0.f; m = dl; }
        else { dl = fmaxf(a, 0.f); m += dl; fsc = EX(-dl); l *= fsc; resc = true; }
#pragma unroll
        for (int i = 0; i < 16; ++i) { T0[i] -= dl; T1[i] -= dl; negm[i] = -m; }
      }
      SBAR();
      float sa = 0.f, sb = 0.f;
#define EX4(X, i) do { X[i] = EX(X[i]); X[i + 1] = EX(X[i + 1]); X[i + 2] = EX(X[i + 2]); X[i + 3] = EX(X[i + 3]); PIN(X); } while (0)
#define EX2(X, i) do { X[i] = EX(X[i]); X[i + 1] = EX(X[i + 1]); PIN(X); } while (0)
      HOOK0();
      o[0] = MFMA(v4, BFR(pk1), o[0]); v2 = VF(0, 2); v3 = VF(1, 2); EX4(T0, 0); SBAR();
      o[1] = MFMA(v5, BFR(pk1), o[1]); EX4(T0, 4); SBAR();
      o[2] = MFMA(v0, BFR(pk1), o[2]); v4 = VF(2, 2); v5 = VF(3, 2); EX4(T0, 8); SBAR();
      o[3] = MFMA(v1, BFR(pk1), o[3]); EX4(T0, 12); SBAR();
      HOOK1();
      o[0] = MFMA(v2, BFR(pk2), o[0]); v0 = VF(0, 3); v1 = VF(1, 3); EX2(T1, 0); ADD2(T0[0], T0[1]); pk0[0] = cvtpk(T0[0], T0[1]); PIN(pk0); SBAR();
      o[1] = MFMA(v3, BFR(pk2), o[1]); EX2(T1, 2); ADD2(T0[2], T0[3]); pk0[1] = cvtpk(T0[2], T0[3]); PIN(pk0); SBAR();
      o[2] = MFMA(v4, BFR(pk2), o[2]); v2 = VF(2, 3); v3 = VF(3, 3); EX2(T1, 4); ADD2(T0[4], T0[5]); pk0[2] = cvtpk(T0[4], T0[5]); PIN(pk0); SBAR();
      o[3] = MFMA(v5, BFR(pk2), o[3]); EX2(T1, 6); ADD2(T0[6], T0[7]); pk0[3] = cvtpk(T0[6], T0[7]); PIN(pk0); SBAR();
      HOOK2();
      o[0] = MFMA(v0, BFR(pk3), o[0]); k0 = KF(0); k1 = KF(1); EX2(T1, 8); ADD2(T0[8], T0[9]); pk1[0] = cvtpk(T0[8], T0[9]); PIN(pk1); SBAR();
      o[1] = MFMA(v1, BFR(pk3), o[1]); EX2(T1, 10); ADD2(T0[10], T0[11]); pk1[1] = cvtpk(T0[10], T0[11]); PIN(pk1); SBAR();
      o[2] = MFMA(v2, BFR(pk3), o[2]); k2 = KF(2); k3 = KF(3); EX2(T1, 12); ADD2(T0[12], T0[13]); pk1[2] = cvtpk(T0[12], T0[13]); PIN(pk1); SBAR();
      o[3] = MFMA(v3, BFR(pk3), o[3]); EX2(T1, 14); ADD2(T0[14], T0[15]); pk1[3] = cvtpk(T0[14], T0[15]); PIN(pk1); SBAR();
      HOOK3();
      l += sa + sb;
      S1 = T1;
      if (__builtin_expect(resc, 0)) {
#pragma unroll
        for (int d = 0; d < 4; ++d)
#pragma unroll
          for (int i = 0; i < 16; ++i) o[d][i] *= fsc;
      }
      if (first_next && g >= 0) {
        const float inv = 1.0f / swap32_add(lB);
#pragma unroll
        for (int d = 0; d < 4; ++d) {
          float y[16];
#pragma unroll
          for (int i = 0; i < 16; ++i) y[i] = o[d][i] * inv;
          store_ctx(sCtx, y, bi * 8 + 2 * rg, h, d ^ 2, r, hi);
          o[d] = f32x16{};
        }
      }
      if (g == 10) {
        const int role = (int)(blockIdx.x & 7) >> 1, id = (int)(blockIdx.x >> 3) * 8 + wid;
        touch(role == 3 ? mk_srd(w2, 1376u * 512u * 2u) : mk_srd(wo, 4096u * 512u * 2u), (unsigned)lane * 128u, id < 176 ? (unsigned)((role == 3 ? 0 : role * 22 * 65536) + id * 8192) : OOBS);
      }
      if (grp == 1 && g == 14) {
        const unsigned src = (16u << 20) + (unsigned)(h * 524288 + (P1 + 2 * (16 - P1) + 1) * 16384 + rg * 4096), dst = lds0 + (unsigned)(L_KS2 + 16384 + rg * 4096);
        dma2<0>(sK, vL, src, dst); dma2<2048>(sK, vL, src, dst);
      }
    }
  }
  if (grp == 0) asm volatile("s_waitcnt vmcnt(0) lgkmcnt(0)\n\ts_barrier" ::: "memory");
    {
    const lds_cptr vp = (lds_cptr)lds + (lane * 16 + (grp ? L_KS2 + 16384 : L_V));
    const lds_cptr vpl = vp + grp * 8192, vpl_hi = vp + (8192 - grp * 8192);
#pragma unroll
    for (int i = 0; i < 16; ++i) l += S1[i];
    pk2 = pack8(S1, 0); pk3 = pack8(S1, 1);
#pragma unroll
    for (int d = 0; d < 4; ++d) { o[d] = MFMA(VF(d, 0), BFR(pk0), o[d]); o[d] = MFMA(VF(d, 1), BFR(pk1), o[d]); o[d] = MFMA(VF(d, 2), BFR(pk2), o[d]); o[d] = MFMA(VF(d, 3), BFR(pk3), o[d]); }
  }
  {
    l = swap32_add(l);
    char* mybuf = lds + grp * 32768 + rg * 8192;
    const char* pbuf = lds + (grp ^ 1) * 32768 + rg * 8192;
    float* ml = (float*)(lds + L_QA);
    asm volatile("s_waitcnt lgkmcnt(0)\n\ts_barrier" ::: "memory");
#pragma unroll
    for (int t = 0; t < 2; ++t)
#pragma unroll
      for (int q = 0; q < 4; ++q) {
        f32x4 v = {o[2 + t][4 * q], o[2 + t][4 * q + 1], o[2 + t][4 * q + 2], o[2 + t][4 * q + 3]};
        *(f32x4*)(mybuf + ((t * 4 + q) * 64 + lane) * 16) = v;
      }
    if (hi == 0) { ml[wid * 64 + r] = m; ml[wid * 64 + 32 + r] = l; }
    asm volatile("s_waitcnt lgkmcnt(0)\n\ts_barrier" ::: "memory");
    const int pw = wid ^ 4;
    const float m2 = ml[pw * 64 + r], l2 = ml[pw * 64 + 32 + r];
    const float mm = fmaxf(m, m2);
    const float f1 = EX(m - mm), f2 = EX(m2 - mm);
    const float inv = 1.0f / (l * f1 + l2 * f2);
    const float g1 = f1 * inv, g2 = f2 * inv;
#pragma unroll
    for (int t = 0; t < 2; ++t) {
      float y[16];
#pragma unroll
      for (int q = 0; q < 4; ++q) {
        const f32x4 v = *(const f32x4*)(pbuf + ((t * 4 + q) * 64 + lane) * 16);
#pragma unroll
        for (int e = 0; e < 4; ++e) y[4 * q + e] = o[t][4 * q + e] * g1 + v[e] * g2;
      }
      store_ctx(sCtx, y, (15 - bi) * 8 + 2 * rg, h, 2 * grp + t, r, hi);
    }
  }
#undef KF
#undef VF
}


typedef unsigned u32x2 __attribute__((ext_vector_type(2)));
DI void phaseD(char* lds, char* ws, const u16* x, const u16* wo, const u16* w2, u16* out, u64 tok, int h, int bi, int wid, int lane) {
  const int b = blockIdx.x, xcd = b & 7, jj = b >> 3, role = xcd >> 1, mt = (xcd & 1) * 8 + (jj >> 2), nt = jj & 3;
  const int c = lane & 15, g = lane >> 4, kgrp = wid >> 2, wr = (wid >> 1) & 1, wc = wid & 1;
  const unsigned lds0 = (unsigned)(uintptr_t)lds;
  const bool ish = role == 3;
  const int s0 = ish ? 0 : role * 22;
  constexpr int NS = 22;
  const __amdgpu_buffer_rsrc_t sA = ish ? mk_srd(ws + WS_H, 128u * 44u * 1024u) : mk_srd(ws + WS_CTX, 16u << 20);
  const __amdgpu_buffer_rsrc_t sW = ish ? mk_srd(w2, 1376u * 512u * 2u) : mk_srd(wo, 4096u * 512u * 2u);
  const unsigned vL = (unsigned)lane * 16u;
  const unsigned aRow = (unsigned)((mt * 8 + wid) * (ish ? 44 : 128) + s0 * 2) * 1024u;
  unsigned vW[2];
#pragma unroll
  for (int m = 0; m < 2; ++m) vW[m] = (unsigned)(((m * 4 + ((lane >> 2) & 3)) * 512 + (lane >> 4) * 32 + ((lane & 3) ^ (2 * m)) * 8) * 2);
  const unsigned wBase = (unsigned)((wid * 8 * 512 + nt * 128) * 2) + (unsigned)s0 * 65536u;
  const unsigned dw = lds0 + (unsigned)(wid * 2048);
  const unsigned aLate = role == 2 ? OOBS : aRow;
  auto pA = [&](int s, int m) { dma16(sA, vL, (s < 20 ? aRow : aLate) + (unsigned)(s * 2048 + m * 1024), dw + (unsigned)((s % 5) * 32768 + m * 1024)); };
  auto pW = [&](int s, int m) { dma16(sW, vW[m], wBase + (unsigned)s * 65536u, dw + (unsigned)((s % 5) * 32768 + 16384 + m * 1024)); };
  asm volatile("s_waitcnt lgkmcnt(0)\n\ts_barrier" ::: "memory");
#pragma unroll
  for (int s = 0; s < 5; ++s) { pW(s, 0); pW(s, 1); }
  wait_bar<10>();
  signal_w0(wid, 1u, flagp(ws, F_C + bi * 32 + h), tok); signal_w0(wid, 1u, flagp(ws, F_C + (15 - bi) * 32 + h), tok);
  { check_w0<false, false>(wid, flagp(ws, F_C + mt * 32), (unsigned)(lane & 31) * 8u, tok, ~0ull); }
  asm volatile("s_barrier" ::: "memory");
#pragma unroll
  for (int s = 0; s < 5; ++s) { pA(s, 0); pA(s, 1); }
  const int ab = (wr * 4 + 2 * kgrp) * 2048 + kgrp * 1024 + lane * 16, abx = (wr * 4 + 2 - 2 * kgrp) * 2048 + kgrp * 1024 + lane * 16;
  int wb[2][2];
  {
    const int kq0 = ish ? g : 4 * (g >> 1) + (g & 1), kst = ish ? 4 : 2;
#pragma unroll
    for (int par = 0; par < 2; ++par)
#pragma unroll
      for (int t = 0; t < 2; ++t) { const int kq = kgrp * 8 + kq0 + t * kst;
        wb[par][t] = 16384 + kq * 1024 + wc * 512 + ((lane >> 2) & 3) * 64 + ((par ^ (kq & 1)) * 32) + (lane & 3) * 8; }
  }
  f32x4 acc[4][4];
#pragma unroll
  for (int i = 0; i < 4; ++i)
#pragma unroll
    for (int j = 0; j < 4; ++j) acc[i][j] = f32x4{};
  bf16x8 af[2][4], wf[2][4];
  wait_bar<8>();
  {
    lds_cptr base = (lds_cptr)lds;
#pragma unroll
    for (int j = 0; j < 4; ++j) wf[0][j] = cat8(ldstr(base + wb[j & 1][0] + (j >> 1) * 256), ldstr(base + wb[j & 1][1] + (j >> 1) * 256));
#pragma unroll
    for (int i = 0; i < 4; ++i) af[0][i] = ldsr128(base + ((i < 2) ? ab : abx) + (i & 1) * 2048);
  }
#pragma unroll
  for (int s = 0; s < NS; ++s) {
    const int hf = s & 1;
    const bool nx = s + 1 < NS, pf = s + 5 < NS;
    if (nx) {
      constexpr int dummy = 0; (void)dummy;
      const int yo = (s < 3) ? (6 - 2 * s) + 4 * s : 12;
      const int tail = NS - 2 - s;
      const int n = (tail < 3) ? 4 * tail : yo;
      if (n == 12) wait_bar<12>(); else if (n == 10) wait_bar<10>(); else if (n == 8) wait_bar<8>(); else if (n == 6) wait_bar<6>(); else if (n == 4) wait_bar<4>(); else wait_bar<0>();
    }
    SBAR();
    lds_cptr base = (lds_cptr)lds + ((s + 1) % 5) * 32768;
    s16x4 wl[4], wh[4];
#define K2M(i, j) acc[i][j] = MFMA16(wf[hf][j], af[hf][i], acc[i][j])
#define PIN4(i) asm volatile("" : "+v"(acc[i][0]), "+v"(acc[i][1]), "+v"(acc[i][2]), "+v"(acc[i][3]))
    K2M(0, 0); if (nx) wl[0] = ldstr(base + wb[0][0]); SBAR();
    K2M(0, 1); if (nx) wh[0] = ldstr(base + wb[0][1]); SBAR();
    K2M(0, 2); if (nx) af[hf ^ 1][0] = ldsr128(base + ab); SBAR();
    K2M(0, 3); if (nx) wl[1] = ldstr(base + wb[1][0]); SBAR();
    PIN4(0); if (pf) pA(s + 5, 0); SBAR();
    K2M(1, 0); if (nx) wh[1] = ldstr(base + wb[1][1]); SBAR();
    K2M(1, 1); if (nx) wl[2] = ldstr(base + wb[0][0] + 256); SBAR();
    K2M(1, 2); if (nx) wh[2] = ldstr(base + wb[0][1] + 256); SBAR();
    K2M(1, 3); if (nx) wl[3] = ldstr(base + wb[1][0] + 256); SBAR();
    PIN4(1); if (pf) pA(s + 5, 1); SBAR();
    K2M(2, 0); if (nx) wh[3] = ldstr(base + wb[1][1] + 256); SBAR();
    K2M(2, 1); if (nx) af[hf ^ 1][1] = ldsr128(base + ab + 2048); SBAR();
    K2M(2, 2); if (nx) af[hf ^ 1][2] = ldsr128(base + abx); SBAR();
    K2M(2, 3); if (nx) af[hf ^ 1][3] = ldsr128(base + abx + 2048); SBAR();
    PIN4(2); if (pf) pW(s + 5, 0); SBAR();
    K2M(3, 0); K2M(3, 1); SBAR();
    if (pf) pW(s + 5, 1); SBAR();
    K2M(3, 2); K2M(3, 3); SBAR(); PIN4(3);
#undef K2M
    if (nx) {
#pragma unroll
      for (int j = 0; j < 4; ++j) wf[hf ^ 1][j] = cat8(wl[j], wh[j]);
    }
  }
  asm volatile("s_waitcnt vmcnt(0) lgkmcnt(0)\n\ts_barrier" ::: "memory");
  {
    char* mybuf = lds + wid * 8192;
    const char* pbuf = lds + (wid ^ 4) * 8192;
#pragma unroll
    for (int i = 0; i < 2; ++i)
#pragma unroll
      for (int j = 0; j < 4; ++j) *(f32x4*)(mybuf + ((i * 4 + j) * 64 + lane) * 16) = acc[2 + i][j];
    asm volatile("s_waitcnt lgkmcnt(0)\n\ts_barrier" ::: "memory");
#pragma unroll
    for (int i = 0; i < 2; ++i)
#pragma unroll
      for (int j = 0; j < 4; ++j) acc[i][j] += *(const f32x4*)(pbuf + ((i * 4 + j) * 64 + lane) * 16);
  }
  const int qd = 2 * wr + kgrp, tile = mt * 4 + nt;
  const __amdgpu_buffer_rsrc_t sP = mk_srd(ws + WS_PART, 16u << 20);
  f32x4 own[2];
#pragma unroll
  for (int j = 0; j < 4; ++j) {
    if (j != role) {
      const u32x4 w = {cvtpk(acc[0][j][0], acc[0][j][1]), cvtpk(acc[0][j][2], acc[0][j][3]), cvtpk(acc[1][j][0], acc[1][j][1]), cvtpk(acc[1][j][2], acc[1][j][3])};
      st16_wt(sP, (unsigned)((((tile * 4 + j) * 4 + role) * 8 + wid) * 1024) + vL, w);
    } else { own[0] = acc[0][j]; own[1] = acc[1][j]; }
  }
  u32x2 xv[2];
  const size_t obase = (size_t)(mt * 128 + qd * 32 + c) * 512 + nt * 128 + wc * 64 + role * 16 + 4 * g;
#pragma unroll
  for (int i = 0; i < 2; ++i) xv[i] = *(const u32x2*)(x + obase + i * 16 * 512);
  asm volatile("s_waitcnt vmcnt(0)\n\ts_barrier" ::: "memory");
  signal_w0(wid, 1u, flagp(ws, F_P + tile * 4 + role), tok);
  { check_w0<false, false>(wid, flagp(ws, F_P + tile * 4), (unsigned)(lane & 3) * 8u, tok, ~0ull); }
  asm volatile("s_barrier" ::: "memory");
  f32x4 p[3][2];
#pragma unroll
  for (int rr = 0; rr < 3; ++rr) {
    const int src = (role + 1 + rr) & 3;
    const u32x4 w = __builtin_amdgcn_raw_buffer_load_b128(sP, (unsigned)((((tile * 4 + role) * 4 + src) * 8 + wid) * 1024) + vL, 0, 16);
#pragma unroll
    for (int i = 0; i < 2; ++i) p[rr][i] = (f32x4){__uint_as_float(w[2 * i] << 16), __uint_as_float(w[2 * i] & 0xffff0000u), __uint_as_float(w[2 * i + 1] << 16), __uint_as_float(w[2 * i + 1] & 0xffff0000u)};
  }
#pragma unroll
  for (int i = 0; i < 2; ++i) {
    f32x4 ya = ish ? own[i] : (role == 2 ? p[0][i] : (role == 1 ? p[1][i] : p[2][i]));
    f32x4 sa = (own[i] + p[0][i]) + (p[1][i] + p[2][i]) - ya;
    {
      const f32x4 z = {};
      sa = (ish ? z : own[i]) + (role == 2 ? z : p[0][i]) + (role == 1 ? z : p[1][i]) + (role == 0 ? z : p[2][i]);
    }
    float o4[4];
#pragma unroll
    for (int e = 0; e < 4; ++e) {
      const float y = rbf(ya[e]);
      const float gt = rbf(y * __builtin_amdgcn_rcpf(1.f + __builtin_amdgcn_exp2f(-1.4426950408889634f * y)));
      const float sg = rbf(rbf(sa[e]) * gt);
      const float xe = (e & 1) ? __uint_as_float(xv[i][e >> 1] & 0xffff0000u) : __uint_as_float(xv[i][e >> 1] << 16);
      o4[e] = xe + sg;
    }
    u32x2 w = {cvtpk(o4[0], o4[1]), cvtpk(o4[2], o4[3])};
    *(u32x2*)(out + obase + i * 16 * 512) = w;
  }
}


__global__ __launch_bounds__(512, 2) __attribute__((amdgpu_num_vgpr(252))) void fused_block(const u16* __restrict__ x, const u16* __restrict__ ckv, const u16* __restrict__ wq, const u16* __restrict__ wuk, const u16* __restrict__ wuv,
                                                      const u16* __restrict__ wo, const u16* __restrict__ w1, const u16* __restrict__ w2, u16* __restrict__ out, char* __restrict__ ws) {
  extern __shared__ __attribute__((aligned(16))) char lds[];
  const int tid = threadIdx.x, lane = tid & 63, wid = __builtin_amdgcn_readfirstlane(tid >> 6);
  const int b = blockIdx.x, xcd = b & 7, jj = b >> 3, h = 4 * xcd + (jj >> 3), mt = jj & 7, bi = mt;
  const __amdgpu_buffer_rsrc_t sC = mk_srd(ckv, 2048u * 512u * 2u), sUK = mk_srd(wuk, 512u * 4096u * 2u), sUV = mk_srd(wuv, 512u * 4096u * 2u);
  const int R = b * 8 + wid;
  u32x4 xr;
  const unsigned vC = (unsigned)((lane >> 2) * 1024 + (((lane & 3) ^ (3 * ((lane >> 5) & 1))) * 16));
  const unsigned vW = (unsigned)((((lane >> 2) & 3) * 4096 + (lane >> 4) * 32 + ((lane & 3) ^ (2 * ((wid >> 1) & 1))) * 8) * 2);
  const unsigned sCbase = (unsigned)((mt * 256 + wid * 32) * 1024);
  const unsigned sWbase = (unsigned)((wid * 4 * 4096 + h * 128) * 2);
  auto st0 = [&](int s) {
    const unsigned bb = (unsigned)(uintptr_t)lds + (unsigned)(PA_BASE + s * 32768);
#pragma unroll
    for (int m = 0; m < 2; ++m) dma16(sC, vC, sCbase + (unsigned)(m * 16 * 1024 + s * 64), bb + (unsigned)((wid * 2 + m) * 1024));
#pragma unroll
    for (int m = 0; m < 2; ++m) dma16(m ? sUV : sUK, vW, sWbase + (unsigned)((s * 32 * 4096) * 2), bb + 16384u + (unsigned)((wid * 2 + m) * 1024));
  };
  { const u16* xp = x + (size_t)R * 512 + lane * 8; asm volatile("global_load_dwordx4 %0, %1, off" : "=v"(xr) : "v"(xp) : "memory"); }
  st0(0); st0(1);
  const u64 tok = ((hw_dispatch_id() + 1ull) * 0x9E3779B97F4A7C15ull) ^ ((u64)(uintptr_t)__builtin_amdgcn_dispatch_ptr() << 7) ^ 0x5bd1e9955bd1e995ull;
  PBsrc pb;
  pb.sX = mk_srd(ws + WS_XN, 2048u * 512u * 2u); pb.sQ = mk_srd(wq, 512u * 4096u * 2u); pb.s1 = mk_srd(w1, 512u * 1376u * 2u);
  pb.vL = pb.vQ = pb.v1 = 0u;
  pb.sXbase = (unsigned)(((wid < 4) ? (15 - bi) * 8 : bi * 8 - 8) + 2 * wid) * 16384u;
  pb.sQbase = sWbase; pb.s1base = h < 22 ? (unsigned)((wid * 4 * 1376 + h * 64) * 2) : OOBS;
  {
    asm volatile("s_waitcnt vmcnt(8)" : "+v"(xr) :: "memory");
    st0(2); st0(3);
    float v[8];
#pragma unroll
    for (int e = 0; e < 4; ++e) { v[2 * e] = __uint_as_float(xr[e] << 16); v[2 * e + 1] = __uint_as_float(xr[e] & 0xffff0000u); }
    float s = 0;
#pragma unroll
    for (int e = 0; e < 8; ++e) s += v[e];
#pragma unroll
    for (int o = 32; o; o >>= 1) s += __shfl_xor(s, o);
    const float mean = s * (1.f / 512.f);
    float q = 0;
#pragma unroll
    for (int e = 0; e < 8; ++e) { const float d = v[e] - mean; q += d * d; }
#pragma unroll
    for (int o = 32; o; o >>= 1) q += __shfl_xor(q, o);
    const float mb = rbf(mean), vb = rbf(q * (1.f / 512.f));
    const float den = rbf(__builtin_sqrtf(rbf(vb + 1e-5f)));
    float o8[8];
#pragma unroll
    for (int e = 0; e < 8; ++e) o8[e] = rbf(v[e] - mb) / den;
    u32x4 w = {cvtpk(o8[0], o8[1]), cvtpk(o8[2], o8[3]), cvtpk(o8[4], o8[5]), cvtpk(o8[6], o8[7])};
    st16_wt(pb.sX, (unsigned)((((R >> 4) * 16 + (lane >> 2)) * 64 + ((lane & 3) * 16 + (R & 15))) * 16), w);
  }
  {
    const int id = jj * 8 + wid;
    touch(sC, (unsigned)lane * 128u, (unsigned)id * 8192u);
    unsigned long long pcv; unsigned len;
    asm volatile("s_getpc_b64 %0\n.Lfb_pc:\n\ts_mov_b32 %1, .Lfb_end-.Lfb_pc" : "=s"(pcv), "=s"(len));
    const bool w = id < 128;
    touch(w ? ((id & 64) ? sUV : sUK) : mk_srd((const char*)(pcv & ~127ull), len), w ? (unsigned)(lane >> 3) * 8192u + (unsigned)(lane & 7) * 128u : (unsigned)lane * 128u,
          w ? (unsigned)((id & 63) * 8 * 8192 + xcd * 1024) : (unsigned)(id - 128) * 8192u);
    asm volatile("buffer_load_dword v254, %0, %1, %2 offen sc0 sc1" :: "v"((unsigned)lane * 65536u), "s"(mk_srd(ws, 81u << 20)), "s"(id >= 160 && id < 181 ? (unsigned)(id - 160) * (4u << 20) : OOBS) : "memory", "v254");
  }
  const __amdgpu_buffer_rsrc_t sK = mk_srd(ws + WS_KF, 32u << 20);
  u32x4 kv[16];
  if (wid < 4) phaseA<true>(lds, ws, sC, sUK, sUV, sCbase, sWbase, pb, tok, h, mt, wid, lane_id(), kv);
  else         phaseA<false>(lds, ws, sC, sUK, sUV, sCbase, sWbase, pb, tok, h, mt, wid, lane_id(), kv);
  if (h < 22) phaseB<true>(lds, ws, pb, sK, tok, h, bi, wid, lane_id(), kv);
  else        phaseB<false>(lds, ws, pb, sK, tok, h, bi, wid, lane_id(), kv);
  phaseC(lds, ws, sK, wo, w2, tok, h, bi, wid, lane_id());
  phaseD(lds, ws, x, wo, w2, out, tok, h, bi, wid, lane_id());
  asm volatile(".Lfb_end:");
}

extern "C" void kernel_launch(void* const* d_in, const int* in_sizes, int n_in, void* d_out, int out_size, void* d_ws, size_t ws_size, hipStream_t stream) {
  (void)in_sizes; (void)n_in; (void)out_size; (void)ws_size;
  (void)hipFuncSetAttribute((const void*)fused_block, hipFuncAttributeMaxDynamicSharedMemorySize, 163840);
  fused_block<<<256, 512, 163840, stream>>>((const u16*)d_in[0], (const u16*)d_in[1], (const u16*)d_in[2], (const u16*)d_in[3], (const u16*)d_in[4], (const u16*)d_in[5],
                                            (const u16*)d_in[7], (const u16*)d_in[8], (u16*)d_out, (char*)d_ws);
}
```

```cpp
#include <hip/hip_runtime.h>
#include <hip/hip_bf16.h>
#include <stdint.h>

typedef unsigned short u16;
typedef short bf16x8 __attribute__((ext_vector_type(8)));
typedef short s16x4 __attribute__((ext_vector_type(4)));
typedef float f32x16 __attribute__((ext_vector_type(16)));
typedef float f32x4 __attribute__((ext_vector_type(4)));
typedef float f32x2 __attribute__((ext_vector_type(2)));
typedef __bf16 bf16x2_t __attribute__((ext_vector_type(2)));
typedef unsigned u32x4 __attribute__((ext_vector_type(4)));
#define LAS __attribute__((address_space(3)))
typedef LAS const char* lds_cptr;
#define DI __device__ __forceinline__
#define MFMA(a, b, c) __builtin_amdgcn_mfma_f32_32x32x16_bf16((a), (b), (c), 0, 0, 0)
#define SBAR() __builtin_amdgcn_sched_barrier(0)

constexpr size_t WS_KF = 0;
constexpr size_t WS_VF = 16u << 20;
constexpr size_t WS_CTX = 32u << 20;
constexpr size_t WS_XN = 48u << 20;
constexpr size_t WS_H = 50u << 20;
constexpr size_t WS_PART = 56u << 20;
constexpr size_t WS_FLAG = 80u << 20;

DI unsigned cvtpk(float lo, float hi) { f32x2 v = {lo, hi}; bf16x2_t b = __builtin_convertvector(v, bf16x2_t); return __builtin_bit_cast(unsigned, b); }
DI float bf2f(unsigned v16) { return __uint_as_float(v16 << 16); }
DI float rbf(float f) { return bf2f(cvtpk(f, f) & 0xffffu); }
DI u32x4 pack8(const f32x16& x, int s) {
  u32x4 p = {cvtpk(x[8 * s], x[8 * s + 1]), cvtpk(x[8 * s + 2], x[8 * s + 3]), cvtpk(x[8 * s + 4], x[8 * s + 5]), cvtpk(x[8 * s + 6], x[8 * s + 7])};
  return p;
}
DI __amdgpu_buffer_rsrc_t mk_srd(const void* p, unsigned bytes) { return __builtin_amdgcn_make_buffer_rsrc((void*)p, (short)0, (int)bytes, 0x00020000); }
DI void dma16(__amdgpu_buffer_rsrc_t srd, unsigned voff, unsigned soff, unsigned lds_dst) {
  unsigned keep;
  asm volatile("s_mov_b32 %0, m0\n\ts_mov_b32 m0, %1\n\ts_nop 0\n\tbuffer_load_dwordx4 %2, %3, %4 offen lds\n\ts_mov_b32 m0, %0"
               : "=&s"(keep) : "s"(lds_dst), "v"(voff), "s"(srd), "s"(soff) : "memory");
}
DI bf16x8 ldsr128(lds_cptr p) { return *(const LAS bf16x8*)p; }
typedef short v4i16_t __attribute__((ext_vector_type(4)));
DI s16x4 ldstr(lds_cptr p) { return __builtin_bit_cast(s16x4, __builtin_amdgcn_ds_read_tr16_b64_v4i16((LAS v4i16_t*)p)); }
DI bf16x8 cat8(s16x4 a, s16x4 b) { return __builtin_shufflevector(a, b, 0, 1, 2, 3, 4, 5, 6, 7); }


template <int N> DI void wait_bar() { asm volatile("s_waitcnt vmcnt(%0) lgkmcnt(0)\n\ts_barrier" :: "i"(N) : "memory"); }
#define MFMA16(a, b, c) __builtin_amdgcn_mfma_f32_16x16x32_bf16((a), (b), (c), 0, 0, 0)
#define SGB(mask, n) __builtin_amdgcn_sched_group_barrier(mask, n, 0)
constexpr unsigned OOBS = 0x7f000000u, DROP = 0x38000u;
typedef unsigned long long u64;
extern "C" __device__ u64 hw_dispatch_id() __asm("llvm.amdgcn.dispatch.id");
constexpr int F_X = 0, F_KV = 256, F_C = 512, F_P = 1024;
DI int lane_id() { int l; asm volatile("v_mbcnt_lo_u32_b32 %0, -1, 0\n\tv_mbcnt_hi_u32_b32 %0, -1, %0" : "=v"(l)); return l; }
DI u64* flagp(char* ws, int i) { return (u64*)(ws + WS_FLAG) + i; }
DI void st16_wt(__amdgpu_buffer_rsrc_t srd, unsigned off, u32x4 w) { __builtin_amdgcn_raw_buffer_store_b128(w, srd, off, 0, 16); }
constexpr int L_V = 0, L_QA = 65536, L_QB = 98304, L_KS2 = 98304, L_KS0 = 131072;
constexpr float QSCALE = 0.08838834764831845f * 1.4426950408889634f;
constexpr float THR = 8.0f;
#define PIN(x) asm volatile("" : "+v"(x))
DI float max3f(float a, float b, float c) { float r; asm("v_max3_f32 %0, %1, %2, %3" : "=v"(r) : "v"(a), "v"(b), "v"(c)); return r; }
DI float max2f(float a, float b) { float r; asm("v_max_f32_e32 %0, %1, %2" : "=v"(r) : "v"(a), "v"(b)); return r; }
DI float swap32_max(float m) { auto rr = __builtin_amdgcn_permlane32_swap(__float_as_uint(m), __float_as_uint(m), false, false); return fmaxf(__uint_as_float(rr[0]), __uint_as_float(rr[1])); }
DI float swap32_add(float m) { auto rr = __builtin_amdgcn_permlane32_swap(__float_as_uint(m), __float_as_uint(m), false, false); return __uint_as_float(rr[0]) + __uint_as_float(rr[1]); }


constexpr int PB_STAGE = 32768;
DI constexpr int pb_slot(int s) { return (s & 3) * PB_STAGE; }
constexpr int PA_BASE = 32768;
struct PBsrc { __amdgpu_buffer_rsrc_t sX, sQ, s1; unsigned vL, vQ, v1, sXbase, sQbase, s1base; };
DI void pb_lanes(PBsrc& p, int wid, int lane) {
  const int c4 = (lane & 3) ^ (2 * ((wid >> 1) & 1));
  p.vL = (unsigned)lane * 16u;
  p.vQ = (unsigned)((((lane >> 2) & 3) * 4096 + (lane >> 4) * 32 + c4 * 8) * 2);
  p.v1 = lane < 32 ? (unsigned)((((lane >> 2) & 3) * 1376 + (lane >> 4) * 32 + c4 * 8) * 2) : OOBS;
}
DI void pb_piece(const PBsrc& p, unsigned lds0, int wid, int s, int m) {
  const unsigned b = lds0 + (unsigned)pb_slot(s);
  if (m < 2) dma16(p.sX, p.vL, p.sXbase + (unsigned)(m * 16384 + s * 1024), b + (unsigned)((wid * 2 + m) * 1024));
  else if (m == 2) dma16(p.sQ, p.vQ, p.sQbase + (unsigned)(s * 32 * 4096 * 2), b + 16384u + (unsigned)(wid * 2048));
  else dma16(p.s1, p.v1, p.s1base + (unsigned)(s * 32 * 1376 * 2), b + 16384u + 1024u + (unsigned)(wid * 2048));
}
template <int N0, int N> DI void wait_bar2(int wid) {
  asm volatile("s_cmp_eq_u32 %0, 0\n\ts_cbranch_scc1 1f\n\ts_waitcnt vmcnt(%2)\n\ts_branch 2f\n1:\n\ts_waitcnt vmcnt(%1)\n2:\n\ts_waitcnt lgkmcnt(0)\n\ts_barrier" :: "s"(wid), "i"(N0), "i"(N) : "memory", "scc");
}
DI void signal_w0(int wid, unsigned en, u64* p, u64 tok) {
  unsigned z = 0u;
  asm volatile("s_cmp_eq_u32 %0, 0\n\ts_cbranch_scc0 1f\n\ts_cmp_eq_u32 %4, 0\n\ts_cbranch_scc1 1f\n\ts_mov_b64 exec, 1\n\tglobal_store_dwordx2 %2, %3, %1 sc1\n\ts_mov_b64 exec, -1\n1:"
               :: "s"(wid), "s"(p), "v"(z), "v"(tok), "s"(en) : "memory", "scc");
}
DI void poll_w0(int wid, const u64* p, unsigned voff) {
  asm volatile("s_cmp_eq_u32 %0, 0\n\ts_cbranch_scc0 1f\n\tglobal_load_dwordx2 v[252:253], %2, %1 sc1\n1:" :: "s"(wid), "s"(p), "v"(voff) : "memory", "scc", "v252", "v253"); }
template <bool INV, bool POLLED> DI void check_w0(int wid, const u64* p, unsigned voff, u64 tok, u64 need) {
  unsigned cnt; u64 m;
  asm volatile("s_cmp_eq_u32 %2, 0\n\ts_cbranch_scc0 9f\n\ts_mov_b32 %1, 0x40000\n\t.if %8 == 0\n\ts_branch 3f\n\t.endif\n"
               "1:\n\tv_cmp_ne_u64_e64 %0, v[252:253], %5\n\ts_and_b64 %0, %0, %6\n\ts_cbranch_scc0 2f\n\ts_sub_u32 %1, %1, 1\n\ts_cmp_eq_u32 %1, 0\n\ts_cbranch_scc1 2f\n\ts_sleep 1\n"
               "3:\n\tglobal_load_dwordx2 v[252:253], %4, %3 sc1\n\ts_waitcnt vmcnt(0)\n\ts_branch 1b\n"
               "2:\n\t.if %7\n\tbuffer_inv sc1\n\t.endif\n9:"
               : "=&s"(m), "=&s"(cnt) : "s"(wid), "s"(p), "v"(voff), "v"(tok), "s"(need), "i"(INV ? 1 : 0), "i"(POLLED ? 1 : 0) : "memory", "scc", "v252", "v253");
}
template <int NF, int NS_> DI void wait_barf(unsigned fast) {
  asm volatile("s_cmp_eq_u32 %0, 0\n\ts_cbranch_scc1 1f\n\ts_waitcnt vmcnt(%1)\n\ts_branch 2f\n1:\n\ts_waitcnt vmcnt(%2)\n2:\n\ts_waitcnt lgkmcnt(0)\n\ts_barrier" :: "s"(fast), "i"(NF), "i"(NS_) : "memory", "scc");
}
DI void st16_if(unsigned en, u32x4 w, __amdgpu_buffer_rsrc_t srd, unsigned voff, unsigned soff) {
  asm volatile("s_cmp_eq_u32 %4, 0\n\ts_cbranch_scc1 1f\n\tbuffer_store_dwordx4 %0, %1, %2, %3 offen sc1\n\ts_nop 1\n1:" :: "v"(w), "v"(voff), "s"(srd), "s"(soff), "s"(en) : "memory", "scc");
}
DI void touch(__amdgpu_buffer_rsrc_t srd, unsigned voff, unsigned soff) {
  asm volatile("buffer_load_dword v254, %0, %1, %2 offen" :: "v"(voff), "s"(srd), "s"(soff) : "memory", "v254");
}

template <bool KT>
DI void phaseA(char* lds, char* ws, __amdgpu_buffer_rsrc_t sC, __amdgpu_buffer_rsrc_t sUK, __amdgpu_buffer_rsrc_t sUV, unsigned sCbase, unsigned sWbase,
               PBsrc pb, __amdgpu_buffer_rsrc_t sCode, unsigned t2so, u64 tok, int h, int mt, int wid, int lane, u32x4 (&kv)[16]) {
  const int c = lane & 15, g = lane >> 4, km = wid & 1, nq = wid >> 1;
  const unsigned vC = (unsigned)((lane >> 2) * 1024 + (((lane & 3) ^ (3 * ((lane >> 5) & 1))) * 16));
  const unsigned vW = (unsigned)((((lane >> 2) & 3) * 4096 + (lane >> 4) * 32 + ((lane & 3) ^ (2 * ((wid >> 1) & 1))) * 8) * 2);
  const unsigned lds0 = (unsigned)(uintptr_t)lds;
  auto piece = [&](int s, int m) {
    if (s < 16) {
      const unsigned b = lds0 + (unsigned)(PA_BASE + (s & 3) * 32768);
      if (m < 2) dma16(sC, vC, sCbase + (unsigned)(m * 16 * 1024 + s * 64), b + (unsigned)((wid * 2 + m) * 1024));
      else       dma16((m & 1) ? sUV : sUK, vW, sWbase + (unsigned)((s * 32 * 4096) * 2), b + 16384u + (unsigned)((wid * 2 + (m & 1)) * 1024));
    } else pb_piece(pb, lds0, wid, s - 16, m);
  };
  auto warmA = [&]() {
    const int l2 = lane_id(), id = (int)(blockIdx.x >> 3) * 8 + wid;
    touch(sC, (unsigned)l2 * 128u, (unsigned)id * 8192u);
    touch(sCode, id < 128 ? (unsigned)(l2 >> 3) * 8192u + (unsigned)(l2 & 7) * 128u : (unsigned)l2 * 128u, t2so);
  };
  auto warmB = [&]() {
    const int l2 = lane_id(), id = (int)(blockIdx.x >> 3) * 8 + wid, xg = (int)(blockIdx.x & 7);
    const bool q = id < 64;
    const unsigned vo = (unsigned)(l2 >> 3) * (q ? 8192u : 2752u) + (unsigned)(l2 & 7) * 128u;
    const unsigned so = q ? (unsigned)(id * 8 * 8192 + xg * 1024) : (id < 128 ? (unsigned)((id - 64) * 8 * 2752 + xg * 512) : OOBS);
    touch(q ? pb.sQ : pb.s1, vo, so);
  };
  const int cb = (128 * km + c) * 64 + ((g ^ (3 * ((c >> 3) & 1))) * 16);
  int wb[2];
#pragma unroll
  for (int par = 0; par < 2; ++par) wb[par] = 16384 + g * 4096 + nq * 512 + ((lane >> 2) & 3) * 64 + ((par ^ (g & 1)) * 32) + (lane & 3) * 8;
  f32x4 acc[8][4];
#pragma unroll
  for (int i = 0; i < 8; ++i)
#pragma unroll
    for (int j = 0; j < 4; ++j) acc[i][j] = f32x4{};
  bf16x8 cf[2][4], wf[2][4];
  auto rdW = [&](int s, int buf) {
    lds_cptr base = (lds_cptr)lds + (PA_BASE + (s & 3) * 32768);
#pragma unroll
    for (int j = 0; j < 4; ++j) wf[buf][j] = cat8(ldstr(base + wb[j & 1] + (j >> 1) * 256), ldstr(base + wb[j & 1] + 2048 + (j >> 1) * 256));
  };
  auto rdC = [&](int s, int half, int buf) {
    lds_cptr base = (lds_cptr)lds + (PA_BASE + (s & 3) * 32768);
#pragma unroll
    for (int i = 0; i < 4; ++i) cf[buf][i] = ldsr128(base + cb + (half * 4 + i) * 1024);
  };
  auto mm1 = [&](int wbuf, int cbuf, int half, int i) {
#pragma unroll
    for (int j = 0; j < 4; ++j) acc[half * 4 + i][j] = KT ? MFMA16(wf[wbuf][j], cf[cbuf][i], acc[half * 4 + i][j]) : MFMA16(cf[cbuf][i], wf[wbuf][j], acc[half * 4 + i][j]);
  };
  auto pin4 = [&](int half) {
#pragma unroll
    for (int i = 0; i < 4; ++i) asm volatile("" : "+v"(acc[half * 4 + i][0]), "+v"(acc[half * 4 + i][1]), "+v"(acc[half * 4 + i][2]), "+v"(acc[half * 4 + i][3]));
  };
  const int bi = mt;
  wait_bar<13>();
  rdW(0, 0); rdC(0, 0, 0);
#pragma unroll
  for (int s = 0; s < 16; ++s) {
    const int wbuf = s & 1;
    rdC(s, 1, 1);
    mm1(wbuf, 0, 0, 0); mm1(wbuf, 0, 0, 1); mm1(wbuf, 0, 0, 2); mm1(wbuf, 0, 0, 3);
    SGB(0x008, 2); SGB(0x100, 1); SGB(0x008, 2); SGB(0x100, 1); SGB(0x008, 2); SGB(0x100, 1); SGB(0x008, 2); SGB(0x100, 1); SGB(0x008, 8); SBAR(); pin4(0);
    if (KT && s == 10) { const int l2 = lane_id(); check_w0<true, true>(wid, flagp(ws, F_X), (unsigned)((((l2 & 16) ? 15 - bi : bi) * 16 + (l2 & 15)) * 8), tok, ~0ull); }
    if (s < 15) {
      if (s == 0) wait_bar<9>(); else if (s <= 2) wait_bar<11>(); else if (s == 3) wait_bar<10>(); else if (s == 5 || s == 6 || s == 7) wait_bar<9>(); else wait_bar<8>();
      SBAR();
      if (s == 12) pb_lanes(pb, wid, lane_id());
      if (KT && s == 4) signal_w0(wid, 1u, flagp(ws, F_X + blockIdx.x), tok);
      if (KT && s == 6) { const int l2 = lane_id(); poll_w0(wid, flagp(ws, F_X), (unsigned)((((l2 & 16) ? 15 - bi : bi) * 16 + (l2 & 15)) * 8)); }
      rdW(s + 1, wbuf ^ 1); rdC(s + 1, 0, 0);
      mm1(wbuf, 1, 1, 0); SGB(0x008, 1); SGB(0x100, 3); SGB(0x008, 1); SGB(0x100, 3); SGB(0x008, 1); SGB(0x100, 3); SGB(0x008, 1); SGB(0x100, 3); SBAR(); piece(s + 4, 0); SBAR();
      mm1(wbuf, 1, 1, 1); SBAR(); piece(s + 4, 1); SBAR();
      mm1(wbuf, 1, 1, 2); SBAR(); piece(s + 4, 2); SBAR();
      mm1(wbuf, 1, 1, 3); SBAR(); pin4(1); piece(s + 4, 3); if (s == 0) warmA(); if (s == 4) warmB(); SBAR();
    } else {
      mm1(wbuf, 1, 1, 0); SBAR(); piece(19, 0); SBAR(); mm1(wbuf, 1, 1, 1); SBAR(); piece(19, 1); SBAR(); mm1(wbuf, 1, 1, 2); SBAR(); piece(19, 2); SBAR(); mm1(wbuf, 1, 1, 3); SBAR(); piece(19, 3); SBAR();
    }
  }
  if (KT) {
#pragma unroll
    for (int i = 0; i < 8; ++i)
#pragma unroll
      for (int dp = 0; dp < 2; ++dp)
        kv[i * 2 + dp] = (u32x4){cvtpk(acc[i][2 * dp][0], acc[i][2 * dp][1]), cvtpk(acc[i][2 * dp][2], acc[i][2 * dp][3]), cvtpk(acc[i][2 * dp + 1][0], acc[i][2 * dp + 1][1]), cvtpk(acc[i][2 * dp + 1][2], acc[i][2 * dp + 1][3])};
  } else {
#pragma unroll
    for (int ip = 0; ip < 4; ++ip)
#pragma unroll
      for (int j = 0; j < 4; ++j) {
        unsigned a0 = cvtpk(acc[2 * ip][j][0], acc[2 * ip][j][1]), a1 = cvtpk(acc[2 * ip][j][2], acc[2 * ip][j][3]);
        unsigned b0 = cvtpk(acc[2 * ip + 1][j][0], acc[2 * ip + 1][j][1]), b1 = cvtpk(acc[2 * ip + 1][j][2], acc[2 * ip + 1][j][3]);
        auto r0 = __builtin_amdgcn_permlane32_swap(a0, b0, false, false);
        auto r1 = __builtin_amdgcn_permlane32_swap(a1, b1, false, false);
        kv[ip * 4 + j] = (u32x4){r0[0], r1[0], r0[1], r1[1]};
      }
  }
}

template <bool FFN>
DI void phaseB(char* lds, char* ws, PBsrc pb, __amdgpu_buffer_rsrc_t sK, u64 tok, int h, int bi, int wid, int lane, const u32x4 (&kv)[16]) {
  pb_lanes(pb, wid, lane);
  const int c = lane & 15, g = lane >> 4, rgp = wid >> 1, ch = wid & 1;
  const unsigned lds0 = (unsigned)(uintptr_t)lds;
  constexpr int NP = 4;
  auto kvpiece = [&](unsigned base, int tile, int ldsoff, int m) {
    dma16(sK, pb.vL, base + (unsigned)(h * 524288 + tile * 16384 + (wid * 2 + m) * 1024), lds0 + (unsigned)(ldsoff + (wid * 2 + m) * 1024));
  };
  const int v0slot = L_V + ((0 - (2 * bi + 2)) & 3) * 16384;
  constexpr int NJ = FFN ? 6 : 4;
  const int xb = rgp * 4096 + lane * 16;
  int wb[2];
#pragma unroll
  for (int par = 0; par < 2; ++par) wb[par] = 16384 + g * 4096 + ((lane >> 2) & 3) * 64 + ((par ^ (g & 1)) * 32) + (lane & 3) * 8;
  f32x4 acc[4][NJ];
#pragma unroll
  for (int i = 0; i < 4; ++i)
#pragma unroll
    for (int j = 0; j < NJ; ++j) acc[i][j] = f32x4{};
  bf16x8 xf[2][2], wf[2][NJ];
  auto rdW = [&](int s, int buf) {
    lds_cptr base = (lds_cptr)lds + pb_slot(s);
#pragma unroll
    for (int j = 0; j < NJ; ++j) { const int u = (j < 4) ? (2 * ch + (j >> 1)) : (4 + ch);
      wf[buf][j] = cat8(ldstr(base + wb[j & 1] + u * 256), ldstr(base + wb[j & 1] + 2048 + u * 256)); }
  };
  auto rdX = [&](int s, int half, int buf) {
    lds_cptr base = (lds_cptr)lds + pb_slot(s);
#pragma unroll
    for (int i = 0; i < 2; ++i) xf[buf][i] = ldsr128(base + xb + (half * 2 + i) * 1024);
  };
  auto mm1 = [&](int wbuf, int xbuf, int half, int i) {
#pragma unroll
    for (int j = 0; j < NJ; ++j) acc[half * 2 + i][j] = MFMA16(wf[wbuf][j], xf[xbuf][i], acc[half * 2 + i][j]);
  };
  auto pin1 = [&](int i) {
#pragma unroll
    for (int j = 0; j < NJ; j += 2) asm volatile("" : "+v"(acc[i][j]), "+v"(acc[i][j + 1]));
  };
  const int km = wid & 1, nq = wid >> 1;
  const bool kt = wid < 4;
  const unsigned kvV = (unsigned)((g >> 1) * 1024 + ((g & 1) * 32 + c) * 16);
  const unsigned kvS = kt ? (unsigned)(((h * 32 + bi * 4 + 2 * km) * 16 + 4 * nq) * 1024) : (16u << 20) + (unsigned)(((h * 32 + bi * 4 + 2 * km) * 16 + 8 * (nq - 2)) * 1024);
  const unsigned fast = (unsigned)__builtin_amdgcn_readfirstlane((int)((unsigned)(bi - 2) >> 31)), slow = (unsigned)__builtin_amdgcn_readfirstlane((int)(fast ^ 1u));
  auto kvstore = [&](int n, unsigned en) {
    const int i = n >> 1, dp = n & 1, ip = n >> 2, j = n & 3;
    const unsigned ok = (unsigned)(((i >> 2) * 16 + ((i >> 1) & 1) * 8 + 2 * dp) * 1024 + (i & 1) * 256);
    const unsigned ov = (unsigned)(((ip >> 1) * 16 + (j >> 1) * 4 + 2 * (ip & 1)) * 1024 + (j & 1) * 256);
    st16_if(en, kv[n], sK, kvV, kvS + (kt ? ok : ov));
  };
  wait_bar<12>();
  rdW(0, 0); rdX(0, 0, 0);
#pragma unroll
  for (int s = 0; s < 16; ++s) {
    const int wbuf = s & 1;
    rdX(s, 1, 1);
    mm1(wbuf, 0, 0, 0); mm1(wbuf, 0, 0, 1);
    SGB(0x008, 1); SGB(0x100, 1); SGB(0x008, 1); SGB(0x100, 1); SGB(0x008, 2 * NJ - 2); SBAR(); pin1(0); pin1(1);
    if (s == 14) check_w0<true, true>(wid, flagp(ws, F_KV + h * 8), 0u, tok, 1ull);
    if (s < 15) {
      if (s == 0) wait_bar<8>(); else if (s == 1 || s == 9) wait_bar<10>();
      else if (s <= 8) { if (s % 3 == 0) wait_barf<12, 10>(fast); else wait_barf<12, 11>(fast); }
      else if (s <= 11) wait_barf<8, 11>(fast); else if (s == 12) wait_barf<8, 10>(fast);
      else if (s == 13) wait_bar2<0, 4>(wid); else wait_bar<0>();
      SBAR();
      if (s == 10) signal_w0(wid, fast, flagp(ws, F_KV + h * 8 + bi), tok);
      if (s == 14) signal_w0(wid, slow, flagp(ws, F_KV + h * 8 + bi), tok);
      if (s == 12) poll_w0(wid, flagp(ws, F_KV + h * 8), 0u);
      rdW(s + 1, wbuf ^ 1); rdX(s + 1, 0, 0);
      mm1(wbuf, 1, 1, 0);
#pragma unroll
      for (int q = 0; q < NJ; ++q) { SGB(0x008, 1); SGB(0x100, 3); }
      SBAR(); pin1(2);
      if (s + 4 < 16) { pb_piece(pb, lds0, wid, s + 4, 0); pb_piece(pb, lds0, wid, s + 4, 1); SBAR(); }
      if (s <= 7) { kvstore(2 * s, fast); SBAR(); }
      mm1(wbuf, 1, 1, 1); SBAR(); pin1(3);
      if (s + 4 < 16) { pb_piece(pb, lds0, wid, s + 4, 2); pb_piece(pb, lds0, wid, s + 4, 3); SBAR(); }
      if (s <= 7) { kvstore(2 * s + 1, fast); SBAR(); }
      if (s <= 11) { const int n0 = s + (s + 2) / 3; kvstore(n0, slow); if (s % 3 == 0) kvstore(n0 + 1, slow); SBAR(); }
    } else {
      wait_bar2<0, 63>(wid);
      kvpiece(0u, 0, L_KS0, 0); kvpiece(0u, 0, L_KS0, 1); kvpiece(0u, 1, L_KS0 + 16384, 0); kvpiece(0u, 1, L_KS0 + 16384, 1);
      kvpiece(16u << 20, 0, v0slot, 0); kvpiece(16u << 20, 0, v0slot, 1);
      SBAR();
      mm1(wbuf, 1, 1, 0); mm1(wbuf, 1, 1, 1);
    }
  }
  char* qdst = lds + ((rgp < 2) ? L_QA : L_QB);
  const __amdgpu_buffer_rsrc_t sH = mk_srd(ws + WS_H, 128u * 44u * 1024u);
#pragma unroll
  for (int ii = 0; ii < 4; ++ii) {
    const int rb32 = 2 * (rgp & 1) + (ii >> 1), r = 16 * (ii & 1) + c;
#pragma unroll
    for (int dpp = 0; dpp < 2; ++dpp) {
      const int ks = 2 * (2 * ch + dpp) + (g >> 1);
      const f32x4 a = acc[ii][2 * dpp] * QSCALE, b = acc[ii][2 * dpp + 1] * QSCALE;
      u32x4 w = {cvtpk(a[0], a[1]), cvtpk(a[2], a[3]), cvtpk(b[0], b[1]), cvtpk(b[2], b[3])};
      *(u32x4*)(qdst + ((rb32 * 8 + ks) * 64 + (g & 1) * 32 + r) * 16) = w;
    }
    if (FFN) {
      const int rb16 = ((rgp < 2) ? (15 - bi) * 8 : bi * 8) + 4 * (rgp & 1) + ii;
      const bool valid = (h * 64 + ch * 32) < 1376;
      f32x4 a = acc[ii][NJ - 2], b = acc[ii][NJ - 1];
#pragma unroll
      for (int e = 0; e < 4; ++e) { a[e] = valid ? fmaxf(a[e], 0.f) : 0.f; b[e] = valid ? fmaxf(b[e], 0.f) : 0.f; }
      u32x4 w = {cvtpk(a[0], a[1]), cvtpk(a[2], a[3]), cvtpk(b[0], b[1]), cvtpk(b[2], b[3])};
      st16_wt(sH, (unsigned)(((rb16 * 44 + 2 * h + ch) * 64 + lane) * 16), w);
    }
  }
}

DI void store_ctx(__amdgpu_buffer_rsrc_t sCtx, const float (&y)[16], int rowblk16, int h, int db, int r, int hi) {
#pragma unroll
  for (int sx = 0; sx < 2; ++sx) {
    u32x4 w = {cvtpk(y[8 * sx], y[8 * sx + 1]), cvtpk(y[8 * sx + 2], y[8 * sx + 3]), cvtpk(y[8 * sx + 4], y[8 * sx + 5]), cvtpk(y[8 * sx + 6], y[8 * sx + 7])};
    st16_wt(sCtx, (unsigned)((((rowblk16 + (r >> 4)) * 128 + h * 4 + db) * 64 + (2 * sx + hi) * 16 + (r & 15)) * 16), w);
  }
}

template <int OFF> DI void dma2(__amdgpu_buffer_rsrc_t srd, unsigned voff, unsigned soff, unsigned lds_dst) {
  asm volatile("s_mov_b32 m0, %0\n\ts_nop 0\n\tbuffer_load_dwordx4 %1, %2, %3 offen offset:%4 lds\n\tbuffer_load_dwordx4 %1, %2, %3 offen offset:%5 lds"
               :: "s"(lds_dst), "v"(voff), "s"(srd), "s"(soff), "i"(OFF), "i"(OFF + 1024) : "memory");
}
DI unsigned rdl(unsigned v, int l) { return (unsigned)__builtin_amdgcn_readlane((int)v, l); }

DI void phaseC(char* lds, char* ws, __amdgpu_buffer_rsrc_t sK, const u16* wo, const u16* w2, u64 tok, int h, int bi, int wid, int lane) {
  const unsigned pvo = (unsigned)(lane & 7) * 8u;
  const __amdgpu_buffer_rsrc_t sCtx = mk_srd(ws + WS_CTX, 16u << 20);
  const int rg = wid & 3, grp = wid >> 2, r = lane & 31, hi = lane >> 5;
  const unsigned lds0 = (unsigned)(uintptr_t)lds;
  const int P1 = 2 * bi + 2;
  const unsigned vL = (unsigned)lane * 16u;
  unsigned tKs, tVs, tC;
  {
    const unsigned hbase = (unsigned)(h * 524288);
    auto tix = [&](int g) { return g < P1 ? g : P1 + 2 * (g - P1) + grp; };
    auto kslot = [&](int g) { return ((grp && g >= P1) ? L_KS2 : L_KS0) + (g & 1) * 16384; };
    auto vslot = [&](int g) { return g < P1 ? L_V + ((g - P1) & 3) * 16384 : ((grp && g == 16) ? L_KS2 + 16384 : L_V + (2 * grp + (g & 1)) * 16384); };
    const int k = lane, gk = k + 1;
    const bool ksh = gk < P1, kok = gk <= 16;
    const unsigned ko = ksh ? wid * 2048 : rg * 4096;
    tKs = kok ? (hbase + (unsigned)(tix(gk) * 16384) + ko) | (unsigned)(kslot(gk) >> 14) : (OOBS | 14u);
    const bool vsh = k < P1;
    const int gv = (vsh || !grp) ? k : k - 1;
    const bool vok = vsh || gv >= P1;
    const unsigned vo = vsh ? wid * 2048 : rg * 4096;
    tVs = vok ? ((16u << 20) + hbase + (unsigned)(tix(gv) * 16384) + vo) | (unsigned)(vslot(gv) >> 14) : (OOBS | 14u);
    const int g = lane - 1;
    const bool first = (g < 0) || (grp && g + 1 == P1);
    const bool mask = (g + 1 == 16) || (grp && (g + 1 == P1 - 2 || g + 1 == P1 - 1));
    const unsigned qsrc = (g < 0 && grp) ? (unsigned)L_QB : (unsigned)L_QA;
    tC = (unsigned)(kslot(g + 1) >> 14) | ((unsigned)((g < 0 ? L_KS0 : vslot(g)) >> 14) << 4) | ((unsigned)(kslot(g + 2) >> 14) << 8) | ((qsrc >> 14) << 12) |
         (first ? 1u << 16 : 0u) | (mask ? 1u << 17 : 0u) | ((kok && !ksh) ? 1u << 18 : 0u) | ((vok && !vsh) ? 1u << 19 : 0u) |
         ((unsigned)((((grp && g + 1 < P1) ? bi : 15 - bi) * 4 + rg - tix(g + 1) * 2) & 255) << 24);
  }
  const float rl = (float)(r - 4 * hi) + 0.5f;
  bf16x8 qf[8];
  f32x16 o[4], S1, T0, T1, negm;
  u32x4 pk0 = {}, pk1 = {}, pk2, pk3;
  float m = 0.f, l = 0.f, lB = 0.f, ma, mb;
  bf16x8 k0, k1, k2, k3, v0, v1, v2, v3;
#pragma unroll
  for (int d = 0; d < 4; ++d) o[d] = f32x16{};
  S1 = f32x16{};
#define KF(i) ldsr128(kp + (i) * 1024)
#define VF(db, ks) ldsr128(((db) < 2 ? vpl : vpl_hi) + (((db) & 1) * 4 + (ks)) * 1024)
#define EX(v) __builtin_amdgcn_exp2f(v)
#define BFR(x) __builtin_bit_cast(bf16x8, x)
#define ADD2(a0, a1) asm("v_add_f32_e32 %0, %0, %2\n\tv_add_f32_e32 %1, %1, %3" : "+v"(sa), "+v"(sb) : "v"(a0), "v"(a1))
#define MX2(X, i) asm("v_max3_f32 %0, %0, %2, %3\n\tv_max3_f32 %1, %1, %4, %5" : "+v"(ma), "+v"(mb) : "v"(X[i]), "v"(X[i + 1]), "v"(X[i + 2]), "v"(X[i + 3]))
  unsigned ksrc = 0x7f000000u, kdst = lds0 + 0x38000u, vsrc = 0x7f000000u, vdst = lds0 + 0x38000u, dfl = 0;
#define HOOK0() do { if (dfl & 1) dma2<0>(sK, vL, ksrc, kdst); SBAR(); } while (0)
#define HOOK1() do { if (dfl & 2) dma2<2048>(sK, vL, ksrc, kdst); SBAR(); } while (0)
#define HOOK2() do { if (dfl & 1) dma2<0>(sK, vL, vsrc, vdst); SBAR(); } while (0)
#define HOOK3() do { if (dfl & 4) dma2<2048>(sK, vL, vsrc, vdst); SBAR(); } while (0)
#define PLAN(k) do { const unsigned a_ = rdl(tKs, k), b_ = rdl(tVs, k); dfl = 1u | ((rdl(tC, k) >> 17) & 6u); ksrc = a_ & ~15u; vsrc = b_ & ~15u; \
    kdst = lds0 + (a_ & 15u) * 16384u + ((dfl & 2u) ? rg * 4096u : wid * 2048u); vdst = lds0 + (b_ & 15u) * 16384u + ((dfl & 4u) ? rg * 4096u : wid * 2048u); } while (0)
  #pragma unroll 1
  for (int g = -1; g < 16; ++g) {
    const unsigned tc = rdl(tC, g + 1);
    const lds_cptr kp = (lds_cptr)lds + (lane * 16 + (tc & 15u) * 16384u);
    const lds_cptr vp = (lds_cptr)lds + (lane * 16 + ((tc >> 4) & 15u) * 16384u);
    const lds_cptr vpl = vp + grp * 8192, vpl_hi = vp + (8192 - grp * 8192);
    const bool first_next = (tc >> 16) & 1u;
    dfl = 0;
    if (grp == 0 || g <= 0) {
      if (g < 0) asm volatile("s_waitcnt vmcnt(4) lgkmcnt(0)\n\ts_barrier" ::: "memory");
      else {
        const int gk = g + 2 > 16 ? 16 : g + 2, tmax = gk < P1 ? gk : 2 * gk - P1 + 1;
        asm volatile("s_waitcnt vmcnt(0)" ::: "memory");
        check_w0<false, true>(wid, flagp(ws, F_KV + h * 8), pvo, tok, (2ull << (tmax >> 2)) - 1ull);
        asm volatile("s_waitcnt lgkmcnt(0)\n\ts_barrier" ::: "memory");
        PLAN(g + 1);
      }
      poll_w0(wid, flagp(ws, F_KV + h * 8), pvo);
            k0 = KF(0); k1 = KF(1); k2 = KF(2); k3 = KF(3);
    }
    if (first_next) {
      const lds_cptr qp = (lds_cptr)lds + (lane * 16 + rg * 8192 + ((tc >> 12) & 15u) * 16384u);
#pragma unroll
      for (int ks = 0; ks < 8; ++ks) qf[ks] = ldsr128(qp + ks * 1024);
      negm = f32x16{};
    }
    PIN(negm);
    SBAR();
    {
      float sa = 0.f, sb = 0.f;
      bf16x8 k4, k5;
      T0 = MFMA(k0, qf[0], negm); k4 = KF(4); k5 = KF(5); ADD2(S1[0], S1[1]); pk2[0] = cvtpk(S1[0], S1[1]); PIN(pk2); SBAR();
      T0 = MFMA(k1, qf[1], T0); ADD2(S1[2], S1[3]); pk2[1] = cvtpk(S1[2], S1[3]); PIN(pk2); SBAR();
      T0 = MFMA(k2, qf[2], T0); k0 = KF(6); k1 = KF(7); ADD2(S1[4], S1[5]); pk2[2] = cvtpk(S1[4], S1[5]); PIN(pk2); SBAR();
      T0 = MFMA(k3, qf[3], T0); ADD2(S1[6], S1[7]); pk2[3] = cvtpk(S1[6], S1[7]); PIN(pk2); SBAR();
      HOOK0();
      T0 = MFMA(k4, qf[4], T0); k2 = KF(8); k3 = KF(9); ADD2(S1[8], S1[9]); pk3[0] = cvtpk(S1[8], S1[9]); PIN(pk3); SBAR();
      T0 = MFMA(k5, qf[5], T0); ADD2(S1[10], S1[11]); pk3[1] = cvtpk(S1[10], S1[11]); PIN(pk3); SBAR();
      T0 = MFMA(k0, qf[6], T0); k4 = KF(10); k5 = KF(11); ADD2(S1[12], S1[13]); pk3[2] = cvtpk(S1[12], S1[13]); PIN(pk3); SBAR();
      T0 = MFMA(k1, qf[7], T0); ADD2(S1[14], S1[15]); pk3[3] = cvtpk(S1[14], S1[15]); PIN(pk3); SBAR();
      HOOK1();
      l += sa + sb; PIN(pk2); PIN(pk3);
      T1 = MFMA(k2, qf[0], negm); k0 = KF(12); k1 = KF(13); SBAR();
      T1 = MFMA(k3, qf[1], T1); SBAR();
      T1 = MFMA(k4, qf[2], T1); k2 = KF(14); k3 = KF(15); SBAR();
      T1 = MFMA(k5, qf[3], T1); SBAR();
      HOOK2();
      ma = T0[0]; mb = T0[1];
      T1 = MFMA(k0, qf[4], T1); v0 = VF(0, 0); v1 = VF(1, 0); MX2(T0, 2); SBAR();
      T1 = MFMA(k1, qf[5], T1); MX2(T0, 6); SBAR();
      T1 = MFMA(k2, qf[6], T1); v2 = VF(2, 0); v3 = VF(3, 0); MX2(T0, 10); SBAR();
      T1 = MFMA(k3, qf[7], T1); ma = max3f(ma, T0[14], T0[15]); SBAR();
      HOOK3();
    }
    if (grp == 1 && g >= 0) {
      asm volatile("s_waitcnt vmcnt(0) lgkmcnt(0)\n\ts_barrier" ::: "memory");
      v0 = VF(0, 0); v1 = VF(1, 0); v2 = VF(2, 0); v3 = VF(3, 0);
      if (g < 15) PLAN(g + 2); else dfl = 0;
          } else dfl = 0;
    {
      const lds_cptr kp = (lds_cptr)lds + (lane * 16 + ((tc >> 8) & 15u) * 16384u);
      bf16x8 v4, v5;
      o[0] = MFMA(v0, BFR(pk0), o[0]); v4 = VF(0, 1); v5 = VF(1, 1); SBAR();
      o[1] = MFMA(v1, BFR(pk0), o[1]); SBAR();
      o[2] = MFMA(v2, BFR(pk0), o[2]); v0 = VF(2, 1); v1 = VF(3, 1); MX2(T1, 0); MX2(T1, 4); SBAR();
      o[3] = MFMA(v3, BFR(pk0), o[3]); MX2(T1, 8); MX2(T1, 12); SBAR();
      ma = max2f(ma, mb);
      bool resc = false; float fsc = 1.f;
      if (__builtin_expect(((tc >> 16) & 3u) != 0u || __any(ma > THR), 0)) {
        if ((tc >> 17) & 1u) {
          const float lfB = ((float)(((int)tc >> 24) * 32 + 64) + rl) * 1.0e30f;
#pragma unroll
          for (int i = 0; i < 16; ++i) { const int kk = (i & 3) + 8 * (i >> 2); float t0_, t1_;
            asm("v_add_f32_e32 %2, %5, %4\n\tv_add_f32_e32 %3, %6, %4\n\tv_min_f32_e32 %0, %0, %2\n\tv_min_f32_e32 %1, %1, %3"
                : "+v"(T0[i]), "+v"(T1[i]), "=&v"(t0_), "=&v"(t1_) : "v"(lfB), "i"(__builtin_bit_cast(int, -(float)(kk + 64) * 1.0e30f)), "i"(__builtin_bit_cast(int, -(float)(kk + 96) * 1.0e30f))); }
        }
        ma = T0[0]; mb = T0[1];
        MX2(T0, 2); MX2(T0, 6); MX2(T0, 10); ma = max3f(ma, T0[14], T0[15]); MX2(T1, 0); MX2(T1, 4); MX2(T1, 8); MX2(T1, 12);
        const float a = swap32_max(max2f(ma, mb));
        float dl;
        if (first_next) { dl = fmaxf(a, -1.0e29f); lB = l; l = 0.f; m = dl; }
        else { dl = fmaxf(a, 0.f); m += dl; fsc = EX(-dl); l *= fsc; resc = true; }
#pragma unroll
        for (int i = 0; i < 16; ++i) { T0[i] -= dl; T1[i] -= dl; negm[i] = -m; }
      }
      SBAR();
      float sa = 0.f, sb = 0.f;
#define EX4(X, i) do { X[i] = EX(X[i]); X[i + 1] = EX(X[i + 1]); X[i + 2] = EX(X[i + 2]); X[i + 3] = EX(X[i + 3]); PIN(X); } while (0)
#define EX2(X, i) do { X[i] = EX(X[i]); X[i + 1] = EX(X[i + 1]); PIN(X); } while (0)
      HOOK0();
      o[0] = MFMA(v4, BFR(pk1), o[0]); v2 = VF(0, 2); v3 = VF(1, 2); EX4(T0, 0); SBAR();
      o[1] = MFMA(v5, BFR(pk1), o[1]); EX4(T0, 4); SBAR();
      o[2] = MFMA(v0, BFR(pk1), o[2]); v4 = VF(2, 2); v5 = VF(3, 2); EX4(T0, 8); SBAR();
      o[3] = MFMA(v1, BFR(pk1), o[3]); EX4(T0, 12); SBAR();
      HOOK1();
      o[0] = MFMA(v2, BFR(pk2), o[0]); v0 = VF(0, 3); v1 = VF(1, 3); EX2(T1, 0); ADD2(T0[0], T0[1]); pk0[0] = cvtpk(T0[0], T0[1]); PIN(pk0); SBAR();
      o[1] = MFMA(v3, BFR(pk2), o[1]); EX2(T1, 2); ADD2(T0[2], T0[3]); pk0[1] = cvtpk(T0[2], T0[3]); PIN(pk0); SBAR();
      o[2] = MFMA(v4, BFR(pk2), o[2]); v2 = VF(2, 3); v3 = VF(3, 3); EX2(T1, 4); ADD2(T0[4], T0[5]); pk0[2] = cvtpk(T0[4], T0[5]); PIN(pk0); SBAR();
      o[3] = MFMA(v5, BFR(pk2), o[3]); EX2(T1, 6); ADD2(T0[6], T0[7]); pk0[3] = cvtpk(T0[6], T0[7]); PIN(pk0); SBAR();
      HOOK2();
      o[0] = MFMA(v0, BFR(pk3), o[0]); k0 = KF(0); k1 = KF(1); EX2(T1, 8); ADD2(T0[8], T0[9]); pk1[0] = cvtpk(T0[8], T0[9]); PIN(pk1); SBAR();
      o[1] = MFMA(v1, BFR(pk3), o[1]); EX2(T1, 10); ADD2(T0[10], T0[11]); pk1[1] = cvtpk(T0[10], T0[11]); PIN(pk1); SBAR();
      o[2] = MFMA(v2, BFR(pk3), o[2]); k2 = KF(2); k3 = KF(3); EX2(T1, 12); ADD2(T0[12], T0[13]); pk1[2] = cvtpk(T0[12], T0[13]); PIN(pk1); SBAR();
      o[3] = MFMA(v3, BFR(pk3), o[3]); EX2(T1, 14); ADD2(T0[14], T0[15]); pk1[3] = cvtpk(T0[14], T0[15]); PIN(pk1); SBAR();
      HOOK3();
      l += sa + sb;
      S1 = T1;
      if (__builtin_expect(resc, 0)) {
#pragma unroll
        for (int d = 0; d < 4; ++d)
#pragma unroll
          for (int i = 0; i < 16; ++i) o[d][i] *= fsc;
      }
      if (first_next && g >= 0) {
        const float inv = 1.0f / swap32_add(lB);
#pragma unroll
        for (int d = 0; d < 4; ++d) {
          float y[16];
#pragma unroll
          for (int i = 0; i < 16; ++i) y[i] = o[d][i] * inv;
          store_ctx(sCtx, y, bi * 8 + 2 * rg, h, d ^ 2, r, hi);
          o[d] = f32x16{};
        }
      }
      if (g == 10) {
        const int role = (int)(blockIdx.x & 7) >> 1, id = (int)(blockIdx.x >> 3) * 8 + wid;
        touch(role == 3 ? mk_srd(w2, 1376u * 512u * 2u) : mk_srd(wo, 4096u * 512u * 2u), (unsigned)lane * 128u, id < 176 ? (unsigned)((role == 3 ? 0 : role * 22 * 65536) + id * 8192) : OOBS);
      }
      if (grp == 1 && g == 14) {
        const unsigned src = (16u << 20) + (unsigned)(h * 524288 + (P1 + 2 * (16 - P1) + 1) * 16384 + rg * 4096), dst = lds0 + (unsigned)(L_KS2 + 16384 + rg * 4096);
        dma2<0>(sK, vL, src, dst); dma2<2048>(sK, vL, src, dst);
      }
    }
  }
  if (grp == 0) asm volatile("s_waitcnt vmcnt(0) lgkmcnt(0)\n\ts_barrier" ::: "memory");
    {
    const lds_cptr vp = (lds_cptr)lds + (lane * 16 + (grp ? L_KS2 + 16384 : L_V));
    const lds_cptr vpl = vp + grp * 8192, vpl_hi = vp + (8192 - grp * 8192);
#pragma unroll
    for (int i = 0; i < 16; ++i) l += S1[i];
    pk2 = pack8(S1, 0); pk3 = pack8(S1, 1);
#pragma unroll
    for (int d = 0; d < 4; ++d) { o[d] = MFMA(VF(d, 0), BFR(pk0), o[d]); o[d] = MFMA(VF(d, 1), BFR(pk1), o[d]); o[d] = MFMA(VF(d, 2), BFR(pk2), o[d]); o[d] = MFMA(VF(d, 3), BFR(pk3), o[d]); }
  }
  {
    l = swap32_add(l);
    char* mybuf = lds + grp * 32768 + rg * 8192;
    const char* pbuf = lds + (grp ^ 1) * 32768 + rg * 8192;
    float* ml = (float*)(lds + L_QA);
    asm volatile("s_waitcnt lgkmcnt(0)\n\ts_barrier" ::: "memory");
#pragma unroll
    for (int t = 0; t < 2; ++t)
#pragma unroll
      for (int q = 0; q < 4; ++q) {
        f32x4 v = {o[2 + t][4 * q], o[2 + t][4 * q + 1], o[2 + t][4 * q + 2], o[2 + t][4 * q + 3]};
        *(f32x4*)(mybuf + ((t * 4 + q) * 64 + lane) * 16) = v;
      }
    if (hi == 0) { ml[wid * 64 + r] = m; ml[wid * 64 + 32 + r] = l; }
    asm volatile("s_waitcnt lgkmcnt(0)\n\ts_barrier" ::: "memory");
    const int pw = wid ^ 4;
    const float m2 = ml[pw * 64 + r], l2 = ml[pw * 64 + 32 + r];
    const float mm = fmaxf(m, m2);
    const float f1 = EX(m - mm), f2 = EX(m2 - mm);
    const float inv = 1.0f / (l * f1 + l2 * f2);
    const float g1 = f1 * inv, g2 = f2 * inv;
#pragma unroll
    for (int t = 0; t < 2; ++t) {
      float y[16];
#pragma unroll
      for (int q = 0; q < 4; ++q) {
        const f32x4 v = *(const f32x4*)(pbuf + ((t * 4 + q) * 64 + lane) * 16);
#pragma unroll
        for (int e = 0; e < 4; ++e) y[4 * q + e] = o[t][4 * q + e] * g1 + v[e] * g2;
      }
      store_ctx(sCtx, y, (15 - bi) * 8 + 2 * rg, h, 2 * grp + t, r, hi);
    }
  }
#undef KF
#undef VF
}


typedef unsigned u32x2 __attribute__((ext_vector_type(2)));
DI void phaseD(char* lds, char* ws, const u16* x, const u16* wo, const u16* w2, u16* out, u64 tok, int h, int bi, int wid, int lane) {
  const int b = blockIdx.x, xcd = b & 7, jj = b >> 3, role = xcd >> 1, mt = (xcd & 1) * 8 + (jj >> 2), nt = jj & 3;
  const int c = lane & 15, g = lane >> 4, kgrp = wid >> 2, wr = (wid >> 1) & 1, wc = wid & 1;
  const unsigned lds0 = (unsigned)(uintptr_t)lds;
  const bool ish = role == 3;
  const int s0 = ish ? 0 : role * 22;
  constexpr int NS = 22;
  const __amdgpu_buffer_rsrc_t sA = ish ? mk_srd(ws + WS_H, 128u * 44u * 1024u) : mk_srd(ws + WS_CTX, 16u << 20);
  const __amdgpu_buffer_rsrc_t sW = ish ? mk_srd(w2, 1376u * 512u * 2u) : mk_srd(wo, 4096u * 512u * 2u);
  const unsigned vL = (unsigned)lane * 16u;
  const unsigned aRow = (unsigned)((mt * 8 + wid) * (ish ? 44 : 128) + s0 * 2) * 1024u;
  unsigned vW[2];
#pragma unroll
  for (int m = 0; m < 2; ++m) vW[m] = (unsigned)(((m * 4 + ((lane >> 2) & 3)) * 512 + (lane >> 4) * 32 + ((lane & 3) ^ (2 * m)) * 8) * 2);
  const unsigned wBase = (unsigned)((wid * 8 * 512 + nt * 128) * 2) + (unsigned)s0 * 65536u;
  const unsigned dw = lds0 + (unsigned)(wid * 2048);
  const unsigned aLate = role == 2 ? OOBS : aRow;
  auto pA = [&](int s, int m) { dma16(sA, vL, (s < 20 ? aRow : aLate) + (unsigned)(s * 2048 + m * 1024), dw + (unsigned)((s % 5) * 32768 + m * 1024)); };
  auto pW = [&](int s, int m) { dma16(sW, vW[m], wBase + (unsigned)s * 65536u, dw + (unsigned)((s % 5) * 32768 + 16384 + m * 1024)); };
  asm volatile("s_waitcnt lgkmcnt(0)\n\ts_barrier" ::: "memory");
#pragma unroll
  for (int s = 0; s < 5; ++s) { pW(s, 0); pW(s, 1); }
  wait_bar<10>();
  signal_w0(wid, 1u, flagp(ws, F_C + bi * 32 + h), tok); signal_w0(wid, 1u, flagp(ws, F_C + (15 - bi) * 32 + h), tok);
  { check_w0<false, false>(wid, flagp(ws, F_C + mt * 32), (unsigned)(lane & 31) * 8u, tok, ~0ull); }
  asm volatile("s_barrier" ::: "memory");
#pragma unroll
  for (int s = 0; s < 5; ++s) { pA(s, 0); pA(s, 1); }
  const int ab = (wr * 4 + 2 * kgrp) * 2048 + kgrp * 1024 + lane * 16, abx = (wr * 4 + 2 - 2 * kgrp) * 2048 + kgrp * 1024 + lane * 16;
  int wb[2][2];
  {
    const int kq0 = ish ? g : 4 * (g >> 1) + (g & 1), kst = ish ? 4 : 2;
#pragma unroll
    for (int par = 0; par < 2; ++par)
#pragma unroll
      for (int t = 0; t < 2; ++t) { const int kq = kgrp * 8 + kq0 + t * kst;
        wb[par][t] = 16384 + kq * 1024 + wc * 512 + ((lane >> 2) & 3) * 64 + ((par ^ (kq & 1)) * 32) + (lane & 3) * 8; }
  }
  f32x4 acc[4][4];
#pragma unroll
  for (int i = 0; i < 4; ++i)
#pragma unroll
    for (int j = 0; j < 4; ++j) acc[i][j] = f32x4{};
  bf16x8 af[2][4], wf[2][4];
  wait_bar<8>();
  {
    lds_cptr base = (lds_cptr)lds;
#pragma unroll
    for (int j = 0; j < 4; ++j) wf[0][j] = cat8(ldstr(base + wb[j & 1][0] + (j >> 1) * 256), ldstr(base + wb[j & 1][1] + (j >> 1) * 256));
#pragma unroll
    for (int i = 0; i < 4; ++i) af[0][i] = ldsr128(base + ((i < 2) ? ab : abx) + (i & 1) * 2048);
  }
#pragma unroll
  for (int s = 0; s < NS; ++s) {
    const int hf = s & 1;
    const bool nx = s + 1 < NS, pf = s + 5 < NS;
    if (nx) {
      constexpr int dummy = 0; (void)dummy;
      const int yo = (s < 3) ? (6 - 2 * s) + 4 * s : 12;
      const int tail = NS - 2 - s;
      const int n = (tail < 3) ? 4 * tail : yo;
      if (n == 12) wait_bar<12>(); else if (n == 10) wait_bar<10>(); else if (n == 8) wait_bar<8>(); else if (n == 6) wait_bar<6>(); else if (n == 4) wait_bar<4>(); else wait_bar<0>();
    }
    SBAR();
    lds_cptr base = (lds_cptr)lds + ((s + 1) % 5) * 32768;
    s16x4 wl[4], wh[4];
#define K2M(i, j) acc[i][j] = MFMA16(wf[hf][j], af[hf][i], acc[i][j])
#define PIN4(i) asm volatile("" : "+v"(acc[i][0]), "+v"(acc[i][1]), "+v"(acc[i][2]), "+v"(acc[i][3]))
    K2M(0, 0); if (nx) wl[0] = ldstr(base + wb[0][0]); SBAR();
    K2M(0, 1); if (nx) wh[0] = ldstr(base + wb[0][1]); SBAR();
    K2M(0, 2); if (nx) af[hf ^ 1][0] = ldsr128(base + ab); SBAR();
    K2M(0, 3); if (nx) wl[1] = ldstr(base + wb[1][0]); SBAR();
    PIN4(0); if (pf) pA(s + 5, 0); SBAR();
    K2M(1, 0); if (nx) wh[1] = ldstr(base + wb[1][1]); SBAR();
    K2M(1, 1); if (nx) wl[2] = ldstr(base + wb[0][0] + 256); SBAR();
    K2M(1, 2); if (nx) wh[2] = ldstr(base + wb[0][1] + 256); SBAR();
    K2M(1, 3); if (nx) wl[3] = ldstr(base + wb[1][0] + 256); SBAR();
    PIN4(1); if (pf) pA(s + 5, 1); SBAR();
    K2M(2, 0); if (nx) wh[3] = ldstr(base + wb[1][1] + 256); SBAR();
    K2M(2, 1); if (nx) af[hf ^ 1][1] = ldsr128(base + ab + 2048); SBAR();
    K2M(2, 2); if (nx) af[hf ^ 1][2] = ldsr128(base + abx); SBAR();
    K2M(2, 3); if (nx) af[hf ^ 1][3] = ldsr128(base + abx + 2048); SBAR();
    PIN4(2); if (pf) pW(s + 5, 0); SBAR();
    K2M(3, 0); K2M(3, 1); SBAR();
    if (pf) pW(s + 5, 1); SBAR();
    K2M(3, 2); K2M(3, 3); SBAR(); PIN4(3);
#undef K2M
    if (nx) {
#pragma unroll
      for (int j = 0; j < 4; ++j) wf[hf ^ 1][j] = cat8(wl[j], wh[j]);
    }
  }
  asm volatile("s_waitcnt vmcnt(0) lgkmcnt(0)\n\ts_barrier" ::: "memory");
  {
    char* mybuf = lds + wid * 8192;
    const char* pbuf = lds + (wid ^ 4) * 8192;
#pragma unroll
    for (int i = 0; i < 2; ++i)
#pragma unroll
      for (int j = 0; j < 4; ++j) *(f32x4*)(mybuf + ((i * 4 + j) * 64 + lane) * 16) = acc[2 + i][j];
    asm volatile("s_waitcnt lgkmcnt(0)\n\ts_barrier" ::: "memory");
#pragma unroll
    for (int i = 0; i < 2; ++i)
#pragma unroll
      for (int j = 0; j < 4; ++j) acc[i][j] += *(const f32x4*)(pbuf + ((i * 4 + j) * 64 + lane) * 16);
  }
  const int qd = 2 * wr + kgrp, tile = mt * 4 + nt;
  const __amdgpu_buffer_rsrc_t sP = mk_srd(ws + WS_PART, 16u << 20);
  f32x4 own[2];
#pragma unroll
  for (int j = 0; j < 4; ++j) {
    if (j != role) {
      const u32x4 w = {cvtpk(acc[0][j][0], acc[0][j][1]), cvtpk(acc[0][j][2], acc[0][j][3]), cvtpk(acc[1][j][0], acc[1][j][1]), cvtpk(acc[1][j][2], acc[1][j][3])};
      st16_wt(sP, (unsigned)((((tile * 4 + j) * 4 + role) * 8 + wid) * 1024) + vL, w);
    } else { own[0] = acc[0][j]; own[1] = acc[1][j]; }
  }
  u32x2 xv[2];
  const size_t obase = (size_t)(mt * 128 + qd * 32 + c) * 512 + nt * 128 + wc * 64 + role * 16 + 4 * g;
#pragma unroll
  for (int i = 0; i < 2; ++i) xv[i] = *(const u32x2*)(x + obase + i * 16 * 512);
  asm volatile("s_waitcnt vmcnt(0)\n\ts_barrier" ::: "memory");
  signal_w0(wid, 1u, flagp(ws, F_P + tile * 4 + role), tok);
  { check_w0<false, false>(wid, flagp(ws, F_P + tile * 4), (unsigned)(lane & 3) * 8u, tok, ~0ull); }
  asm volatile("s_barrier" ::: "memory");
  f32x4 p[3][2];
#pragma unroll
  for (int rr = 0; rr < 3; ++rr) {
    const int src = (role + 1 + rr) & 3;
    const u32x4 w = __builtin_amdgcn_raw_buffer_load_b128(sP, (unsigned)((((tile * 4 + role) * 4 + src) * 8 + wid) * 1024) + vL, 0, 16);
#pragma unroll
    for (int i = 0; i < 2; ++i) p[rr][i] = (f32x4){__uint_as_float(w[2 * i] << 16), __uint_as_float(w[2 * i] & 0xffff0000u), __uint_as_float(w[2 * i + 1] << 16), __uint_as_float(w[2 * i + 1] & 0xffff0000u)};
  }
#pragma unroll
  for (int i = 0; i < 2; ++i) {
    f32x4 ya = ish ? own[i] : (role == 2 ? p[0][i] : (role == 1 ? p[1][i] : p[2][i]));
    f32x4 sa = (own[i] + p[0][i]) + (p[1][i] + p[2][i]) - ya;
    {
      const f32x4 z = {};
      sa = (ish ? z : own[i]) + (role == 2 ? z : p[0][i]) + (role == 1 ? z : p[1][i]) + (role == 0 ? z : p[2][i]);
    }
    float o4[4];
#pragma unroll
    for (int e = 0; e < 4; ++e) {
      const float y = rbf(ya[e]);
      const float gt = rbf(y * __builtin_amdgcn_rcpf(1.f + __builtin_amdgcn_exp2f(-1.4426950408889634f * y)));
      const float sg = rbf(rbf(sa[e]) * gt);
      const float xe = (e & 1) ? __uint_as_float(xv[i][e >> 1] & 0xffff0000u) : __uint_as_float(xv[i][e >> 1] << 16);
      o4[e] = xe + sg;
    }
    u32x2 w = {cvtpk(o4[0], o4[1]), cvtpk(o4[2], o4[3])};
    *(u32x2*)(out + obase + i * 16 * 512) = w;
  }
}


__global__ __launch_bounds__(512, 2) __attribute__((amdgpu_num_vgpr(252))) void fused_block(const u16* __restrict__ x, const u16* __restrict__ ckv, const u16* __restrict__ wq, const u16* __restrict__ wuk, const u16* __restrict__ wuv,
                                                      const u16* __restrict__ wo, const u16* __restrict__ w1, const u16* __restrict__ w2, u16* __restrict__ out, char* __restrict__ ws) {
  extern __shared__ __attribute__((aligned(16))) char lds[];
  const int tid = threadIdx.x, lane = tid & 63, wid = __builtin_amdgcn_readfirstlane(tid >> 6);
  const int b = blockIdx.x, xcd = b & 7, jj = b >> 3, h = 4 * xcd + (jj >> 3), mt = jj & 7, bi = mt;
  const __amdgpu_buffer_rsrc_t sC = mk_srd(ckv, 2048u * 512u * 2u), sUK = mk_srd(wuk, 512u * 4096u * 2u), sUV = mk_srd(wuv, 512u * 4096u * 2u);
  const int R = b * 8 + wid;
  u32x4 xr;
  const unsigned vC = (unsigned)((lane >> 2) * 1024 + (((lane & 3) ^ (3 * ((lane >> 5) & 1))) * 16));
  const unsigned vW = (unsigned)((((lane >> 2) & 3) * 4096 + (lane >> 4) * 32 + ((lane & 3) ^ (2 * ((wid >> 1) & 1))) * 8) * 2);
  const unsigned sCbase = (unsigned)((mt * 256 + wid * 32) * 1024);
  const unsigned sWbase = (unsigned)((wid * 4 * 4096 + h * 128) * 2);
  auto st0 = [&](int s) {
    const unsigned bb = (unsigned)(uintptr_t)lds + (unsigned)(PA_BASE + s * 32768);
#pragma unroll
    for (int m = 0; m < 2; ++m) dma16(sC, vC, sCbase + (unsigned)(m * 16 * 1024 + s * 64), bb + (unsigned)((wid * 2 + m) * 1024));
#pragma unroll
    for (int m = 0; m < 2; ++m) dma16(m ? sUV : sUK, vW, sWbase + (unsigned)((s * 32 * 4096) * 2), bb + 16384u + (unsigned)((wid * 2 + m) * 1024));
  };
  { const u16* xp = x + (size_t)R * 512 + lane * 8; asm volatile("global_load_dwordx4 %0, %1, off" : "=v"(xr) : "v"(xp) : "memory"); }
  st0(0); st0(1);
  const u64 tok = ((hw_dispatch_id() + 1ull) * 0x9E3779B97F4A7C15ull) ^ ((u64)(uintptr_t)__builtin_amdgcn_dispatch_ptr() << 7) ^ 0x5bd1e9955bd1e995ull;
  PBsrc pb;
  pb.sX = mk_srd(ws + WS_XN, 2048u * 512u * 2u); pb.sQ = mk_srd(wq, 512u * 4096u * 2u); pb.s1 = mk_srd(w1, 512u * 1376u * 2u);
  pb.vL = pb.vQ = pb.v1 = 0u;
  pb.sXbase = (unsigned)(((wid < 4) ? (15 - bi) * 8 : bi * 8 - 8) + 2 * wid) * 16384u;
  pb.sQbase = sWbase; pb.s1base = h < 22 ? (unsigned)((wid * 4 * 1376 + h * 64) * 2) : OOBS;
  {
    asm volatile("s_waitcnt vmcnt(8)" : "+v"(xr) :: "memory");
    st0(2); st0(3);
    float v[8];
#pragma unroll
    for (int e = 0; e < 4; ++e) { v[2 * e] = __uint_as_float(xr[e] << 16); v[2 * e + 1] = __uint_as_float(xr[e] & 0xffff0000u); }
    float s = 0;
#pragma unroll
    for (int e = 0; e < 8; ++e) s += v[e];
#pragma unroll
    for (int o = 32; o; o >>= 1) s += __shfl_xor(s, o);
    const float mean = s * (1.f / 512.f);
    float q = 0;
#pragma unroll
    for (int e = 0; e < 8; ++e) { const float d = v[e] - mean; q += d * d; }
#pragma unroll
    for (int o = 32; o; o >>= 1) q += __shfl_xor(q, o);
    const float mb = rbf(mean), vb = rbf(q * (1.f / 512.f));
    const float den = rbf(__builtin_sqrtf(rbf(vb + 1e-5f)));
    float o8[8];
#pragma unroll
    for (int e = 0; e < 8; ++e) o8[e] = rbf(v[e] - mb) / den;
    u32x4 w = {cvtpk(o8[0], o8[1]), cvtpk(o8[2], o8[3]), cvtpk(o8[4], o8[5]), cvtpk(o8[6], o8[7])};
    st16_wt(pb.sX, (unsigned)((((R >> 4) * 16 + (lane >> 2)) * 64 + ((lane & 3) * 16 + (R & 15))) * 16), w);
  }
  unsigned long long pcv; unsigned len;
  asm volatile("s_getpc_b64 %0\n.Lfb_pc:\n\ts_mov_b32 %1, .Lfb_end-.Lfb_pc" : "=s"(pcv), "=s"(len));
  const int tid2 = jj * 8 + wid;
  const __amdgpu_buffer_rsrc_t sCode = tid2 < 128 ? ((tid2 & 64) ? sUV : sUK) : mk_srd((const char*)(pcv & ~127ull), len);
  const unsigned t2so = tid2 < 128 ? (unsigned)((tid2 & 63) * 8 * 8192 + xcd * 1024) : (unsigned)(tid2 - 128) * 8192u;
  const __amdgpu_buffer_rsrc_t sK = mk_srd(ws + WS_KF, 32u << 20);
  u32x4 kv[16];
  if (wid < 4) phaseA<true>(lds, ws, sC, sUK, sUV, sCbase, sWbase, pb, sCode, t2so, tok, h, mt, wid, lane_id(), kv);
  else         phaseA<false>(lds, ws, sC, sUK, sUV, sCbase, sWbase, pb, sCode, t2so, tok, h, mt, wid, lane_id(), kv);
  if (h < 22) phaseB<true>(lds, ws, pb, sK, tok, h, bi, wid, lane_id(), kv);
  else        phaseB<false>(lds, ws, pb, sK, tok, h, bi, wid, lane_id(), kv);
  phaseC(lds, ws, sK, wo, w2, tok, h, bi, wid, lane_id());
  phaseD(lds, ws, x, wo, w2, out, tok, h, bi, wid, lane_id());
  asm volatile(".Lfb_end:");
}

extern "C" void kernel_launch(void* const* d_in, const int* in_sizes, int n_in, void* d_out, int out_size, void* d_ws, size_t ws_size, hipStream_t stream) {
  (void)in_sizes; (void)n_in; (void)out_size; (void)ws_size;
  (void)hipFuncSetAttribute((const void*)fused_block, hipFuncAttributeMaxDynamicSharedMemorySize, 163840);
  fused_block<<<256, 512, 163840, stream>>>((const u16*)d_in[0], (const u16*)d_in[1], (const u16*)d_in[2], (const u16*)d_in[3], (const u16*)d_in[4], (const u16*)d_in[5],
                                            (const u16*)d_in[7], (const u16*)d_in[8], (u16*)d_out, (char*)d_ws);
}
```

```cpp
#include <hip/hip_runtime.h>
#include <hip/hip_bf16.h>
#include <stdint.h>

typedef unsigned short u16;
typedef short bf16x8 __attribute__((ext_vector_type(8)));
typedef short s16x4 __attribute__((ext_vector_type(4)));
typedef float f32x16 __attribute__((ext_vector_type(16)));
typedef float f32x4 __attribute__((ext_vector_type(4)));
typedef float f32x2 __attribute__((ext_vector_type(2)));
typedef __bf16 bf16x2_t __attribute__((ext_vector_type(2)));
typedef unsigned u32x4 __attribute__((ext_vector_type(4)));
#define LAS __attribute__((address_space(3)))
typedef LAS const char* lds_cptr;
#define DI __device__ __forceinline__
#define MFMA(a, b, c) __builtin_amdgcn_mfma_f32_32x32x16_bf16((a), (b), (c), 0, 0, 0)
#define SBAR() __builtin_amdgcn_sched_barrier(0)

constexpr size_t WS_KF = 0;
constexpr size_t WS_VF = 16u << 20;
constexpr size_t WS_CTX = 32u << 20;
constexpr size_t WS_XN = 48u << 20;
constexpr size_t WS_H = 50u << 20;
constexpr size_t WS_PART = 56u << 20;
constexpr size_t WS_FLAG = 80u << 20;

DI unsigned cvtpk(float lo, float hi) { f32x2 v = {lo, hi}; bf16x2_t b = __builtin_convertvector(v, bf16x2_t); return __builtin_bit_cast(unsigned, b); }
DI float bf2f(unsigned v16) { return __uint_as_float(v16 << 16); }
DI float rbf(float f) { return bf2f(cvtpk(f, f) & 0xffffu); }
DI u32x4 pack8(const f32x16& x, int s) {
  u32x4 p = {cvtpk(x[8 * s], x[8 * s + 1]), cvtpk(x[8 * s + 2], x[8 * s + 3]), cvtpk(x[8 * s + 4], x[8 * s + 5]), cvtpk(x[8 * s + 6], x[8 * s + 7])};
  return p;
}
DI __amdgpu_buffer_rsrc_t mk_srd(const void* p, unsigned bytes) { return __builtin_amdgcn_make_buffer_rsrc((void*)p, (short)0, (int)bytes, 0x00020000); }
DI void dma16(__amdgpu_buffer_rsrc_t srd, unsigned voff, unsigned soff, unsigned lds_dst) {
  unsigned keep;
  asm volatile("s_mov_b32 %0, m0\n\ts_mov_b32 m0, %1\n\ts_nop 0\n\tbuffer_load_dwordx4 %2, %3, %4 offen lds\n\ts_mov_b32 m0, %0"
               : "=&s"(keep) : "s"(lds_dst), "v"(voff), "s"(srd), "s"(soff) : "memory");
}
DI bf16x8 ldsr128(lds_cptr p) { return *(const LAS bf16x8*)p; }
typedef short v4i16_t __attribute__((ext_vector_type(4)));
DI s16x4 ldstr(lds_cptr p) { return __builtin_bit_cast(s16x4, __builtin_amdgcn_ds_read_tr16_b64_v4i16((LAS v4i16_t*)p)); }
DI bf16x8 cat8(s16x4 a, s16x4 b) { return __builtin_shufflevector(a, b, 0, 1, 2, 3, 4, 5, 6, 7); }


template <int N> DI void wait_bar() { asm volatile("s_waitcnt vmcnt(%0) lgkmcnt(0)\n\ts_barrier" :: "i"(N) : "memory"); }
#define MFMA16(a, b, c) __builtin_amdgcn_mfma_f32_16x16x32_bf16((a), (b), (c), 0, 0, 0)
#define SGB(mask, n) __builtin_amdgcn_sched_group_barrier(mask, n, 0)
constexpr unsigned OOBS = 0x7f000000u, DROP = 0x38000u;
typedef unsigned long long u64;
extern "C" __device__ u64 hw_dispatch_id() __asm("llvm.amdgcn.dispatch.id");
constexpr int F_X = 0, F_KV = 256, F_C = 512, F_P = 1024;
DI int lane_id() { int l; asm volatile("v_mbcnt_lo_u32_b32 %0, -1, 0\n\tv_mbcnt_hi_u32_b32 %0, -1, %0" : "=v"(l)); return l; }
DI u64* flagp(char* ws, int i) { return (u64*)(ws + WS_FLAG) + i; }
DI void st16_wt(__amdgpu_buffer_rsrc_t srd, unsigned off, u32x4 w) { __builtin_amdgcn_raw_buffer_store_b128(w, srd, off, 0, 16); }
constexpr int L_V = 0, L_QA = 65536, L_QB = 98304, L_KS2 = 98304, L_KS0 = 131072;
constexpr float QSCALE = 0.08838834764831845f * 1.4426950408889634f;
constexpr float THR = 8.0f;
#define PIN(x) asm volatile("" : "+v"(x))
DI float max3f(float a, float b, float c) { float r; asm("v_max3_f32 %0, %1, %2, %3" : "=v"(r) : "v"(a), "v"(b), "v"(c)); return r; }
DI float max2f(float a, float b) { float r; asm("v_max_f32_e32 %0, %1, %2" : "=v"(r) : "v"(a), "v"(b)); return r; }
DI float swap32_max(float m) { auto rr = __builtin_amdgcn_permlane32_swap(__float_as_uint(m), __float_as_uint(m), false, false); return fmaxf(__uint_as_float(rr[0]), __uint_as_float(rr[1])); }
DI float swap32_add(float m) { auto rr = __builtin_amdgcn_permlane32_swap(__float_as_uint(m), __float_as_uint(m), false, false); return __uint_as_float(rr[0]) + __uint_as_float(rr[1]); }


constexpr int PB_STAGE = 32768;
DI constexpr int pb_slot(int s) { return (s & 3) * PB_STAGE; }
constexpr int PA_BASE = 32768;
struct PBsrc { __amdgpu_buffer_rsrc_t sX, sQ, s1; unsigned vL, vQ, v1, sXbase, sQbase, s1base; };
DI void pb_lanes(PBsrc& p, int wid, int lane) {
  const int c4 = (lane & 3) ^ (2 * ((wid >> 1) & 1));
  p.vL = (unsigned)lane * 16u;
  p.vQ = (unsigned)((((lane >> 2) & 3) * 4096 + (lane >> 4) * 32 + c4 * 8) * 2);
  p.v1 = lane < 32 ? (unsigned)((((lane >> 2) & 3) * 1376 + (lane >> 4) * 32 + c4 * 8) * 2) : OOBS;
}
DI void pb_piece(const PBsrc& p, unsigned lds0, int wid, int s, int m) {
  const unsigned b = lds0 + (unsigned)pb_slot(s);
  if (m < 2) dma16(p.sX, p.vL, p.sXbase + (unsigned)(m * 16384 + s * 1024), b + (unsigned)((wid * 2 + m) * 1024));
  else if (m == 2) dma16(p.sQ, p.vQ, p.sQbase + (unsigned)(s * 32 * 4096 * 2), b + 16384u + (unsigned)(wid * 2048));
  else dma16(p.s1, p.v1, p.s1base + (unsigned)(s * 32 * 1376 * 2), b + 16384u + 1024u + (unsigned)(wid * 2048));
}
template <int N0, int N> DI void wait_bar2(int wid) {
  asm volatile("s_cmp_eq_u32 %0, 0\n\ts_cbranch_scc1 1f\n\ts_waitcnt vmcnt(%2)\n\ts_branch 2f\n1:\n\ts_waitcnt vmcnt(%1)\n2:\n\ts_waitcnt lgkmcnt(0)\n\ts_barrier" :: "s"(wid), "i"(N0), "i"(N) : "memory", "scc");
}
DI void signal_w0(int wid, unsigned en, u64* p, u64 tok) {
  unsigned z = 0u;
  asm volatile("s_cmp_eq_u32 %0, 0\n\ts_cbranch_scc0 1f\n\ts_cmp_eq_u32 %4, 0\n\ts_cbranch_scc1 1f\n\ts_mov_b64 exec, 1\n\tglobal_store_dwordx2 %2, %3, %1 sc1\n\ts_mov_b64 exec, -1\n1:"
               :: "s"(wid), "s"(p), "v"(z), "v"(tok), "s"(en) : "memory", "scc");
}
DI void poll_w0(int wid, const u64* p, unsigned voff) {
  asm volatile("s_cmp_eq_u32 %0, 0\n\ts_cbranch_scc0 1f\n\tglobal_load_dwordx2 v[252:253], %2, %1 sc1\n1:" :: "s"(wid), "s"(p), "v"(voff) : "memory", "scc", "v252", "v253"); }
template <bool INV, bool POLLED> DI void check_w0(int wid, const u64* p, unsigned voff, u64 tok, u64 need) {
  unsigned cnt; u64 m;
  asm volatile("s_cmp_eq_u32 %2, 0\n\ts_cbranch_scc0 9f\n\ts_mov_b32 %1, 0x40000\n\t.if %8 == 0\n\ts_branch 3f\n\t.endif\n"
               "1:\n\tv_cmp_ne_u64_e64 %0, v[252:253], %5\n\ts_and_b64 %0, %0, %6\n\ts_cbranch_scc0 2f\n\ts_sub_u32 %1, %1, 1\n\ts_cmp_eq_u32 %1, 0\n\ts_cbranch_scc1 2f\n\ts_sleep 1\n"
               "3:\n\tglobal_load_dwordx2 v[252:253], %4, %3 sc1\n\ts_waitcnt vmcnt(0)\n\ts_branch 1b\n"
               "2:\n\t.if %7\n\tbuffer_inv sc1\n\t.endif\n9:"
               : "=&s"(m), "=&s"(cnt) : "s"(wid), "s"(p), "v"(voff), "v"(tok), "s"(need), "i"(INV ? 1 : 0), "i"(POLLED ? 1 : 0) : "memory", "scc", "v252", "v253");
}
template <int NF, int NS_> DI void wait_barf(unsigned fast) {
  asm volatile("s_cmp_eq_u32 %0, 0\n\ts_cbranch_scc1 1f\n\ts_waitcnt vmcnt(%1)\n\ts_branch 2f\n1:\n\ts_waitcnt vmcnt(%2)\n2:\n\ts_waitcnt lgkmcnt(0)\n\ts_barrier" :: "s"(fast), "i"(NF), "i"(NS_) : "memory", "scc");
}
DI void st16_if(unsigned en, u32x4 w, __amdgpu_buffer_rsrc_t srd, unsigned voff, unsigned soff) {
  asm volatile("s_cmp_eq_u32 %4, 0\n\ts_cbranch_scc1 1f\n\tbuffer_store_dwordx4 %0, %1, %2, %3 offen sc1\n\ts_nop 1\n1:" :: "v"(w), "v"(voff), "s"(srd), "s"(soff), "s"(en) : "memory", "scc");
}
DI void touch(__amdgpu_buffer_rsrc_t srd, unsigned voff, unsigned soff) {
  asm volatile("buffer_load_dword v254, %0, %1, %2 offen" :: "v"(voff), "s"(srd), "s"(soff) : "memory", "v254");
}

template <bool KT>
DI void phaseA(char* lds, char* ws, __amdgpu_buffer_rsrc_t sC, __amdgpu_buffer_rsrc_t sUK, __amdgpu_buffer_rsrc_t sUV, unsigned sCbase, unsigned sWbase,
               PBsrc pb, u64 tok, int h, int mt, int wid, int lane, u32x4 (&kv)[16]) {
  const int c = lane & 15, g = lane >> 4, km = wid & 1, nq = wid >> 1;
  const unsigned vC = (unsigned)((lane >> 2) * 1024 + (((lane & 3) ^ (3 * ((lane >> 5) & 1))) * 16));
  const unsigned vW = (unsigned)((((lane >> 2) & 3) * 4096 + (lane >> 4) * 32 + ((lane & 3) ^ (2 * ((wid >> 1) & 1))) * 8) * 2);
  const unsigned lds0 = (unsigned)(uintptr_t)lds;
  auto piece = [&](int s, int m) {
    if (s < 16) {
      const unsigned b = lds0 + (unsigned)(PA_BASE + (s & 3) * 32768);
      if (m < 2) dma16(sC, vC, sCbase + (unsigned)(m * 16 * 1024 + s * 64), b + (unsigned)((wid * 2 + m) * 1024));
      else       dma16((m & 1) ? sUV : sUK, vW, sWbase + (unsigned)((s * 32 * 4096) * 2), b + 16384u + (unsigned)((wid * 2 + (m & 1)) * 1024));
    } else pb_piece(pb, lds0, wid, s - 16, m);
  };
  auto warmB = [&]() {
    const int l2 = lane_id(), id = (int)(blockIdx.x >> 3) * 8 + wid, xg = (int)(blockIdx.x & 7);
    const bool q = id < 64;
    const unsigned vo = (unsigned)(l2 >> 3) * (q ? 8192u : 2752u) + (unsigned)(l2 & 7) * 128u;
    const unsigned so = q ? (unsigned)(id * 8 * 8192 + xg * 1024) : (id < 128 ? (unsigned)((id - 64) * 8 * 2752 + xg * 512) : OOBS);
    touch(q ? pb.sQ : pb.s1, vo, so);
  };
  const int cb = (128 * km + c) * 64 + ((g ^ (3 * ((c >> 3) & 1))) * 16);
  int wb[2];
#pragma unroll
  for (int par = 0; par < 2; ++par) wb[par] = 16384 + g * 4096 + nq * 512 + ((lane >> 2) & 3) * 64 + ((par ^ (g & 1)) * 32) + (lane & 3) * 8;
  f32x4 acc[8][4];
#pragma unroll
  for (int i = 0; i < 8; ++i)
#pragma unroll
    for (int j = 0; j < 4; ++j) acc[i][j] = f32x4{};
  bf16x8 cf[2][4], wf[2][4];
  auto rdW = [&](int s, int buf) {
    lds_cptr base = (lds_cptr)lds + (PA_BASE + (s & 3) * 32768);
#pragma unroll
    for (int j = 0; j < 4; ++j) wf[buf][j] = cat8(ldstr(base + wb[j & 1] + (j >> 1) * 256), ldstr(base + wb[j & 1] + 2048 + (j >> 1) * 256));
  };
  auto rdC = [&](int s, int half, int buf) {
    lds_cptr base = (lds_cptr)lds + (PA_BASE + (s & 3) * 32768);
#pragma unroll
    for (int i = 0; i < 4; ++i) cf[buf][i] = ldsr128(base + cb + (half * 4 + i) * 1024);
  };
  auto mm1 = [&](int wbuf, int cbuf, int half, int i) {
#pragma unroll
    for (int j = 0; j < 4; ++j) acc[half * 4 + i][j] = KT ? MFMA16(wf[wbuf][j], cf[cbuf][i], acc[half * 4 + i][j]) : MFMA16(cf[cbuf][i], wf[wbuf][j], acc[half * 4 + i][j]);
  };
  auto pin4 = [&](int half) {
#pragma unroll
    for (int i = 0; i < 4; ++i) asm volatile("" : "+v"(acc[half * 4 + i][0]), "+v"(acc[half * 4 + i][1]), "+v"(acc[half * 4 + i][2]), "+v"(acc[half * 4 + i][3]));
  };
  const int bi = mt;
  wait_bar<15>();
  rdW(0, 0); rdC(0, 0, 0);
#pragma unroll
  for (int s = 0; s < 16; ++s) {
    const int wbuf = s & 1;
    rdC(s, 1, 1);
    mm1(wbuf, 0, 0, 0); mm1(wbuf, 0, 0, 1); mm1(wbuf, 0, 0, 2); mm1(wbuf, 0, 0, 3);
    SGB(0x008, 2); SGB(0x100, 1); SGB(0x008, 2); SGB(0x100, 1); SGB(0x008, 2); SGB(0x100, 1); SGB(0x008, 2); SGB(0x100, 1); SGB(0x008, 8); SBAR(); pin4(0);
    if (KT && s == 10) { const int l2 = lane_id(); check_w0<true, true>(wid, flagp(ws, F_X), (unsigned)((((l2 & 16) ? 15 - bi : bi) * 16 + (l2 & 15)) * 8), tok, ~0ull); }
    if (s < 15) {
      if (s == 0) wait_bar<11>(); else if (s <= 2) wait_bar<11>(); else if (s == 5 || s == 6 || s == 7) wait_bar<9>(); else wait_bar<8>();
      SBAR();
      if (s == 12) pb_lanes(pb, wid, lane_id());
      if (KT && s == 4) signal_w0(wid, 1u, flagp(ws, F_X + blockIdx.x), tok);
      if (KT && s == 6) { const int l2 = lane_id(); poll_w0(wid, flagp(ws, F_X), (unsigned)((((l2 & 16) ? 15 - bi : bi) * 16 + (l2 & 15)) * 8)); }
      rdW(s + 1, wbuf ^ 1); rdC(s + 1, 0, 0);
      mm1(wbuf, 1, 1, 0); SGB(0x008, 1); SGB(0x100, 3); SGB(0x008, 1); SGB(0x100, 3); SGB(0x008, 1); SGB(0x100, 3); SGB(0x008, 1); SGB(0x100, 3); SBAR(); piece(s + 4, 0); SBAR();
      mm1(wbuf, 1, 1, 1); SBAR(); piece(s + 4, 1); SBAR();
      mm1(wbuf, 1, 1, 2); SBAR(); piece(s + 4, 2); SBAR();
      mm1(wbuf, 1, 1, 3); SBAR(); pin4(1); piece(s + 4, 3); if (s == 4) warmB(); SBAR();
    } else {
      mm1(wbuf, 1, 1, 0); SBAR(); piece(19, 0); SBAR(); mm1(wbuf, 1, 1, 1); SBAR(); piece(19, 1); SBAR(); mm1(wbuf, 1, 1, 2); SBAR(); piece(19, 2); SBAR(); mm1(wbuf, 1, 1, 3); SBAR(); piece(19, 3); SBAR();
    }
  }
  if (KT) {
#pragma unroll
    for (int i = 0; i < 8; ++i)
#pragma unroll
      for (int dp = 0; dp < 2; ++dp)
        kv[i * 2 + dp] = (u32x4){cvtpk(acc[i][2 * dp][0], acc[i][2 * dp][1]), cvtpk(acc[i][2 * dp][2], acc[i][2 * dp][3]), cvtpk(acc[i][2 * dp + 1][0], acc[i][2 * dp + 1][1]), cvtpk(acc[i][2 * dp + 1][2], acc[i][2 * dp + 1][3])};
  } else {
#pragma unroll
    for (int ip = 0; ip < 4; ++ip)
#pragma unroll
      for (int j = 0; j < 4; ++j) {
        unsigned a0 = cvtpk(acc[2 * ip][j][0], acc[2 * ip][j][1]), a1 = cvtpk(acc[2 * ip][j][2], acc[2 * ip][j][3]);
        unsigned b0 = cvtpk(acc[2 * ip + 1][j][0], acc[2 * ip + 1][j][1]), b1 = cvtpk(acc[2 * ip + 1][j][2], acc[2 * ip + 1][j][3]);
        auto r0 = __builtin_amdgcn_permlane32_swap(a0, b0, false, false);
        auto r1 = __builtin_amdgcn_permlane32_swap(a1, b1, false, false);
        kv[ip * 4 + j] = (u32x4){r0[0], r1[0], r0[1], r1[1]};
      }
  }
}

template <bool FFN>
DI void phaseB(char* lds, char* ws, PBsrc pb, __amdgpu_buffer_rsrc_t sK, u64 tok, int h, int bi, int wid, int lane, const u32x4 (&kv)[16]) {
  pb_lanes(pb, wid, lane);
  const int c = lane & 15, g = lane >> 4, rgp = wid >> 1, ch = wid & 1;
  const unsigned lds0 = (unsigned)(uintptr_t)lds;
  constexpr int NP = 4;
  auto kvpiece = [&](unsigned base, int tile, int ldsoff, int m) {
    dma16(sK, pb.vL, base + (unsigned)(h * 524288 + tile * 16384 + (wid * 2 + m) * 1024), lds0 + (unsigned)(ldsoff + (wid * 2 + m) * 1024));
  };
  const int v0slot = L_V + ((0 - (2 * bi + 2)) & 3) * 16384;
  constexpr int NJ = FFN ? 6 : 4;
  const int xb = rgp * 4096 + lane * 16;
  int wb[2];
#pragma unroll
  for (int par = 0; par < 2; ++par) wb[par] = 16384 + g * 4096 + ((lane >> 2) & 3) * 64 + ((par ^ (g & 1)) * 32) + (lane & 3) * 8;
  f32x4 acc[4][NJ];
#pragma unroll
  for (int i = 0; i < 4; ++i)
#pragma unroll
    for (int j = 0; j < NJ; ++j) acc[i][j] = f32x4{};
  bf16x8 xf[2][2], wf[2][NJ];
  auto rdW = [&](int s, int buf) {
    lds_cptr base = (lds_cptr)lds + pb_slot(s);
#pragma unroll
    for (int j = 0; j < NJ; ++j) { const int u = (j < 4) ? (2 * ch + (j >> 1)) : (4 + ch);
      wf[buf][j] = cat8(ldstr(base + wb[j & 1] + u * 256), ldstr(base + wb[j & 1] + 2048 + u * 256)); }
  };
  auto rdX = [&](int s, int half, int buf) {
    lds_cptr base = (lds_cptr)lds + pb_slot(s);
#pragma unroll
    for (int i = 0; i < 2; ++i) xf[buf][i] = ldsr128(base + xb + (half * 2 + i) * 1024);
  };
  auto mm1 = [&](int wbuf, int xbuf, int half, int i) {
#pragma unroll
    for (int j = 0; j < NJ; ++j) acc[half * 2 + i][j] = MFMA16(wf[wbuf][j], xf[xbuf][i], acc[half * 2 + i][j]);
  };
  auto pin1 = [&](int i) {
#pragma unroll
    for (int j = 0; j < NJ; j += 2) asm volatile("" : "+v"(acc[i][j]), "+v"(acc[i][j + 1]));
  };
  const int km = wid & 1, nq = wid >> 1;
  const bool kt = wid < 4;
  const unsigned kvV = (unsigned)((g >> 1) * 1024 + ((g & 1) * 32 + c) * 16);
  const unsigned kvS = kt ? (unsigned)(((h * 32 + bi * 4 + 2 * km) * 16 + 4 * nq) * 1024) : (16u << 20) + (unsigned)(((h * 32 + bi * 4 + 2 * km) * 16 + 8 * (nq - 2)) * 1024);
  const unsigned fast = (unsigned)__builtin_amdgcn_readfirstlane((int)((unsigned)(bi - 2) >> 31)), slow = (unsigned)__builtin_amdgcn_readfirstlane((int)(fast ^ 1u));
  auto kvstore = [&](int n, unsigned en) {
    const int i = n >> 1, dp = n & 1, ip = n >> 2, j = n & 3;
    const unsigned ok = (unsigned)(((i >> 2) * 16 + ((i >> 1) & 1) * 8 + 2 * dp) * 1024 + (i & 1) * 256);
    const unsigned ov = (unsigned)(((ip >> 1) * 16 + (j >> 1) * 4 + 2 * (ip & 1)) * 1024 + (j & 1) * 256);
    st16_if(en, kv[n], sK, kvV, kvS + (kt ? ok : ov));
  };
  wait_bar<12>();
  rdW(0, 0); rdX(0, 0, 0);
#pragma unroll
  for (int s = 0; s < 16; ++s) {
    const int wbuf = s & 1;
    rdX(s, 1, 1);
    mm1(wbuf, 0, 0, 0); mm1(wbuf, 0, 0, 1);
    SGB(0x008, 1); SGB(0x100, 1); SGB(0x008, 1); SGB(0x100, 1); SGB(0x008, 2 * NJ - 2); SBAR(); pin1(0); pin1(1);
    if (s == 14) check_w0<true, true>(wid, flagp(ws, F_KV + h * 8), 0u, tok, 1ull);
    if (s < 15) {
      if (s == 0) wait_bar<8>(); else if (s == 1 || s == 9) wait_bar<10>();
      else if (s <= 8) { if (s % 3 == 0) wait_barf<12, 10>(fast); else wait_barf<12, 11>(fast); }
      else if (s <= 11) wait_barf<8, 11>(fast); else if (s == 12) wait_barf<8, 10>(fast);
      else if (s == 13) wait_bar2<0, 4>(wid); else wait_bar<0>();
      SBAR();
      if (s == 10) signal_w0(wid, fast, flagp(ws, F_KV + h * 8 + bi), tok);
      if (s == 14) signal_w0(wid, slow, flagp(ws, F_KV + h * 8 + bi), tok);
      if (s == 12) poll_w0(wid, flagp(ws, F_KV + h * 8), 0u);
      rdW(s + 1, wbuf ^ 1); rdX(s + 1, 0, 0);
      mm1(wbuf, 1, 1, 0);
#pragma unroll
      for (int q = 0; q < NJ; ++q) { SGB(0x008, 1); SGB(0x100, 3); }
      SBAR(); pin1(2);
      if (s + 4 < 16) { pb_piece(pb, lds0, wid, s + 4, 0); pb_piece(pb, lds0, wid, s + 4, 1); SBAR(); }
      if (s <= 7) { kvstore(2 * s, fast); SBAR(); }
      mm1(wbuf, 1, 1, 1); SBAR(); pin1(3);
      if (s + 4 < 16) { pb_piece(pb, lds0, wid, s + 4, 2); pb_piece(pb, lds0, wid, s + 4, 3); SBAR(); }
      if (s <= 7) { kvstore(2 * s + 1, fast); SBAR(); }
      if (s <= 11) { const int n0 = s + (s + 2) / 3; kvstore(n0, slow); if (s % 3 == 0) kvstore(n0 + 1, slow); SBAR(); }
    } else {
      wait_bar2<0, 63>(wid);
      kvpiece(0u, 0, L_KS0, 0); kvpiece(0u, 0, L_KS0, 1); kvpiece(0u, 1, L_KS0 + 16384, 0); kvpiece(0u, 1, L_KS0 + 16384, 1);
      kvpiece(16u << 20, 0, v0slot, 0); kvpiece(16u << 20, 0, v0slot, 1);
      SBAR();
      mm1(wbuf, 1, 1, 0); mm1(wbuf, 1, 1, 1);
    }
  }
  char* qdst = lds + ((rgp < 2) ? L_QA : L_QB);
  const __amdgpu_buffer_rsrc_t sH = mk_srd(ws + WS_H, 128u * 44u * 1024u);
#pragma unroll
  for (int ii = 0; ii < 4; ++ii) {
    const int rb32 = 2 * (rgp & 1) + (ii >> 1), r = 16 * (ii & 1) + c;
#pragma unroll
    for (int dpp = 0; dpp < 2; ++dpp) {
      const int ks = 2 * (2 * ch + dpp) + (g >> 1);
      const f32x4 a = acc[ii][2 * dpp] * QSCALE, b = acc[ii][2 * dpp + 1] * QSCALE;
      u32x4 w = {cvtpk(a[0], a[1]), cvtpk(a[2], a[3]), cvtpk(b[0], b[1]), cvtpk(b[2], b[3])};
      *(u32x4*)(qdst + ((rb32 * 8 + ks) * 64 + (g & 1) * 32 + r) * 16) = w;
    }
    if (FFN) {
      const int rb16 = ((rgp < 2) ? (15 - bi) * 8 : bi * 8) + 4 * (rgp & 1) + ii;
      const bool valid = (h * 64 + ch * 32) < 1376;
      f32x4 a = acc[ii][NJ - 2], b = acc[ii][NJ - 1];
#pragma unroll
      for (int e = 0; e < 4; ++e) { a[e] = valid ? fmaxf(a[e], 0.f) : 0.f; b[e] = valid ? fmaxf(b[e], 0.f) : 0.f; }
      u32x4 w = {cvtpk(a[0], a[1]), cvtpk(a[2], a[3]), cvtpk(b[0], b[1]), cvtpk(b[2], b[3])};
      st16_wt(sH, (unsigned)(((rb16 * 44 + 2 * h + ch) * 64 + lane) * 16), w);
    }
  }
}

DI void store_ctx(__amdgpu_buffer_rsrc_t sCtx, const float (&y)[16], int rowblk16, int h, int db, int r, int hi) {
#pragma unroll
  for (int sx = 0; sx < 2; ++sx) {
    u32x4 w = {cvtpk(y[8 * sx], y[8 * sx + 1]), cvtpk(y[8 * sx + 2], y[8 * sx + 3]), cvtpk(y[8 * sx + 4], y[8 * sx + 5]), cvtpk(y[8 * sx + 6], y[8 * sx + 7])};
    st16_wt(sCtx, (unsigned)((((rowblk16 + (r >> 4)) * 128 + h * 4 + db) * 64 + (2 * sx + hi) * 16 + (r & 15)) * 16), w);
  }
}

template <int OFF> DI void dma2(__amdgpu_buffer_rsrc_t srd, unsigned voff, unsigned soff, unsigned lds_dst) {
  asm volatile("s_mov_b32 m0, %0\n\ts_nop 0\n\tbuffer_load_dwordx4 %1, %2, %3 offen offset:%4 lds\n\tbuffer_load_dwordx4 %1, %2, %3 offen offset:%5 lds"
               :: "s"(lds_dst), "v"(voff), "s"(srd), "s"(soff), "i"(OFF), "i"(OFF + 1024) : "memory");
}
DI unsigned rdl(unsigned v, int l) { return (unsigned)__builtin_amdgcn_readlane((int)v, l); }

DI void phaseC(char* lds, char* ws, __amdgpu_buffer_rsrc_t sK, const u16* wo, const u16* w2, u64 tok, int h, int bi, int wid, int lane) {
  const unsigned pvo = (unsigned)(lane & 7) * 8u;
  const __amdgpu_buffer_rsrc_t sCtx = mk_srd(ws + WS_CTX, 16u << 20);
  const int rg = wid & 3, grp = wid >> 2, r = lane & 31, hi = lane >> 5;
  const unsigned lds0 = (unsigned)(uintptr_t)lds;
  const int P1 = 2 * bi + 2;
  const unsigned vL = (unsigned)lane * 16u;
  unsigned tKs, tVs, tC;
  {
    const unsigned hbase = (unsigned)(h * 524288);
    auto tix = [&](int g) { return g < P1 ? g : P1 + 2 * (g - P1) + grp; };
    auto kslot = [&](int g) { return ((grp && g >= P1) ? L_KS2 : L_KS0) + (g & 1) * 16384; };
    auto vslot = [&](int g) { return g < P1 ? L_V + ((g - P1) & 3) * 16384 : ((grp && g == 16) ? L_KS2 + 16384 : L_V + (2 * grp + (g & 1)) * 16384); };
    const int k = lane, gk = k + 1;
    const bool ksh = gk < P1, kok = gk <= 16;
    const unsigned ko = ksh ? wid * 2048 : rg * 4096;
    tKs = kok ? (hbase + (unsigned)(tix(gk) * 16384) + ko) | (unsigned)(kslot(gk) >> 14) : (OOBS | 14u);
    const bool vsh = k < P1;
    const int gv = (vsh || !grp) ? k : k - 1;
    const bool vok = vsh || gv >= P1;
    const unsigned vo = vsh ? wid * 2048 : rg * 4096;
    tVs = vok ? ((16u << 20) + hbase + (unsigned)(tix(gv) * 16384) + vo) | (unsigned)(vslot(gv) >> 14) : (OOBS | 14u);
    const int g = lane - 1;
    const bool first = (g < 0) || (grp && g + 1 == P1);
    const bool mask = (g + 1 == 16) || (grp && (g + 1 == P1 - 2 || g + 1 == P1 - 1));
    const unsigned qsrc = (g < 0 && grp) ? (unsigned)L_QB : (unsigned)L_QA;
    tC = (unsigned)(kslot(g + 1) >> 14) | ((unsigned)((g < 0 ? L_KS0 : vslot(g)) >> 14) << 4) | ((unsigned)(kslot(g + 2) >> 14) << 8) | ((qsrc >> 14) << 12) |
         (first ? 1u << 16 : 0u) | (mask ? 1u << 17 : 0u) | ((kok && !ksh) ? 1u << 18 : 0u) | ((vok && !vsh) ? 1u << 19 : 0u) |
         ((unsigned)((((grp && g + 1 < P1) ? bi : 15 - bi) * 4 + rg - tix(g + 1) * 2) & 255) << 24);
  }
  const float rl = (float)(r - 4 * hi) + 0.5f;
  bf16x8 qf[8];
  f32x16 o[4], S1, T0, T1, negm;
  u32x4 pk0 = {}, pk1 = {}, pk2, pk3;
  float m = 0.f, l = 0.f, lB = 0.f, ma, mb;
  bf16x8 k0, k1, k2, k3, v0, v1, v2, v3;
#pragma unroll
  for (int d = 0; d < 4; ++d) o[d] = f32x16{};
  S1 = f32x16{};
#define KF(i) ldsr128(kp + (i) * 1024)
#define VF(db, ks) ldsr128(((db) < 2 ? vpl : vpl_hi) + (((db) & 1) * 4 + (ks)) * 1024)
#define EX(v) __builtin_amdgcn_exp2f(v)
#define BFR(x) __builtin_bit_cast(bf16x8, x)
#define ADD2(a0, a1) asm("v_add_f32_e32 %0, %0, %2\n\tv_add_f32_e32 %1, %1, %3" : "+v"(sa), "+v"(sb) : "v"(a0), "v"(a1))
#define MX2(X, i) asm("v_max3_f32 %0, %0, %2, %3\n\tv_max3_f32 %1, %1, %4, %5" : "+v"(ma), "+v"(mb) : "v"(X[i]), "v"(X[i + 1]), "v"(X[i + 2]), "v"(X[i + 3]))
  unsigned ksrc = 0x7f000000u, kdst = lds0 + 0x38000u, vsrc = 0x7f000000u, vdst = lds0 + 0x38000u, dfl = 0;
#define HOOK0() do { if (dfl & 1) dma2<0>(sK, vL, ksrc, kdst); SBAR(); } while (0)
#define HOOK1() do { if (dfl & 2) dma2<2048>(sK, vL, ksrc, kdst); SBAR(); } while (0)
#define HOOK2() do { if (dfl & 1) dma2<0>(sK, vL, vsrc, vdst); SBAR(); } while (0)
#define HOOK3() do { if (dfl & 4) dma2<2048>(sK, vL, vsrc, vdst); SBAR(); } while (0)
#define PLAN(k) do { const unsigned a_ = rdl(tKs, k), b_ = rdl(tVs, k); dfl = 1u | ((rdl(tC, k) >> 17) & 6u); ksrc = a_ & ~15u; vsrc = b_ & ~15u; \
    kdst = lds0 + (a_ & 15u) * 16384u + ((dfl & 2u) ? rg * 4096u : wid * 2048u); vdst = lds0 + (b_ & 15u) * 16384u + ((dfl & 4u) ? rg * 4096u : wid * 2048u); } while (0)
  #pragma unroll 1
  for (int g = -1; g < 16; ++g) {
    const unsigned tc = rdl(tC, g + 1);
    const lds_cptr kp = (lds_cptr)lds + (lane * 16 + (tc & 15u) * 16384u);
    const lds_cptr vp = (lds_cptr)lds + (lane * 16 + ((tc >> 4) & 15u) * 16384u);
    const lds_cptr vpl = vp + grp * 8192, vpl_hi = vp + (8192 - grp * 8192);
    const bool first_next = (tc >> 16) & 1u;
    dfl = 0;
    if (grp == 0 || g <= 0) {
      if (g < 0) asm volatile("s_waitcnt vmcnt(4) lgkmcnt(0)\n\ts_barrier" ::: "memory");
      else {
        const int gk = g + 2 > 16 ? 16 : g + 2, tmax = gk < P1 ? gk : 2 * gk - P1 + 1;
        asm volatile("s_waitcnt vmcnt(0)" ::: "memory");
        check_w0<false, true>(wid, flagp(ws, F_KV + h * 8), pvo, tok, (2ull << (tmax >> 2)) - 1ull);
        asm volatile("s_waitcnt lgkmcnt(0)\n\ts_barrier" ::: "memory");
        PLAN(g + 1);
      }
      poll_w0(wid, flagp(ws, F_KV + h * 8), pvo);
            k0 = KF(0); k1 = KF(1); k2 = KF(2); k3 = KF(3);
    }
    if (first_next) {
      const lds_cptr qp = (lds_cptr)lds + (lane * 16 + rg * 8192 + ((tc >> 12) & 15u) * 16384u);
#pragma unroll
      for (int ks = 0; ks < 8; ++ks) qf[ks] = ldsr128(qp + ks * 1024);
      negm = f32x16{};
    }
    PIN(negm);
    SBAR();
    {
      float sa = 0.f, sb = 0.f;
      bf16x8 k4, k5;
      T0 = MFMA(k0, qf[0], negm); k4 = KF(4); k5 = KF(5); ADD2(S1[0], S1[1]); pk2[0] = cvtpk(S1[0], S1[1]); PIN(pk2); SBAR();
      T0 = MFMA(k1, qf[1], T0); ADD2(S1[2], S1[3]); pk2[1] = cvtpk(S1[2], S1[3]); PIN(pk2); SBAR();
      T0 = MFMA(k2, qf[2], T0); k0 = KF(6); k1 = KF(7); ADD2(S1[4], S1[5]); pk2[2] = cvtpk(S1[4], S1[5]); PIN(pk2); SBAR();
      T0 = MFMA(k3, qf[3], T0); ADD2(S1[6], S1[7]); pk2[3] = cvtpk(S1[6], S1[7]); PIN(pk2); SBAR();
      HOOK0();
      T0 = MFMA(k4, qf[4], T0); k2 = KF(8); k3 = KF(9); ADD2(S1[8], S1[9]); pk3[0] = cvtpk(S1[8], S1[9]); PIN(pk3); SBAR();
      T0 = MFMA(k5, qf[5], T0); ADD2(S1[10], S1[11]); pk3[1] = cvtpk(S1[10], S1[11]); PIN(pk3); SBAR();
      T0 = MFMA(k0, qf[6], T0); k4 = KF(10); k5 = KF(11); ADD2(S1[12], S1[13]); pk3[2] = cvtpk(S1[12], S1[13]); PIN(pk3); SBAR();
      T0 = MFMA(k1, qf[7], T0); ADD2(S1[14], S1[15]); pk3[3] = cvtpk(S1[14], S1[15]); PIN(pk3); SBAR();
      HOOK1();
      l += sa + sb; PIN(pk2); PIN(pk3);
      T1 = MFMA(k2, qf[0], negm); k0 = KF(12); k1 = KF(13); SBAR();
      T1 = MFMA(k3, qf[1], T1); SBAR();
      T1 = MFMA(k4, qf[2], T1); k2 = KF(14); k3 = KF(15); SBAR();
      T1 = MFMA(k5, qf[3], T1); SBAR();
      HOOK2();
      ma = T0[0]; mb = T0[1];
      T1 = MFMA(k0, qf[4], T1); v0 = VF(0, 0); v1 = VF(1, 0); MX2(T0, 2); SBAR();
      T1 = MFMA(k1, qf[5], T1); MX2(T0, 6); SBAR();
      T1 = MFMA(k2, qf[6], T1); v2 = VF(2, 0); v3 = VF(3, 0); MX2(T0, 10); SBAR();
      T1 = MFMA(k3, qf[7], T1); ma = max3f(ma, T0[14], T0[15]); SBAR();
      HOOK3();
    }
    if (grp == 1 && g >= 0) {
      asm volatile("s_waitcnt vmcnt(0) lgkmcnt(0)\n\ts_barrier" ::: "memory");
      v0 = VF(0, 0); v1 = VF(1, 0); v2 = VF(2, 0); v3 = VF(3, 0);
      if (g < 15) PLAN(g + 2); else dfl = 0;
          } else dfl = 0;
    {
      const lds_cptr kp = (lds_cptr)lds + (lane * 16 + ((tc >> 8) & 15u) * 16384u);
      bf16x8 v4, v5;
      o[0] = MFMA(v0, BFR(pk0), o[0]); v4 = VF(0, 1); v5 = VF(1, 1); SBAR();
      o[1] = MFMA(v1, BFR(pk0), o[1]); SBAR();
      o[2] = MFMA(v2, BFR(pk0), o[2]); v0 = VF(2, 1); v1 = VF(3, 1); MX2(T1, 0); MX2(T1, 4); SBAR();
      o[3] = MFMA(v3, BFR(pk0), o[3]); MX2(T1, 8); MX2(T1, 12); SBAR();
      ma = max2f(ma, mb);
      bool resc = false; float fsc = 1.f;
      if (__builtin_expect(((tc >> 16) & 3u) != 0u || __any(ma > THR), 0)) {
        if ((tc >> 17) & 1u) {
          const float lfB = ((float)(((int)tc >> 24) * 32 + 64) + rl) * 1.0e30f;
#pragma unroll
          for (int i = 0; i < 16; ++i) { const int kk = (i & 3) + 8 * (i >> 2); float t0_, t1_;
            asm("v_add_f32_e32 %2, %5, %4\n\tv_add_f32_e32 %3, %6, %4\n\tv_min_f32_e32 %0, %0, %2\n\tv_min_f32_e32 %1, %1, %3"
                : "+v"(T0[i]), "+v"(T1[i]), "=&v"(t0_), "=&v"(t1_) : "v"(lfB), "i"(__builtin_bit_cast(int, -(float)(kk + 64) * 1.0e30f)), "i"(__builtin_bit_cast(int, -(float)(kk + 96) * 1.0e30f))); }
        }
        ma = T0[0]; mb = T0[1];
        MX2(T0, 2); MX2(T0, 6); MX2(T0, 10); ma = max3f(ma, T0[14], T0[15]); MX2(T1, 0); MX2(T1, 4); MX2(T1, 8); MX2(T1, 12);
        const float a = swap32_max(max2f(ma, mb));
        float dl;
        if (first_next) { dl = fmaxf(a, -1.0e29f); lB = l; l = 0.f; m = dl; }
        else { dl = fmaxf(a, 0.f); m += dl; fsc = EX(-dl); l *= fsc; resc = true; }
#pragma unroll
        for (int i = 0; i < 16; ++i) { T0[i] -= dl; T1[i] -= dl; negm[i] = -m; }
      }
      SBAR();
      float sa = 0.f, sb = 0.f;
#define EX4(X, i) do { X[i] = EX(X[i]); X[i + 1] = EX(X[i + 1]); X[i + 2] = EX(X[i + 2]); X[i + 3] = EX(X[i + 3]); PIN(X); } while (0)
#define EX2(X, i) do { X[i] = EX(X[i]); X[i + 1] = EX(X[i + 1]); PIN(X); } while (0)
      HOOK0();
      o[0] = MFMA(v4, BFR(pk1), o[0]); v2 = VF(0, 2); v3 = VF(1, 2); EX4(T0, 0); SBAR();
      o[1] = MFMA(v5, BFR(pk1), o[1]); EX4(T0, 4); SBAR();
      o[2] = MFMA(v0, BFR(pk1), o[2]); v4 = VF(2, 2); v5 = VF(3, 2); EX4(T0, 8); SBAR();
      o[3] = MFMA(v1, BFR(pk1), o[3]); EX4(T0, 12); SBAR();
      HOOK1();
      o[0] = MFMA(v2, BFR(pk2), o[0]); v0 = VF(0, 3); v1 = VF(1, 3); EX2(T1, 0); ADD2(T0[0], T0[1]); pk0[0] = cvtpk(T0[0], T0[1]); PIN(pk0); SBAR();
      o[1] = MFMA(v3, BFR(pk2), o[1]); EX2(T1, 2); ADD2(T0[2], T0[3]); pk0[1] = cvtpk(T0[2], T0[3]); PIN(pk0); SBAR();
      o[2] = MFMA(v4, BFR(pk2), o[2]); v2 = VF(2, 3); v3 = VF(3, 3); EX2(T1, 4); ADD2(T0[4], T0[5]); pk0[2] = cvtpk(T0[4], T0[5]); PIN(pk0); SBAR();
      o[3] = MFMA(v5, BFR(pk2), o[3]); EX2(T1, 6); ADD2(T0[6], T0[7]); pk0[3] = cvtpk(T0[6], T0[7]); PIN(pk0); SBAR();
      HOOK2();
      o[0] = MFMA(v0, BFR(pk3), o[0]); k0 = KF(0); k1 = KF(1); EX2(T1, 8); ADD2(T0[8], T0[9]); pk1[0] = cvtpk(T0[8], T0[9]); PIN(pk1); SBAR();
      o[1] = MFMA(v1, BFR(pk3), o[1]); EX2(T1, 10); ADD2(T0[10], T0[11]); pk1[1] = cvtpk(T0[10], T0[11]); PIN(pk1); SBAR();
      o[2] = MFMA(v2, BFR(pk3), o[2]); k2 = KF(2); k3 = KF(3); EX2(T1, 12); ADD2(T0[12], T0[13]); pk1[2] = cvtpk(T0[12], T0[13]); PIN(pk1); SBAR();
      o[3] = MFMA(v3, BFR(pk3), o[3]); EX2(T1, 14); ADD2(T0[14], T0[15]); pk1[3] = cvtpk(T0[14], T0[15]); PIN(pk1); SBAR();
      HOOK3();
      l += sa + sb;
      S1 = T1;
      if (__builtin_expect(resc, 0)) {
#pragma unroll
        for (int d = 0; d < 4; ++d)
#pragma unroll
          for (int i = 0; i < 16; ++i) o[d][i] *= fsc;
      }
      if (first_next && g >= 0) {
        const float inv = 1.0f / swap32_add(lB);
#pragma unroll
        for (int d = 0; d < 4; ++d) {
          float y[16];
#pragma unroll
          for (int i = 0; i < 16; ++i) y[i] = o[d][i] * inv;
          store_ctx(sCtx, y, bi * 8 + 2 * rg, h, d ^ 2, r, hi);
          o[d] = f32x16{};
        }
      }
      if (g == 10) {
        const int role = (int)(blockIdx.x & 7) >> 1, id = (int)(blockIdx.x >> 3) * 8 + wid;
        touch(role == 3 ? mk_srd(w2, 1376u * 512u * 2u) : mk_srd(wo, 4096u * 512u * 2u), (unsigned)lane * 128u, id < 176 ? (unsigned)((role == 3 ? 0 : role * 22 * 65536) + id * 8192) : OOBS);
      }
      if (grp == 1 && g == 14) {
        const unsigned src = (16u << 20) + (unsigned)(h * 524288 + (P1 + 2 * (16 - P1) + 1) * 16384 + rg * 4096), dst = lds0 + (unsigned)(L_KS2 + 16384 + rg * 4096);
        dma2<0>(sK, vL, src, dst); dma2<2048>(sK, vL, src, dst);
      }
    }
  }
  if (grp == 0) asm volatile("s_waitcnt vmcnt(0) lgkmcnt(0)\n\ts_barrier" ::: "memory");
    {
    const lds_cptr vp = (lds_cptr)lds + (lane * 16 + (grp ? L_KS2 + 16384 : L_V));
    const lds_cptr vpl = vp + grp * 8192, vpl_hi = vp + (8192 - grp * 8192);
#pragma unroll
    for (int i = 0; i < 16; ++i) l += S1[i];
    pk2 = pack8(S1, 0); pk3 = pack8(S1, 1);
#pragma unroll
    for (int d = 0; d < 4; ++d) { o[d] = MFMA(VF(d, 0), BFR(pk0), o[d]); o[d] = MFMA(VF(d, 1), BFR(pk1), o[d]); o[d] = MFMA(VF(d, 2), BFR(pk2), o[d]); o[d] = MFMA(VF(d, 3), BFR(pk3), o[d]); }
  }
  {
    l = swap32_add(l);
    char* mybuf = lds + grp * 32768 + rg * 8192;
    const char* pbuf = lds + (grp ^ 1) * 32768 + rg * 8192;
    float* ml = (float*)(lds + L_QA);
    asm volatile("s_waitcnt lgkmcnt(0)\n\ts_barrier" ::: "memory");
#pragma unroll
    for (int t = 0; t < 2; ++t)
#pragma unroll
      for (int q = 0; q < 4; ++q) {
        f32x4 v = {o[2 + t][4 * q], o[2 + t][4 * q + 1], o[2 + t][4 * q + 2], o[2 + t][4 * q + 3]};
        *(f32x4*)(mybuf + ((t * 4 + q) * 64 + lane) * 16) = v;
      }
    if (hi == 0) { ml[wid * 64 + r] = m; ml[wid * 64 + 32 + r] = l; }
    asm volatile("s_waitcnt lgkmcnt(0)\n\ts_barrier" ::: "memory");
    const int pw = wid ^ 4;
    const float m2 = ml[pw * 64 + r], l2 = ml[pw * 64 + 32 + r];
    const float mm = fmaxf(m, m2);
    const float f1 = EX(m - mm), f2 = EX(m2 - mm);
    const float inv = 1.0f / (l * f1 + l2 * f2);
    const float g1 = f1 * inv, g2 = f2 * inv;
#pragma unroll
    for (int t = 0; t < 2; ++t) {
      float y[16];
#pragma unroll
      for (int q = 0; q < 4; ++q) {
        const f32x4 v = *(const f32x4*)(pbuf + ((t * 4 + q) * 64 + lane) * 16);
#pragma unroll
        for (int e = 0; e < 4; ++e) y[4 * q + e] = o[t][4 * q + e] * g1 + v[e] * g2;
      }
      store_ctx(sCtx, y, (15 - bi) * 8 + 2 * rg, h, 2 * grp + t, r, hi);
    }
  }
#undef KF
#undef VF
}


typedef unsigned u32x2 __attribute__((ext_vector_type(2)));
DI void phaseD(char* lds, char* ws, const u16* x, const u16* wo, const u16* w2, u16* out, u64 tok, int h, int bi, int wid, int lane) {
  const int b = blockIdx.x, xcd = b & 7, jj = b >> 3, role = xcd >> 1, mt = (xcd & 1) * 8 + (jj >> 2), nt = jj & 3;
  const int c = lane & 15, g = lane >> 4, kgrp = wid >> 2, wr = (wid >> 1) & 1, wc = wid & 1;
  const unsigned lds0 = (unsigned)(uintptr_t)lds;
  const bool ish = role == 3;
  const int s0 = ish ? 0 : role * 22;
  constexpr int NS = 22;
  const __amdgpu_buffer_rsrc_t sA = ish ? mk_srd(ws + WS_H, 128u * 44u * 1024u) : mk_srd(ws + WS_CTX, 16u << 20);
  const __amdgpu_buffer_rsrc_t sW = ish ? mk_srd(w2, 1376u * 512u * 2u) : mk_srd(wo, 4096u * 512u * 2u);
  const unsigned vL = (unsigned)lane * 16u;
  const unsigned aRow = (unsigned)((mt * 8 + wid) * (ish ? 44 : 128) + s0 * 2) * 1024u;
  unsigned vW[2];
#pragma unroll
  for (int m = 0; m < 2; ++m) vW[m] = (unsigned)(((m * 4 + ((lane >> 2) & 3)) * 512 + (lane >> 4) * 32 + ((lane & 3) ^ (2 * m)) * 8) * 2);
  const unsigned wBase = (unsigned)((wid * 8 * 512 + nt * 128) * 2) + (unsigned)s0 * 65536u;
  const unsigned dw = lds0 + (unsigned)(wid * 2048);
  const unsigned aLate = role == 2 ? OOBS : aRow;
  auto pA = [&](int s, int m) { dma16(sA, vL, (s < 20 ? aRow : aLate) + (unsigned)(s * 2048 + m * 1024), dw + (unsigned)((s % 5) * 32768 + m * 1024)); };
  auto pW = [&](int s, int m) { dma16(sW, vW[m], wBase + (unsigned)s * 65536u, dw + (unsigned)((s % 5) * 32768 + 16384 + m * 1024)); };
  asm volatile("s_waitcnt lgkmcnt(0)\n\ts_barrier" ::: "memory");
#pragma unroll
  for (int s = 0; s < 5; ++s) { pW(s, 0); pW(s, 1); }
  wait_bar<10>();
  signal_w0(wid, 1u, flagp(ws, F_C + bi * 32 + h), tok); signal_w0(wid, 1u, flagp(ws, F_C + (15 - bi) * 32 + h), tok);
  { check_w0<false, false>(wid, flagp(ws, F_C + mt * 32), (unsigned)(lane & 31) * 8u, tok, ~0ull); }
  asm volatile("s_barrier" ::: "memory");
#pragma unroll
  for (int s = 0; s < 5; ++s) { pA(s, 0); pA(s, 1); }
  const int ab = (wr * 4 + 2 * kgrp) * 2048 + kgrp * 1024 + lane * 16, abx = (wr * 4 + 2 - 2 * kgrp) * 2048 + kgrp * 1024 + lane * 16;
  int wb[2][2];
  {
    const int kq0 = ish ? g : 4 * (g >> 1) + (g & 1), kst = ish ? 4 : 2;
#pragma unroll
    for (int par = 0; par < 2; ++par)
#pragma unroll
      for (int t = 0; t < 2; ++t) { const int kq = kgrp * 8 + kq0 + t * kst;
        wb[par][t] = 16384 + kq * 1024 + wc * 512 + ((lane >> 2) & 3) * 64 + ((par ^ (kq & 1)) * 32) + (lane & 3) * 8; }
  }
  f32x4 acc[4][4];
#pragma unroll
  for (int i = 0; i < 4; ++i)
#pragma unroll
    for (int j = 0; j < 4; ++j) acc[i][j] = f32x4{};
  bf16x8 af[2][4], wf[2][4];
  wait_bar<8>();
  {
    lds_cptr base = (lds_cptr)lds;
#pragma unroll
    for (int j = 0; j < 4; ++j) wf[0][j] = cat8(ldstr(base + wb[j & 1][0] + (j >> 1) * 256), ldstr(base + wb[j & 1][1] + (j >> 1) * 256));
#pragma unroll
    for (int i = 0; i < 4; ++i) af[0][i] = ldsr128(base + ((i < 2) ? ab : abx) + (i & 1) * 2048);
  }
#pragma unroll
  for (int s = 0; s < NS; ++s) {
    const int hf = s & 1;
    const bool nx = s + 1 < NS, pf = s + 5 < NS;
    if (nx) {
      constexpr int dummy = 0; (void)dummy;
      const int yo = (s < 3) ? (6 - 2 * s) + 4 * s : 12;
      const int tail = NS - 2 - s;
      const int n = (tail < 3) ? 4 * tail : yo;
      if (n == 12) wait_bar<12>(); else if (n == 10) wait_bar<10>(); else if (n == 8) wait_bar<8>(); else if (n == 6) wait_bar<6>(); else if (n == 4) wait_bar<4>(); else wait_bar<0>();
    }
    SBAR();
    lds_cptr base = (lds_cptr)lds + ((s + 1) % 5) * 32768;
    s16x4 wl[4], wh[4];
#define K2M(i, j) acc[i][j] = MFMA16(wf[hf][j], af[hf][i], acc[i][j])
#define PIN4(i) asm volatile("" : "+v"(acc[i][0]), "+v"(acc[i][1]), "+v"(acc[i][2]), "+v"(acc[i][3]))
    K2M(0, 0); if (nx) wl[0] = ldstr(base + wb[0][0]); SBAR();
    K2M(0, 1); if (nx) wh[0] = ldstr(base + wb[0][1]); SBAR();
    K2M(0, 2); if (nx) af[hf ^ 1][0] = ldsr128(base + ab); SBAR();
    K2M(0, 3); if (nx) wl[1] = ldstr(base + wb[1][0]); SBAR();
    PIN4(0); if (pf) pA(s + 5, 0); SBAR();
    K2M(1, 0); if (nx) wh[1] = ldstr(base + wb[1][1]); SBAR();
    K2M(1, 1); if (nx) wl[2] = ldstr(base + wb[0][0] + 256); SBAR();
    K2M(1, 2); if (nx) wh[2] = ldstr(base + wb[0][1] + 256); SBAR();
    K2M(1, 3); if (nx) wl[3] = ldstr(base + wb[1][0] + 256); SBAR();
    PIN4(1); if (pf) pA(s + 5, 1); SBAR();
    K2M(2, 0); if (nx) wh[3] = ldstr(base + wb[1][1] + 256); SBAR();
    K2M(2, 1); if (nx) af[hf ^ 1][1] = ldsr128(base + ab + 2048); SBAR();
    K2M(2, 2); if (nx) af[hf ^ 1][2] = ldsr128(base + abx); SBAR();
    K2M(2, 3); if (nx) af[hf ^ 1][3] = ldsr128(base + abx + 2048); SBAR();
    PIN4(2); if (pf) pW(s + 5, 0); SBAR();
    K2M(3, 0); K2M(3, 1); SBAR();
    if (pf) pW(s + 5, 1); SBAR();
    K2M(3, 2); K2M(3, 3); SBAR(); PIN4(3);
#undef K2M
    if (nx) {
#pragma unroll
      for (int j = 0; j < 4; ++j) wf[hf ^ 1][j] = cat8(wl[j], wh[j]);
    }
  }
  asm volatile("s_waitcnt vmcnt(0) lgkmcnt(0)\n\ts_barrier" ::: "memory");
  {
    char* mybuf = lds + wid * 8192;
    const char* pbuf = lds + (wid ^ 4) * 8192;
#pragma unroll
    for (int i = 0; i < 2; ++i)
#pragma unroll
      for (int j = 0; j < 4; ++j) *(f32x4*)(mybuf + ((i * 4 + j) * 64 + lane) * 16) = acc[2 + i][j];
    asm volatile("s_waitcnt lgkmcnt(0)\n\ts_barrier" ::: "memory");
#pragma unroll
    for (int i = 0; i < 2; ++i)
#pragma unroll
      for (int j = 0; j < 4; ++j) acc[i][j] += *(const f32x4*)(pbuf + ((i * 4 + j) * 64 + lane) * 16);
  }
  const int qd = 2 * wr + kgrp, tile = mt * 4 + nt;
  const __amdgpu_buffer_rsrc_t sP = mk_srd(ws + WS_PART, 16u << 20);
  f32x4 own[2];
#pragma unroll
  for (int j = 0; j < 4; ++j) {
    if (j != role) {
      const u32x4 w = {cvtpk(acc[0][j][0], acc[0][j][1]), cvtpk(acc[0][j][2], acc[0][j][3]), cvtpk(acc[1][j][0], acc[1][j][1]), cvtpk(acc[1][j][2], acc[1][j][3])};
      st16_wt(sP, (unsigned)((((tile * 4 + j) * 4 + role) * 8 + wid) * 1024) + vL, w);
    } else { own[0] = acc[0][j]; own[1] = acc[1][j]; }
  }
  u32x2 xv[2];
  const size_t obase = (size_t)(mt * 128 + qd * 32 + c) * 512 + nt * 128 + wc * 64 + role * 16 + 4 * g;
#pragma unroll
  for (int i = 0; i < 2; ++i) xv[i] = *(const u32x2*)(x + obase + i * 16 * 512);
  asm volatile("s_waitcnt vmcnt(0)\n\ts_barrier" ::: "memory");
  signal_w0(wid, 1u, flagp(ws, F_P + tile * 4 + role), tok);
  { check_w0<false, false>(wid, flagp(ws, F_P + tile * 4), (unsigned)(lane & 3) * 8u, tok, ~0ull); }
  asm volatile("s_barrier" ::: "memory");
  f32x4 p[3][2];
#pragma unroll
  for (int rr = 0; rr < 3; ++rr) {
    const int src = (role + 1 + rr) & 3;
    const u32x4 w = __builtin_amdgcn_raw_buffer_load_b128(sP, (unsigned)((((tile * 4 + role) * 4 + src) * 8 + wid) * 1024) + vL, 0, 16);
#pragma unroll
    for (int i = 0; i < 2; ++i) p[rr][i] = (f32x4){__uint_as_float(w[2 * i] << 16), __uint_as_float(w[2 * i] & 0xffff0000u), __uint_as_float(w[2 * i + 1] << 16), __uint_as_float(w[2 * i + 1] & 0xffff0000u)};
  }
#pragma unroll
  for (int i = 0; i < 2; ++i) {
    f32x4 ya = ish ? own[i] : (role == 2 ? p[0][i] : (role == 1 ? p[1][i] : p[2][i]));
    f32x4 sa = (own[i] + p[0][i]) + (p[1][i] + p[2][i]) - ya;
    {
      const f32x4 z = {};
      sa = (ish ? z : own[i]) + (role == 2 ? z : p[0][i]) + (role == 1 ? z : p[1][i]) + (role == 0 ? z : p[2][i]);
    }
    float o4[4];
#pragma unroll
    for (int e = 0; e < 4; ++e) {
      const float y = rbf(ya[e]);
      const float gt = rbf(y * __builtin_amdgcn_rcpf(1.f + __builtin_amdgcn_exp2f(-1.4426950408889634f * y)));
      const float sg = rbf(rbf(sa[e]) * gt);
      const float xe = (e & 1) ? __uint_as_float(xv[i][e >> 1] & 0xffff0000u) : __uint_as_float(xv[i][e >> 1] << 16);
      o4[e] = xe + sg;
    }
    u32x2 w = {cvtpk(o4[0], o4[1]), cvtpk(o4[2], o4[3])};
    *(u32x2*)(out + obase + i * 16 * 512) = w;
  }
}


__global__ __launch_bounds__(512, 2) __attribute__((amdgpu_num_vgpr(252))) void fused_block(const u16* __restrict__ x, const u16* __restrict__ ckv, const u16* __restrict__ wq, const u16* __restrict__ wuk, const u16* __restrict__ wuv,
                                                      const u16* __restrict__ wo, const u16* __restrict__ w1, const u16* __restrict__ w2, u16* __restrict__ out, char* __restrict__ ws) {
  extern __shared__ __attribute__((aligned(16))) char lds[];
  const int tid = threadIdx.x, lane = tid & 63, wid = __builtin_amdgcn_readfirstlane(tid >> 6);
  const int b = blockIdx.x, xcd = b & 7, jj = b >> 3, h = 4 * xcd + (jj >> 3), mt = jj & 7, bi = mt;
  const __amdgpu_buffer_rsrc_t sC = mk_srd(ckv, 2048u * 512u * 2u), sUK = mk_srd(wuk, 512u * 4096u * 2u), sUV = mk_srd(wuv, 512u * 4096u * 2u);
  const int R = b * 8 + wid;
  u32x4 xr;
  const unsigned vC = (unsigned)((lane >> 2) * 1024 + (((lane & 3) ^ (3 * ((lane >> 5) & 1))) * 16));
  const unsigned vW = (unsigned)((((lane >> 2) & 3) * 4096 + (lane >> 4) * 32 + ((lane & 3) ^ (2 * ((wid >> 1) & 1))) * 8) * 2);
  const unsigned sCbase = (unsigned)((mt * 256 + wid * 32) * 1024);
  const unsigned sWbase = (unsigned)((wid * 4 * 4096 + h * 128) * 2);
  auto st0 = [&](int s) {
    const unsigned bb = (unsigned)(uintptr_t)lds + (unsigned)(PA_BASE + s * 32768);
#pragma unroll
    for (int m = 0; m < 2; ++m) dma16(sC, vC, sCbase + (unsigned)(m * 16 * 1024 + s * 64), bb + (unsigned)((wid * 2 + m) * 1024));
#pragma unroll
    for (int m = 0; m < 2; ++m) dma16(m ? sUV : sUK, vW, sWbase + (unsigned)((s * 32 * 4096) * 2), bb + 16384u + (unsigned)((wid * 2 + m) * 1024));
  };
  { const u16* xp = x + (size_t)R * 512 + lane * 8; asm volatile("global_load_dwordx4 %0, %1, off" : "=v"(xr) : "v"(xp) : "memory"); }
  st0(0); st0(1);
  const u64 tok = ((hw_dispatch_id() + 1ull) * 0x9E3779B97F4A7C15ull) ^ ((u64)(uintptr_t)__builtin_amdgcn_dispatch_ptr() << 7) ^ 0x5bd1e9955bd1e995ull;
  PBsrc pb;
  pb.sX = mk_srd(ws + WS_XN, 2048u * 512u * 2u); pb.sQ = mk_srd(wq, 512u * 4096u * 2u); pb.s1 = mk_srd(w1, 512u * 1376u * 2u);
  pb.vL = pb.vQ = pb.v1 = 0u;
  pb.sXbase = (unsigned)(((wid < 4) ? (15 - bi) * 8 : bi * 8 - 8) + 2 * wid) * 16384u;
  pb.sQbase = sWbase; pb.s1base = h < 22 ? (unsigned)((wid * 4 * 1376 + h * 64) * 2) : OOBS;
  {
    asm volatile("s_waitcnt vmcnt(8)" : "+v"(xr) :: "memory");
    st0(2); st0(3);
    float v[8];
#pragma unroll
    for (int e = 0; e < 4; ++e) { v[2 * e] = __uint_as_float(xr[e] << 16); v[2 * e + 1] = __uint_as_float(xr[e] & 0xffff0000u); }
    float s = 0;
#pragma unroll
    for (int e = 0; e < 8; ++e) s += v[e];
#pragma unroll
    for (int o = 32; o; o >>= 1) s += __shfl_xor(s, o);
    const float mean = s * (1.f / 512.f);
    float q = 0;
#pragma unroll
    for (int e = 0; e < 8; ++e) { const float d = v[e] - mean; q += d * d; }
#pragma unroll
    for (int o = 32; o; o >>= 1) q += __shfl_xor(q, o);
    const float mb = rbf(mean), vb = rbf(q * (1.f / 512.f));
    const float den = rbf(__builtin_sqrtf(rbf(vb + 1e-5f)));
    float o8[8];
#pragma unroll
    for (int e = 0; e < 8; ++e) o8[e] = rbf(v[e] - mb) / den;
    u32x4 w = {cvtpk(o8[0], o8[1]), cvtpk(o8[2], o8[3]), cvtpk(o8[4], o8[5]), cvtpk(o8[6], o8[7])};
    st16_wt(pb.sX, (unsigned)((((R >> 4) * 16 + (lane >> 2)) * 64 + ((lane & 3) * 16 + (R & 15))) * 16), w);
  }
  {
    const int id = jj * 8 + wid;
    touch(sC, (unsigned)lane * 128u, (unsigned)id * 8192u);
    unsigned long long pcv; unsigned len;
    asm volatile("s_getpc_b64 %0\n.Lfb_pc:\n\ts_mov_b32 %1, .Lfb_end-.Lfb_pc" : "=s"(pcv), "=s"(len));
    const bool w = id < 128;
    touch(w ? ((id & 64) ? sUV : sUK) : mk_srd((const char*)(pcv & ~127ull), len), w ? (unsigned)(lane >> 3) * 8192u + (unsigned)(lane & 7) * 128u : (unsigned)lane * 128u,
          w ? (unsigned)((id & 63) * 8 * 8192 + xcd * 1024) : (unsigned)(id - 128) * 8192u);
  }
  const __amdgpu_buffer_rsrc_t sK = mk_srd(ws + WS_KF, 32u << 20);
  u32x4 kv[16];
  if (wid < 4) phaseA<true>(lds, ws, sC, sUK, sUV, sCbase, sWbase, pb, tok, h, mt, wid, lane_id(), kv);
  else         phaseA<false>(lds, ws, sC, sUK, sUV, sCbase, sWbase, pb, tok, h, mt, wid, lane_id(), kv);
  if (h < 22) phaseB<true>(lds, ws, pb, sK, tok, h, bi, wid, lane_id(), kv);
  else        phaseB<false>(lds, ws, pb, sK, tok, h, bi, wid, lane_id(), kv);
  phaseC(lds, ws, sK, wo, w2, tok, h, bi, wid, lane_id());
  phaseD(lds, ws, x, wo, w2, out, tok, h, bi, wid, lane_id());
  asm volatile(".Lfb_end:");
}

extern "C" void kernel_launch(void* const* d_in, const int* in_sizes, int n_in, void* d_out, int out_size, void* d_ws, size_t ws_size, hipStream_t stream) {
  (void)in_sizes; (void)n_in; (void)out_size; (void)ws_size;
  (void)hipFuncSetAttribute((const void*)fused_block, hipFuncAttributeMaxDynamicSharedMemorySize, 163840);
  fused_block<<<256, 512, 163840, stream>>>((const u16*)d_in[0], (const u16*)d_in[1], (const u16*)d_in[2], (const u16*)d_in[3], (const u16*)d_in[4], (const u16*)d_in[5],
                                            (const u16*)d_in[7], (const u16*)d_in[8], (u16*)d_out, (char*)d_ws);
}
```
